```python
import torch

def _softmax(x, axis=-1):
    return torch.softmax(x, dim=axis)
def _gelu(x):
    return torch.nn.functional.gelu(x, approximate="tanh")
def _silu(x):
    return torch.nn.functional.silu(x)
def _sigmoid(x):
    return torch.sigmoid(x)
def _layernorm(x, axis=-1, eps=1e-5):
    m = x.mean(dim=axis, keepdim=True)
    v = x.var(dim=axis, keepdim=True, unbiased=False)
    return (x - m) / torch.sqrt(v + eps)
def _scatter_add(tgt, idx, src, axis=0):
    return tgt.clone().index_add_(axis, idx, src)
def _segment_sum(src, seg, n):
    out = torch.zeros((n,) + src.shape[1:], dtype=src.dtype)
    return out.index_add_(0, seg, src)
def _pad(x, pw):
    flat = tuple(v for pair in reversed(pw) for v in pair)
    return torch.nn.functional.pad(x, flat)

kernel_name = 'standalone__mha_block_cached__near_ridge'

def setup_inputs():

    _w = (lambda a: (lambda t: t.to(torch.bfloat16) if t.is_floating_point() else t)(torch.as_tensor(a)))
    B, Sq, D = 8, 224, 576
    H, Sk, Dh = 32, 512, 128
    return {
        "x": _w((lambda *s: torch.randn(*s, dtype=torch.bfloat16) * 0.8)(B, Sq, D)),
        "k_cache": _w((lambda *s: torch.randn(*s, dtype=torch.bfloat16) * 0.8)(B, H, Sk, Dh)),
        "v_cache": _w((lambda *s: torch.randn(*s, dtype=torch.bfloat16) * 0.05)(B, H, Sk, Dh)),
        "wq": _w((lambda *s: torch.randn(*s, dtype=torch.bfloat16) * 0.8)(D, H * Dh) * (D ** -0.5)),
        "wo": _w((lambda *s: torch.randn(*s, dtype=torch.bfloat16) * 0.05)(H * Dh, D) * ((H * Dh) ** -0.5)),
    }

def reference(x, k_cache, v_cache, wq, wo):
    B, Sq, D = x.shape
    H, Dh = 32, 128
    q = (lambda x, *d: x.permute(*d))((x @ wq).reshape(B, Sq, H, Dh), 0, 2, 1, 3)
    scores = torch.einsum("bhqd,bhkd->bhqk", q, k_cache) * (Dh ** -0.5)
    p = _softmax(scores, axis=-1)
    o = torch.einsum("bhqk,bhkd->bhqd", p, v_cache)
    return (lambda x, *d: x.permute(*d))(o, 0, 2, 1, 3).reshape(B, Sq, H * Dh) @ wo
```

```cpp
#include <hip/hip_runtime.h>
#include <hip/hip_bf16.h>
#include <stdint.h>

typedef unsigned short u16;
typedef __bf16 bf16x8 __attribute__((ext_vector_type(8)));
typedef __bf16 bf16x4 __attribute__((ext_vector_type(4)));
typedef __bf16 bf16x2 __attribute__((ext_vector_type(2)));
typedef short s16x4 __attribute__((ext_vector_type(4)));
typedef float f32x16 __attribute__((ext_vector_type(16)));
typedef float f32x4 __attribute__((ext_vector_type(4)));
typedef float f32x2 __attribute__((ext_vector_type(2)));
typedef unsigned u32x4 __attribute__((ext_vector_type(4)));
typedef unsigned u32x2 __attribute__((ext_vector_type(2)));

#define LDS_AS __attribute__((address_space(3)))
#define GLB_AS __attribute__((address_space(1)))
#define DI __device__ __forceinline__

constexpr int NB = 8, SQ = 224, DM = 576, NH = 32, SK = 512, DH = 128;
constexpr int HD = NH * DH;

constexpr int XST = 224 * 128;
constexpr int WST = 64 * 256;
constexpr int L_X = 0;
constexpr int L_W = 2 * XST;
constexpr int L_K = L_W + 2 * WST;
constexpr int L_V = L_K + 2 * 16384;
constexpr int L_END1 = L_V + 2 * 16384;

DI unsigned cvtpk(float lo, float hi) {
  f32x2 v = {lo, hi};
  bf16x2 b = __builtin_convertvector(v, bf16x2);
  return __builtin_bit_cast(unsigned, b);
}
DI void glds16(const void* g, char* lds_wave_base) {
  __builtin_amdgcn_global_load_lds((const GLB_AS void*)g, (LDS_AS void*)lds_wave_base, 16, 0, 0);
}
DI bf16x8 lds_read128(const char* p) { return *(const bf16x8*)p; }
DI s16x4 lds_tr(const char* p) { return __builtin_amdgcn_ds_read_tr16_b64_v4i16((LDS_AS s16x4*)p); }
DI bf16x8 cat(s16x4 a, s16x4 b) {
  typedef short s16x8 __attribute__((ext_vector_type(8)));
  s16x8 r = __builtin_shufflevector(a, b, 0, 1, 2, 3, 4, 5, 6, 7);
  return __builtin_bit_cast(bf16x8, r);
}
DI bf16x8 pack8(const f32x16& a, int base, float s) {
  u32x4 w;
  w[0] = cvtpk(a[base + 0] * s, a[base + 1] * s);
  w[1] = cvtpk(a[base + 2] * s, a[base + 3] * s);
  w[2] = cvtpk(a[base + 4] * s, a[base + 5] * s);
  w[3] = cvtpk(a[base + 6] * s, a[base + 7] * s);
  return __builtin_bit_cast(bf16x8, w);
}
#define MFMA32(a, b, c) __builtin_amdgcn_mfma_f32_32x32x16_bf16((a), (b), (c), 0, 0, 0)

__global__ __launch_bounds__(512, 2) void k1_qattn(const u16* __restrict__ x, const u16* __restrict__ kc,
                                                   const u16* __restrict__ vc, const u16* __restrict__ wq,
                                                   const u16* __restrict__ wo, u16* __restrict__ ows,
                                                   u16* __restrict__ wot) {
  extern __shared__ __attribute__((aligned(16))) char lds[];
  const int tid = threadIdx.x, lane = tid & 63;
  const int wid = __builtin_amdgcn_readfirstlane(tid >> 6);
  const int r32 = lane & 31, hh = lane >> 5;
  const int bid = blockIdx.x, b = bid & 7, h = bid >> 3;

  const u16* xb = x + (size_t)b * SQ * DM;
  const u16* wqh = wq + h * DH;
  const u16* kb = kc + (size_t)(b * NH + h) * SK * DH;
  const u16* vb = vc + (size_t)(b * NH + h) * SK * DH;

  auto stage_x = [&](int s, int buf) {
#pragma unroll
    for (int i = 0; i < 4; ++i) {
      const int p = wid + 8 * i;
      if (p < 28) {
        const int row = 8 * p + (lane >> 3);
        const int ch = (lane & 7) ^ ((row >> 1) & 7);
        glds16(xb + row * DM + s * 64 + ch * 8, lds + L_X + buf * XST + p * 1024);
      }
    }
  };
  auto stage_w = [&](int s, int buf) {
#pragma unroll
    for (int i = 0; i < 2; ++i) {
      const int p = wid + 8 * i;
      const int row = 4 * p + (lane >> 4);
      const int ch = (lane & 15) ^ ((row & 3) << 2);
      glds16(wqh + (size_t)(s * 64 + row) * HD + ch * 8, lds + L_W + buf * WST + p * 1024);
    }
  };
  auto stage_k = [&](int t, int buf) {
#pragma unroll
    for (int i = 0; i < 2; ++i) {
      const int p = wid + 8 * i;
      const int row = 4 * p + (lane >> 4);
      const int ch = (lane & 15) ^ (row & 15);
      glds16(kb + (t * 64 + row) * DH + ch * 8, lds + L_K + buf * 16384 + p * 1024);
    }
  };
  auto stage_v = [&](int t, int buf) {
#pragma unroll
    for (int i = 0; i < 2; ++i) {
      const int p = wid + 8 * i;
      const int row = 4 * p + (lane >> 4);
      const int ch = (lane & 15) ^ ((row & 3) << 2);
      glds16(vb + (t * 64 + row) * DH + ch * 8, lds + L_V + buf * 16384 + p * 1024);
    }
  };

  stage_x(0, 0);
  stage_w(0, 0);
  stage_k(0, 0);
  stage_v(0, 0);

  if (wid == 7) {
    const int ks = bid;
#pragma unroll 2
    for (int nt = 0; nt < 18; ++nt) {
      u16 e[8];
#pragma unroll
      for (int j = 0; j < 8; ++j) e[j] = wo[(size_t)(16 * ks + 8 * (j >> 2) + 4 * hh + (j & 3)) * DM + 32 * nt + r32];
      u32x4 w;
      w[0] = e[0] | ((unsigned)e[1] << 16);
      w[1] = e[2] | ((unsigned)e[3] << 16);
      w[2] = e[4] | ((unsigned)e[5] << 16);
      w[3] = e[6] | ((unsigned)e[7] << 16);
      *(u32x4*)(wot + ((size_t)(nt * 256 + ks) * 64 + lane) * 8) = w;
    }
  }

  const int tq = (lane >> 2) & 3, tp = lane & 3, tg = (lane >> 4) & 1;
  const int tps = ((tp & 1) << 1) | (tp >> 1);

  f32x16 qa[4] = {};
  const int qrow = 32 * wid + r32;
  __syncthreads();
  for (int s = 0; s < 9; ++s) {
    const int buf = s & 1;
    if (s + 1 < 9) { stage_x(s + 1, buf ^ 1); stage_w(s + 1, buf ^ 1); }
    if (s == 0) { stage_k(1, 1); stage_v(1, 1); }
    if (wid < 7) {
      const char* xs = lds + L_X + buf * XST + qrow * 128;
      const char* ws = lds + L_W + buf * WST;
#pragma unroll
      for (int ks = 0; ks < 4; ++ks) {
        bf16x8 bf = lds_read128(xs + (((2 * ks + hh) ^ ((qrow >> 1) & 7)) << 4));
#pragma unroll
        for (int t = 0; t < 4; ++t) {
          const char* p0 = ws + (16 * ks + 8 * hh + tq) * 256 + ((t ^ tq) << 6) + 32 * tg + 8 * tps;
          s16x4 a0 = lds_tr(p0), a1 = lds_tr(p0 + 4 * 256);
          qa[t] = MFMA32(cat(a0, a1), bf, qa[t]);
        }
      }
    }
    __syncthreads();
  }
  constexpr float QS = 0.088388347648318440f * 1.4426950408889634f;
  bf16x8 qf[8];
#pragma unroll
  for (int t = 0; t < 4; ++t) { qf[2 * t] = pack8(qa[t], 0, QS); qf[2 * t + 1] = pack8(qa[t], 8, QS); }

  f32x16 oa[4] = {};
  float m = -1e30f, l = 0.f;
  for (int t = 0; t < 8; ++t) {
    const int buf = t & 1;
    if (wid < 7) {
      const char* ks_ = lds + L_K + buf * 16384;
      f32x16 s0 = {}, s1 = {};
#pragma unroll
      for (int ks = 0; ks < 8; ++ks) {
        const int co = ((2 * ks + hh) ^ (r32 & 15)) << 4;
        bf16x8 a0 = lds_read128(ks_ + r32 * 256 + co);
        bf16x8 a1 = lds_read128(ks_ + (32 + r32) * 256 + co);
        s0 = MFMA32(a0, qf[ks], s0);
        s1 = MFMA32(a1, qf[ks], s1);
      }
      float pm = s0[0];
#pragma unroll
      for (int r = 1; r < 16; ++r) pm = fmaxf(pm, s0[r]);
#pragma unroll
      for (int r = 0; r < 16; ++r) pm = fmaxf(pm, s1[r]);
      {
        auto rr = __builtin_amdgcn_permlane32_swap(__float_as_uint(pm), __float_as_uint(pm), false, false);
        pm = fmaxf(__uint_as_float(rr[0]), __uint_as_float(rr[1]));
      }
      if (!__all(pm - m <= 8.f)) {
        const float mn = fmaxf(m, pm);
        const float al = __builtin_amdgcn_exp2f(m - mn);
        m = mn;
        l *= al;
#pragma unroll
        for (int d = 0; d < 4; ++d)
#pragma unroll
          for (int r = 0; r < 16; ++r) oa[d][r] *= al;
      }
      float ps = 0.f;
#pragma unroll
      for (int r = 0; r < 16; ++r) { s0[r] = __builtin_amdgcn_exp2f(s0[r] - m); ps += s0[r]; }
#pragma unroll
      for (int r = 0; r < 16; ++r) { s1[r] = __builtin_amdgcn_exp2f(s1[r] - m); ps += s1[r]; }
      l += ps;
      bf16x8 pf[4];
      pf[0] = pack8(s0, 0, 1.f); pf[1] = pack8(s0, 8, 1.f); pf[2] = pack8(s1, 0, 1.f); pf[3] = pack8(s1, 8, 1.f);
      const char* vs = lds + L_V + buf * 16384;
#pragma unroll
      for (int ks = 0; ks < 4; ++ks) {
#pragma unroll
        for (int d = 0; d < 4; ++d) {
          const char* p0 = vs + (16 * ks + 4 * hh + tq) * 256 + ((d ^ tq) << 6) + 32 * tg + 8 * tp;
          s16x4 a0 = lds_tr(p0), a1 = lds_tr(p0 + 8 * 256);
          oa[d] = MFMA32(cat(a0, a1), pf[ks], oa[d]);
        }
      }
    }
    __syncthreads();
    if (t + 2 < 8) { stage_k(t + 2, buf); stage_v(t + 2, buf); }
    __syncthreads();
  }
  if (wid < 7) {
    {
      auto rr = __builtin_amdgcn_permlane32_swap(__float_as_uint(l), __float_as_uint(l), false, false);
      l = __uint_as_float(rr[0]) + __uint_as_float(rr[1]);
    }
    const float il = 1.f / l;
    u16* op = ows + ((size_t)((b * 7 + wid) * 256 + h * 8) * 64 + lane) * 8;
#pragma unroll
    for (int d = 0; d < 4; ++d) {
      *(bf16x8*)(op + (2 * d) * 512) = pack8(oa[d], 0, il);
      *(bf16x8*)(op + (2 * d + 1) * 512) = pack8(oa[d], 8, il);
    }
  }
}

constexpr int RS = 68;
__global__ __launch_bounds__(256, 1) void k2_oproj(const u16* __restrict__ ows, const u16* __restrict__ wot,
                                                   u16* __restrict__ out) {
  extern __shared__ __attribute__((aligned(16))) char lds[];
  const int tid = threadIdx.x, lane = tid & 63;
  const int wid = __builtin_amdgcn_readfirstlane(tid >> 6);
  const int r32 = lane & 31, hh = lane >> 5;
  const int xcd = blockIdx.x & 7, idx = blockIdx.x >> 3, pr = xcd >> 1, odd = xcd & 1;
  int band, cg;
  if (idx < 27) { band = 7 * pr + 4 * odd + idx / 9; cg = idx % 9; }
  else { band = 7 * pr + 3; cg = (idx - 27) + 5 * odd; if (cg > 8) return; }

  const bf16x8* a0p = (const bf16x8*)ows + ((size_t)(2 * band) * 256 + wid * 64) * 64 + lane;
  const bf16x8* a1p = a0p + 256 * 64;
  const bf16x8* b0p = (const bf16x8*)wot + ((size_t)(2 * cg) * 256 + wid * 64) * 64 + lane;
  const bf16x8* b1p = b0p + 256 * 64;

  f32x16 acc[2][2] = {};
  constexpr int PD = 8;
  bf16x8 ra0[PD], ra1[PD], rb0[PD], rb1[PD];
#pragma unroll
  for (int i = 0; i < PD; ++i) { ra0[i] = a0p[i * 64]; ra1[i] = a1p[i * 64]; rb0[i] = b0p[i * 64]; rb1[i] = b1p[i * 64]; }
  for (int k0 = 0; k0 < 64; k0 += PD) {
#pragma unroll
    for (int i = 0; i < PD; ++i) {
      bf16x8 xa0 = ra0[i], xa1 = ra1[i], xb0 = rb0[i], xb1 = rb1[i];
      if (k0 + PD < 64) {
        const int kn = (k0 + PD + i) * 64;
        ra0[i] = a0p[kn]; ra1[i] = a1p[kn]; rb0[i] = b0p[kn]; rb1[i] = b1p[kn];
      }
      acc[0][0] = MFMA32(xb0, xa0, acc[0][0]);
      acc[0][1] = MFMA32(xb1, xa0, acc[0][1]);
      acc[1][0] = MFMA32(xb0, xa1, acc[1][0]);
      acc[1][1] = MFMA32(xb1, xa1, acc[1][1]);
    }
  }
  float* red = (float*)lds + wid * 64 * RS;
#pragma unroll
  for (int i = 0; i < 2; ++i)
#pragma unroll
    for (int j = 0; j < 2; ++j)
#pragma unroll
      for (int g = 0; g < 4; ++g) {
        f32x4 v = {acc[i][j][4 * g], acc[i][j][4 * g + 1], acc[i][j][4 * g + 2], acc[i][j][4 * g + 3]};
        *(f32x4*)(red + (32 * i + r32) * RS + 32 * j + 8 * g + 4 * hh) = v;
      }
  __syncthreads();
  const int row = tid >> 2, c0 = 16 * (tid & 3);
  float s[16];
#pragma unroll
  for (int e = 0; e < 16; ++e) s[e] = 0.f;
#pragma unroll
  for (int w = 0; w < 4; ++w) {
    const float* p = (const float*)lds + (w * 64 + row) * RS + c0;
#pragma unroll
    for (int g = 0; g < 4; ++g) {
      f32x4 v = *(const f32x4*)(p + 4 * g);
      s[4 * g] += v[0]; s[4 * g + 1] += v[1]; s[4 * g + 2] += v[2]; s[4 * g + 3] += v[3];
    }
  }
  u32x4 w0, w1;
#pragma unroll
  for (int e = 0; e < 4; ++e) { w0[e] = cvtpk(s[2 * e], s[2 * e + 1]); w1[e] = cvtpk(s[8 + 2 * e], s[8 + 2 * e + 1]); }
  u16* op = out + (size_t)(64 * band + row) * DM + 64 * cg + c0;
  *(u32x4*)op = w0;
  *(u32x4*)(op + 8) = w1;
}

extern "C" void kernel_launch(void* const* d_in, const int* in_sizes, int n_in, void* d_out, int out_size, void* d_ws,
                              size_t ws_size, hipStream_t stream) {
  const u16* x = (const u16*)d_in[0];
  const u16* kc = (const u16*)d_in[1];
  const u16* vc = (const u16*)d_in[2];
  const u16* wq = (const u16*)d_in[3];
  const u16* wo = (const u16*)d_in[4];
  u16* ows = (u16*)d_ws;
  u16* wot = (u16*)((char*)d_ws + (size_t)56 * 256 * 1024);
  constexpr int LDS2 = 4 * 64 * RS * 4;
  (void)hipFuncSetAttribute((const void*)k1_qattn, hipFuncAttributeMaxDynamicSharedMemorySize, L_END1);
  (void)hipFuncSetAttribute((const void*)k2_oproj, hipFuncAttributeMaxDynamicSharedMemorySize, LDS2);
  hipLaunchKernelGGL(k1_qattn, dim3(256), dim3(512), L_END1, stream, x, kc, vc, wq, wo, ows, wot);
  hipLaunchKernelGGL(k2_oproj, dim3(256), dim3(256), LDS2, stream, ows, wot, (u16*)d_out);
}
```

```cpp
#include <hip/hip_runtime.h>
#include <hip/hip_bf16.h>
#include <stdint.h>

typedef unsigned short u16;
typedef __bf16 bf16x8 __attribute__((ext_vector_type(8)));
typedef __bf16 bf16x4 __attribute__((ext_vector_type(4)));
typedef __bf16 bf16x2 __attribute__((ext_vector_type(2)));
typedef short s16x4 __attribute__((ext_vector_type(4)));
typedef float f32x16 __attribute__((ext_vector_type(16)));
typedef float f32x4 __attribute__((ext_vector_type(4)));
typedef float f32x2 __attribute__((ext_vector_type(2)));
typedef unsigned u32x4 __attribute__((ext_vector_type(4)));
typedef unsigned u32x2 __attribute__((ext_vector_type(2)));

#define LDS_AS __attribute__((address_space(3)))
#define GLB_AS __attribute__((address_space(1)))
#define DI __device__ __forceinline__

constexpr int NB = 8, SQ = 224, DM = 576, NH = 32, SK = 512, DH = 128;
constexpr int HD = NH * DH;

constexpr int XST = 224 * 128;
constexpr int WST = 64 * 256;
constexpr int L_X = 0;
constexpr int L_W = 2 * XST;
constexpr int L_K = L_W + 2 * WST;
constexpr int L_V = L_K + 2 * 16384;
constexpr int L_END1 = L_V + 2 * 16384;

DI unsigned cvtpk(float lo, float hi) {
  f32x2 v = {lo, hi};
  bf16x2 b = __builtin_convertvector(v, bf16x2);
  return __builtin_bit_cast(unsigned, b);
}
DI void glds16(const void* g, char* lds_wave_base) {
  __builtin_amdgcn_global_load_lds((const GLB_AS void*)g, (LDS_AS void*)lds_wave_base, 16, 0, 0);
}
DI void glds16nt(const void* g, char* lds_wave_base) {
  __builtin_amdgcn_global_load_lds((const GLB_AS void*)g, (LDS_AS void*)lds_wave_base, 16, 0, 2);
}
DI bf16x8 lds_read128(const char* p) { return *(const bf16x8*)p; }
DI s16x4 lds_tr(const char* p) { return __builtin_amdgcn_ds_read_tr16_b64_v4i16((LDS_AS s16x4*)p); }
DI bf16x8 cat(s16x4 a, s16x4 b) {
  typedef short s16x8 __attribute__((ext_vector_type(8)));
  s16x8 r = __builtin_shufflevector(a, b, 0, 1, 2, 3, 4, 5, 6, 7);
  return __builtin_bit_cast(bf16x8, r);
}
DI bf16x8 pack8(const f32x16& a, int base, float s) {
  u32x4 w;
  w[0] = cvtpk(a[base + 0] * s, a[base + 1] * s);
  w[1] = cvtpk(a[base + 2] * s, a[base + 3] * s);
  w[2] = cvtpk(a[base + 4] * s, a[base + 5] * s);
  w[3] = cvtpk(a[base + 6] * s, a[base + 7] * s);
  return __builtin_bit_cast(bf16x8, w);
}
#define MFMA32(a, b, c) __builtin_amdgcn_mfma_f32_32x32x16_bf16((a), (b), (c), 0, 0, 0)

__global__ __launch_bounds__(512, 2) void k1_qattn(const u16* __restrict__ x, const u16* __restrict__ kc,
                                                   const u16* __restrict__ vc, const u16* __restrict__ wq,
                                                   const u16* __restrict__ wo, u16* __restrict__ ows,
                                                   u16* __restrict__ wot
) {
  extern __shared__ __attribute__((aligned(16))) char lds[];
  const int tid = threadIdx.x, lane = tid & 63;
  const int wid = __builtin_amdgcn_readfirstlane(tid >> 6);
  const int r32 = lane & 31, hh = lane >> 5;
  const int bid = blockIdx.x, b = bid & 7, h = bid >> 3;

  const u16* xb = x + (size_t)b * SQ * DM;
  const u16* wqh = wq + h * DH;
  const u16* kb = kc + (size_t)(b * NH + h) * SK * DH;
  const u16* vb = vc + (size_t)(b * NH + h) * SK * DH;

  auto stage_x = [&](int s, int buf) {
#pragma unroll
    for (int i = 0; i < 4; ++i) {
      const int p = wid + 8 * i;
      if (p < 28) {
        const int row = 8 * p + (lane >> 3);
        const int ch = (lane & 7) ^ ((row >> 1) & 7);
        glds16(xb + row * DM + s * 64 + ch * 8, lds + L_X + buf * XST + p * 1024);
      }
    }
  };
  auto stage_w = [&](int s, int buf) {
#pragma unroll
    for (int i = 0; i < 2; ++i) {
      const int p = wid + 8 * i;
      const int row = 4 * p + (lane >> 4);
      const int ch = (lane & 15) ^ ((row & 3) << 2);
      glds16(wqh + (size_t)(s * 64 + row) * HD + ch * 8, lds + L_W + buf * WST + p * 1024);
    }
  };
  auto stage_k = [&](int t, int buf) {
#pragma unroll
    for (int i = 0; i < 2; ++i) {
      const int p = wid + 8 * i;
      const int row = 4 * p + (lane >> 4);
      const int ch = (lane & 15) ^ (row & 15);
      glds16nt(kb + (t * 64 + row) * DH + ch * 8, lds + L_K + buf * 16384 + p * 1024);
    }
  };
  auto stage_v = [&](int t, int buf) {
#pragma unroll
    for (int i = 0; i < 2; ++i) {
      const int p = wid + 8 * i;
      const int row = 4 * p + (lane >> 4);
      const int ch = (lane & 15) ^ ((row & 3) << 2);
      glds16nt(vb + (t * 64 + row) * DH + ch * 8, lds + L_V + buf * 16384 + p * 1024);
    }
  };

  stage_x(0, 0);
  stage_w(0, 0);
  stage_k(0, 0);
  stage_v(0, 0);

  if (wid == 7) {
    const int ks = bid;
#pragma unroll 2
    for (int nt = 0; nt < 18; ++nt) {
      u16 e[8];
#pragma unroll
      for (int j = 0; j < 8; ++j) e[j] = wo[(size_t)(16 * ks + 8 * (j >> 2) + 4 * hh + (j & 3)) * DM + 32 * nt + r32];
      u32x4 w;
      w[0] = e[0] | ((unsigned)e[1] << 16);
      w[1] = e[2] | ((unsigned)e[3] << 16);
      w[2] = e[4] | ((unsigned)e[5] << 16);
      w[3] = e[6] | ((unsigned)e[7] << 16);
      *(u32x4*)(wot + ((size_t)(nt * 256 + ks) * 64 + lane) * 8) = w;
    }
  }

  const int tq = (lane >> 2) & 3, tp = lane & 3, tg = (lane >> 4) & 1;
  const int tps = ((tp & 1) << 1) | (tp >> 1);

  f32x16 qa[4] = {};
  const int qrow = 32 * wid + r32;
  __syncthreads();
  for (int s = 0; s < 9; ++s) {
    const int buf = s & 1;
    if (s + 1 < 9) { stage_x(s + 1, buf ^ 1); stage_w(s + 1, buf ^ 1); }
    if (s == 0) { stage_k(1, 1); stage_v(1, 1); }
    if (wid < 7) {
      const char* xs = lds + L_X + buf * XST + qrow * 128;
      const char* ws = lds + L_W + buf * WST;
#pragma unroll
      for (int ks = 0; ks < 4; ++ks) {
        bf16x8 bf = lds_read128(xs + (((2 * ks + hh) ^ ((qrow >> 1) & 7)) << 4));
#pragma unroll
        for (int t = 0; t < 4; ++t) {
          const char* p0 = ws + (16 * ks + 8 * hh + tq) * 256 + ((t ^ tq) << 6) + 32 * tg + 8 * tps;
          s16x4 a0 = lds_tr(p0), a1 = lds_tr(p0 + 4 * 256);
          qa[t] = MFMA32(cat(a0, a1), bf, qa[t]);
        }
      }
    }
    __syncthreads();
  }
  constexpr float QS = 0.088388347648318440f * 1.4426950408889634f;
  bf16x8 qf[8];
#pragma unroll
  for (int t = 0; t < 4; ++t) { qf[2 * t] = pack8(qa[t], 0, QS); qf[2 * t + 1] = pack8(qa[t], 8, QS); }

  f32x16 oa[4] = {};
  float m = -1e30f, l = 0.f;
  for (int t = 0; t < 8; ++t) {
    const int buf = t & 1;
    if (wid < 7) {
      const char* ks_ = lds + L_K + buf * 16384;
      f32x16 s0 = {}, s1 = {};
#pragma unroll
      for (int ks = 0; ks < 8; ++ks) {
        const int co = ((2 * ks + hh) ^ (r32 & 15)) << 4;
        bf16x8 a0 = lds_read128(ks_ + r32 * 256 + co);
        bf16x8 a1 = lds_read128(ks_ + (32 + r32) * 256 + co);
        s0 = MFMA32(a0, qf[ks], s0);
        s1 = MFMA32(a1, qf[ks], s1);
      }
      float pm = s0[0];
#pragma unroll
      for (int r = 1; r < 16; ++r) pm = fmaxf(pm, s0[r]);
#pragma unroll
      for (int r = 0; r < 16; ++r) pm = fmaxf(pm, s1[r]);
      {
        auto rr = __builtin_amdgcn_permlane32_swap(__float_as_uint(pm), __float_as_uint(pm), false, false);
        pm = fmaxf(__uint_as_float(rr[0]), __uint_as_float(rr[1]));
      }
      if (!__all(pm - m <= 8.f)) {
        const float mn = fmaxf(m, pm);
        const float al = __builtin_amdgcn_exp2f(m - mn);
        m = mn;
        l *= al;
#pragma unroll
        for (int d = 0; d < 4; ++d)
#pragma unroll
          for (int r = 0; r < 16; ++r) oa[d][r] *= al;
      }
      float ps = 0.f;
#pragma unroll
      for (int r = 0; r < 16; ++r) { s0[r] = __builtin_amdgcn_exp2f(s0[r] - m); ps += s0[r]; }
#pragma unroll
      for (int r = 0; r < 16; ++r) { s1[r] = __builtin_amdgcn_exp2f(s1[r] - m); ps += s1[r]; }
      l += ps;
      bf16x8 pf[4];
      pf[0] = pack8(s0, 0, 1.f); pf[1] = pack8(s0, 8, 1.f); pf[2] = pack8(s1, 0, 1.f); pf[3] = pack8(s1, 8, 1.f);
      const char* vs = lds + L_V + buf * 16384;
#pragma unroll
      for (int ks = 0; ks < 4; ++ks) {
#pragma unroll
        for (int d = 0; d < 4; ++d) {
          const char* p0 = vs + (16 * ks + 4 * hh + tq) * 256 + ((d ^ tq) << 6) + 32 * tg + 8 * tp;
          s16x4 a0 = lds_tr(p0), a1 = lds_tr(p0 + 8 * 256);
          oa[d] = MFMA32(cat(a0, a1), pf[ks], oa[d]);
        }
      }
    }
    __syncthreads();
    if (t + 2 < 8) { stage_k(t + 2, buf); stage_v(t + 2, buf); }
    __syncthreads();
  }
  if (wid < 7) {
    {
      auto rr = __builtin_amdgcn_permlane32_swap(__float_as_uint(l), __float_as_uint(l), false, false);
      l = __uint_as_float(rr[0]) + __uint_as_float(rr[1]);
    }
    const float il = 1.f / l;
    u16* op = ows + ((size_t)((b * 7 + wid) * 256 + h * 8) * 64 + lane) * 8;
#pragma unroll
    for (int d = 0; d < 4; ++d) {
      *(bf16x8*)(op + (2 * d) * 512) = pack8(oa[d], 0, il);
      *(bf16x8*)(op + (2 * d + 1) * 512) = pack8(oa[d], 8, il);
    }
  }
}

#define K2W 4
#define K2PD 12
constexpr int RS = 68;
template <int OFF> DI void gld(bf16x8& d, const void* sb, unsigned vo) {
  asm volatile("global_load_dwordx4 %0, %1, %2 offset:%3" : "=v"(d) : "v"(vo), "s"(sb), "n"(OFF) : "memory");
}
__global__ __launch_bounds__(K2W * 64, K2W / 4) void k2_oproj(const u16* __restrict__ ows, const u16* __restrict__ wot,
                                                   u16* __restrict__ out
) {
  extern __shared__ __attribute__((aligned(16))) char lds[];
  const int tid = threadIdx.x, lane = tid & 63;
  const int wid = __builtin_amdgcn_readfirstlane(tid >> 6);
  const int r32 = lane & 31, hh = lane >> 5;
  const int xcd = blockIdx.x & 7, idx = blockIdx.x >> 3, pr = xcd >> 1, odd = xcd & 1;
  int band, cg;
  if (idx < 27) { band = 7 * pr + 4 * odd + idx / 9; cg = idx % 9; }
  else { band = 7 * pr + 3; cg = (idx - 27) + 5 * odd; if (cg > 8) return; }
  constexpr int KS = 256 / K2W;
  const char* a0p = (const char*)ows + ((size_t)(2 * band) * 256 + wid * KS) * 1024;
  const char* a1p = a0p + 256 * 1024;
  const char* b0p = (const char*)wot + ((size_t)(2 * cg) * 256 + wid * KS) * 1024;
  const char* b1p = b0p + 256 * 1024;
  unsigned vo = lane * 16;

  f32x16 acc[2][2] = {};
  constexpr int PD = K2PD;
  bf16x8 ra0[PD], ra1[PD], rb0[PD], rb1[PD];
#pragma unroll
  for (int i = 0; i < PD; ++i) { gld<0>(ra0[i], a0p, vo); gld<0>(rb0[i], b0p, vo); gld<0>(ra1[i], a1p, vo); gld<0>(rb1[i], b1p, vo); vo += 1024; }
#pragma unroll
  for (int k = 0; k < KS; ++k) {
    const int i = k % PD;
    const int left = (KS - 1 - k) < (PD - 1) ? (KS - 1 - k) : (PD - 1);
    switch (left) {
#define WC(n) case n: asm volatile("s_waitcnt vmcnt(%4)" : "+v"(ra0[i]), "+v"(ra1[i]), "+v"(rb0[i]), "+v"(rb1[i]) : "n"(4 * n) : "memory"); break;
      WC(0) WC(1) WC(2) WC(3) WC(4) WC(5) WC(6) WC(7) WC(8) WC(9) WC(10) WC(11) WC(12) WC(13) WC(14)
    }
    acc[0][0] = MFMA32(rb0[i], ra0[i], acc[0][0]);
    acc[0][1] = MFMA32(rb1[i], ra0[i], acc[0][1]);
    acc[1][0] = MFMA32(rb0[i], ra1[i], acc[1][0]);
    acc[1][1] = MFMA32(rb1[i], ra1[i], acc[1][1]);
    __builtin_amdgcn_sched_barrier(0);
    if (k + PD < KS) { gld<0>(ra0[i], a0p, vo); gld<0>(rb0[i], b0p, vo); gld<0>(ra1[i], a1p, vo); gld<0>(rb1[i], b1p, vo); vo += 1024; }
    __builtin_amdgcn_sched_barrier(0);
  }
  float* red = (float*)lds + wid * 64 * RS;
#pragma unroll
  for (int i = 0; i < 2; ++i)
#pragma unroll
    for (int j = 0; j < 2; ++j)
#pragma unroll
      for (int g = 0; g < 4; ++g) {
        f32x4 v = {acc[i][j][4 * g], acc[i][j][4 * g + 1], acc[i][j][4 * g + 2], acc[i][j][4 * g + 3]};
        *(f32x4*)(red + (32 * i + r32) * RS + 32 * j + 8 * g + 4 * hh) = v;
      }
  __syncthreads();
  constexpr int CPT = 64 / K2W;
  const int row = tid / K2W, c0 = CPT * (tid % K2W);
  float s[CPT];
#pragma unroll
  for (int e = 0; e < CPT; ++e) s[e] = 0.f;
#pragma unroll
  for (int w = 0; w < K2W; ++w) {
    const float* p = (const float*)lds + (w * 64 + row) * RS + c0;
#pragma unroll
    for (int g = 0; g < CPT / 4; ++g) {
      f32x4 v = *(const f32x4*)(p + 4 * g);
      s[4 * g] += v[0]; s[4 * g + 1] += v[1]; s[4 * g + 2] += v[2]; s[4 * g + 3] += v[3];
    }
  }
  u16* op = out + (size_t)(64 * band + row) * DM + 64 * cg + c0;
#pragma unroll
  for (int g = 0; g < CPT / 8; ++g) {
    u32x4 w0;
#pragma unroll
    for (int e = 0; e < 4; ++e) w0[e] = cvtpk(s[8 * g + 2 * e], s[8 * g + 2 * e + 1]);
    *(u32x4*)(op + 8 * g) = w0;
  }
}

extern "C" void kernel_launch(void* const* d_in, const int* in_sizes, int n_in, void* d_out, int out_size, void* d_ws,
                              size_t ws_size, hipStream_t stream) {
  const u16* x = (const u16*)d_in[0];
  const u16* kc = (const u16*)d_in[1];
  const u16* vc = (const u16*)d_in[2];
  const u16* wq = (const u16*)d_in[3];
  const u16* wo = (const u16*)d_in[4];
  u16* ows = (u16*)d_ws;
  u16* wot = (u16*)((char*)d_ws + (size_t)56 * 256 * 1024);
  constexpr int LDS2 = K2W * 64 * RS * 4;
  (void)hipFuncSetAttribute((const void*)k1_qattn, hipFuncAttributeMaxDynamicSharedMemorySize, L_END1);
  (void)hipFuncSetAttribute((const void*)k2_oproj, hipFuncAttributeMaxDynamicSharedMemorySize, LDS2);
  hipLaunchKernelGGL(k1_qattn, dim3(256), dim3(512), L_END1, stream, x, kc, vc, wq, wo, ows, wot);
  hipLaunchKernelGGL(k2_oproj, dim3(256), dim3(K2W * 64), LDS2, stream, ows, wot, (u16*)d_out);
}
```

```cpp
#include <hip/hip_runtime.h>
#include <stdint.h>

typedef unsigned short u16;

#define MHA_ASM \
    "s_load_dwordx8 s[8:15], %0, 0x0\n" \
    "s_load_dwordx4 s[16:19], %0, 0x20\n" \
    "s_load_dwordx2 s[20:21], %0, 0x30\n" \
    "s_getpc_b64 s[90:91]\n" \
    "Lmha_pc0:\n" \
    "v_subrev_u32 v99, 192, v0\n" \
    "v_cmp_gt_u32 vcc, 16, v99\n" \
    "s_lshr_b32 s55, %1, 3\n" \
    "s_and_saveexec_b64 s[48:49], vcc\n" \
    "s_cbranch_execz Lmha_nopf\n" \
    "v_lshl_add_u32 v99, v99, 5, s55\n" \
    "v_lshlrev_b32 v99, 7, v99\n" \
    "v_min_u32 v99, Lmha_hot_end-Lmha_pc0-4, v99\n" \
    "global_load_dword v13, v99, s[90:91]\n" \
    "Lmha_nopf:\n" \
    "s_mov_b64 exec, -1\n" \
    "v_lshrrev_b32 v99, 6, v0\n" \
    "v_and_b32 v96, 63, v0\n" \
    "v_readfirstlane_b32 s52, v99\n" \
    "s_and_b32 s55, %1, 1\n" \
    "s_lshl_b32 s55, s55, 2\n" \
    "s_bfe_u32 s53, %1, 0x20003\n" \
    "s_add_u32 s53, s53, s55\n" \
    "s_bfe_u32 s55, %1, 0x20001\n" \
    "s_lshl_b32 s55, s55, 3\n" \
    "s_lshr_b32 s54, %1, 5\n" \
    "s_add_u32 s54, s54, s55\n" \
    "v_bfe_u32 v97, v96, 2, 3\n" \
    "v_bfe_u32 v99, v96, 4, 1\n" \
    "v_and_b32 v100, 3, v96\n" \
    "v_xor_b32 v100, v100, v99\n" \
    "v_lshrrev_b32 v99, 5, v96\n" \
    "v_lshl_add_u32 v98, v99, 2, v100\n" \
    "v_lshlrev_b32 v98, 4, v98\n" \
    "s_add_u32 s55, s53, s54\n" \
    "s_mul_i32 s57, s55, 43\n" \
    "s_lshr_b32 s57, s57, 7\n" \
    "s_mul_i32 s57, s57, 3\n" \
    "s_sub_u32 s57, s55, s57\n" \
    "s_mul_i32 s57, s57, 3\n" \
    "s_cmp_lt_u32 s52, 2\n" \
    "s_cselect_b32 s74, 1, 0\n" \
    "s_mul_i32 s73, s52, 14336\n" \
    "s_mov_b32 s62, 16384\n" \
    "s_mov_b32 s58, 49152\n" \
    "v_xor_b32 v101, 32, v98\n" \
    "s_cbranch_scc0 Lmha_setW\n" \
    "v_mul_u32_u24 v99, 1152, v97\n" \
    "s_mul_i32 s60, s52, 129024\n" \
    "s_mov_b32 s68, 36864\n" \
    "s_mov_b32 s72, -73600\n" \
    "v_add_u32 v100, v99, v98\n" \
    "v_add_u32 v101, v99, v101\n" \
    "v_add_u32 v1, 0, v100\n" \
    "v_add_u32 v2, 8192, v101\n" \
    "v_add_u32 v3, 16384, v100\n" \
    "v_add_u32 v4, 24576, v101\n" \
    "s_mov_b32 s99, s60\n" \
    "s_add_u32 s98, s60, 1152\n" \
    "s_lshl_b32 s57, s57, 7\n" \
    "s_add_u32 s60, s60, s57\n" \
    "s_mul_i32 s55, s53, 258048\n" \
    "s_waitcnt lgkmcnt(0)\n" \
    "s_add_u32 s24, s8, s55\n" \
    "s_addc_u32 s25, s9, 0\n" \
    "s_and_b32 s25, s25, 0xffff\n" \
    "s_mov_b32 s26, 258048\n" \
    "s_mov_b32 s27, 0x00020000\n" \
    "s_branch Lmha_setD\n" \
    "Lmha_setW:\n" \
    "v_lshlrev_b32 v99, 13, v97\n" \
    "s_mov_b32 s60, 0\n" \
    "s_mov_b32 s68, 131072\n" \
    "s_mov_b32 s72, 262144\n" \
    "v_add_u32 v100, v99, v98\n" \
    "v_add_u32 v101, v99, v101\n" \
    "v_add_u32 v1, 4096, v100\n" \
    "v_add_u32 v2, 3200, v100\n" \
    "v_add_u32 v3, 67584, v101\n" \
    "v_add_u32 v4, 66688, v101\n" \
    "s_mov_b32 s99, 0\n" \
    "s_mov_b32 s98, 4718592\n" \
    "s_lshl_b32 s60, s57, 19\n" \
    "s_lshl_b32 s56, s54, 8\n" \
    "s_waitcnt lgkmcnt(0)\n" \
    "s_add_u32 s24, s14, s56\n" \
    "s_addc_u32 s25, s15, 0\n" \
    "s_sub_u32 s24, s24, 4096\n" \
    "s_subb_u32 s25, s25, 0\n" \
    "s_and_b32 s25, s25, 0xffff\n" \
    "s_mov_b32 s26, 4714752\n" \
    "s_mov_b32 s27, 0x00020000\n" \
    "Lmha_setD:\n" \
    "s_cmp_eq_u32 s52, 3\n" \
    "s_cbranch_scc1 Lmha_no01\n" \
    "s_add_u32 m0, s62, s73\n" \
    "s_add_u32 s61, s60, s68\n" \
    "buffer_load_dwordx4 v1, s[24:27], s60 offen offset:0 lds\n" \
    "buffer_load_dwordx4 v2, s[24:27], s60 offen offset:1024 lds\n" \
    "buffer_load_dwordx4 v3, s[24:27], s60 offen offset:2048 lds\n" \
    "buffer_load_dwordx4 v4, s[24:27], s60 offen offset:3072 lds\n" \
    "s_add_u32 m0, m0, 4096\n" \
    "s_lshl1_add_u32 s60, s68, s60\n" \
    "buffer_load_dwordx4 v1, s[24:27], s61 offen offset:0 lds\n" \
    "buffer_load_dwordx4 v2, s[24:27], s61 offen offset:1024 lds\n" \
    "buffer_load_dwordx4 v3, s[24:27], s61 offen offset:2048 lds\n" \
    "buffer_load_dwordx4 v4, s[24:27], s61 offen offset:3072 lds\n" \
    "s_add_u32 m0, m0, 4096\n" \
    "s_lshl1_add_u32 s61, s68, s61\n" \
    "buffer_load_dwordx4 v1, s[24:27], s60 offen offset:0 lds\n" \
    "buffer_load_dwordx4 v2, s[24:27], s60 offen offset:1024 lds\n" \
    "buffer_load_dwordx4 v3, s[24:27], s60 offen offset:2048 lds\n" \
    "buffer_load_dwordx4 v4, s[24:27], s60 offen offset:3072 lds\n" \
    "s_add_u32 m0, m0, 4096\n" \
    "s_nop 0\n" \
    "buffer_load_dwordx4 v1, s[24:27], s61 offen offset:0 lds\n" \
    "buffer_load_dwordx4 v2, s[24:27], s61 offen offset:1024 lds\n" \
    "s_cmp_lg_u32 s74, 0\n" \
    "s_cbranch_scc1 Lmha_skp0\n" \
    "buffer_load_dwordx4 v3, s[24:27], s61 offen offset:2048 lds\n" \
    "buffer_load_dwordx4 v4, s[24:27], s61 offen offset:3072 lds\n" \
    "Lmha_skp0:\n" \
    "s_add_u32 s60, s60, s72\n" \
    "s_cmp_eq_u32 s60, s98\n" \
    "s_cselect_b32 s60, s99, s60\n" \
    "s_add_u32 s62, s62, s58\n" \
    "s_cmp_eq_u32 s62, 163840\n" \
    "s_cselect_b32 s62, 16384, s62\n" \
    "Lmha_no01:\n" \
    "v_lshlrev_b32 v12, 7, v96\n" \
    "v_bfe_u32 v99, v96, 2, 2\n" \
    "v_lshlrev_b32 v99, 8, v99\n" \
    "v_lshrrev_b32 v100, 4, v96\n" \
    "v_lshl_add_u32 v99, v100, 6, v99\n" \
    "v_and_b32 v100, 3, v96\n" \
    "v_lshl_add_u32 v25, v100, 4, v99\n" \
    "v_xor_b32 v26, 16, v25\n" \
    "v_xor_b32 v27, 32, v25\n" \
    "v_xor_b32 v28, 48, v25\n" \
    "v_mul_u32_u24 v99, 1152, v97\n" \
    "v_xor_b32 v101, 32, v98\n" \
    "v_add_u32 v100, v99, v98\n" \
    "v_add_u32 v101, v99, v101\n" \
    "v_add_u32 v5, 0, v100\n" \
    "v_add_u32 v6, 8192, v101\n" \
    "v_add_u32 v7, 16384, v100\n" \
    "v_add_u32 v8, 24576, v101\n" \
    "v_mov_b32 v99, 16908288\n" \
    "global_load_dwordx2 v[10:11], v99, s[20:21] sc1\n" \
    "s_lshl_b32 s55, s53, 5\n" \
    "s_add_u32 s55, s55, s54\n" \
    "s_lshl_b32 s55, s55, 17\n" \
    "s_add_u32 s44, s10, s55\n" \
    "s_addc_u32 s45, s11, 0\n" \
    "s_and_b32 s45, s45, 0xffff\n" \
    "s_mov_b32 s46, 131072\n" \
    "s_mov_b32 s47, 0x00020000\n" \
    "s_add_u32 s36, s12, s55\n" \
    "s_addc_u32 s37, s13, 0\n" \
    "s_and_b32 s37, s37, 0xffff\n" \
    "s_mov_b32 s38, 131072\n" \
    "s_mov_b32 s39, 0x00020000\n" \
    "s_lshr_b32 s55, %1, 4\n" \
    "s_xor_b32 s55, s55, %1\n" \
    "s_and_b32 s55, s55, 15\n" \
    "s_lshl_b32 s65, s55, 13\n" \
    "s_mov_b32 s66, 0\n" \
    "s_mov_b32 s59, 16384\n" \
    "s_lshl_b32 s55, s53, 21\n" \
    "s_lshl_b32 s56, s54, 13\n" \
    "s_add_u32 s55, s55, s56\n" \
    "s_add_u32 s40, s20, s55\n" \
    "s_addc_u32 s41, s21, 0\n" \
    "s_and_b32 s41, s41, 0xffff\n" \
    "s_mov_b32 s42, 0\n" \
    "s_mov_b32 s43, 0x00020000\n" \
    "s_sub_u32 s42, 1835008, s56\n" \
    "v_and_b32 v102, 31, v96\n" \
    "v_lshrrev_b32 v103, 5, v96\n" \
    "v_bfe_u32 v104, v96, 2, 2\n" \
    "v_and_b32 v105, 7, v96\n" \
    "v_lshlrev_b32 v105, 6, v105\n" \
    "v_xor_b32 v99, v103, v104\n" \
    "v_lshl_add_u32 v99, v99, 4, v105\n" \
    "v_lshrrev_b32 v100, 3, v102\n" \
    "v_and_b32 v20, 255, v99\n" \
    "v_lshrrev_b32 v101, 2, v102\n" \
    "v_lshl_add_u32 v20, v101, 10, v20\n" \
    "v_lshl_add_u32 v14, v100, 10, v99\n" \
    "s_lshl_b32 s55, s52, 13\n" \
    "s_add_u32 s55, s55, 16384\n" \
    "v_add_u32 v14, s55, v14\n" \
    "v_xor_b32 v21, 32, v20\n" \
    "v_xor_b32 v15, 32, v14\n" \
    "v_bfe_u32 v97, v96, 4, 1\n" \
    "v_lshlrev_b32 v97, 1, v97\n" \
    "v_and_b32 v98, 1, v96\n" \
    "v_bfe_u32 v106, v96, 1, 1\n" \
    "v_or_b32 v99, v97, v98\n" \
    "v_lshlrev_b32 v100, 1, v103\n" \
    "v_xor_b32 v99, v99, v100\n" \
    "v_lshlrev_b32 v99, 4, v99\n" \
    "v_lshl_add_u32 v99, v106, 3, v99\n" \
    "v_lshl_add_u32 v99, v104, 6, v99\n" \
    "v_lshl_add_u32 v99, v103, 11, v99\n" \
    "v_add_u32 v18, 45056, v99\n" \
    "v_xor_b32 v99, 16, v99\n" \
    "v_add_u32 v19, 45312, v99\n" \
    "v_or_b32 v99, v97, v106\n" \
    "v_xor_b32 v99, v99, v103\n" \
    "v_lshlrev_b32 v99, 4, v99\n" \
    "v_lshl_add_u32 v99, v98, 3, v99\n" \
    "v_lshl_add_u32 v99, v104, 6, v99\n" \
    "v_lshl_add_u32 v99, v103, 8, v99\n" \
    "s_lshl_b32 s55, s52, 12\n" \
    "s_and_b32 s56, s52, 1\n" \
    "s_lshl_b32 s56, s56, 9\n" \
    "s_add_u32 s55, s55, s56\n" \
    "v_add_u32 v16, s55, v99\n" \
    "v_mul_u32_u24 v100, 768, v103\n" \
    "v_add_u32 v99, v99, v100\n" \
    "v_add_u32 v22, 8192, v99\n" \
    "v_xor_b32 v99, 32, v99\n" \
    "v_add_u32 v23, 10240, v99\n" \
    "v_xor_b32 v17, 32, v16\n" \
    "v_add_u32 v17, 1024, v17\n" \
    "v_lshlrev_b32 v24, 4, v96\n" \
    "s_mov_b32 s69, 0x47800000\n" \
    "s_mov_b32 s70, 0x3e0293ee\n" \
    "s_mov_b32 s76, 0\n" \
    "s_mov_b32 s71, 0\n" \
    "s_lshr_b32 s55, %1, 3\n" \
    "s_bfe_u32 s56, %1, 0x20001\n" \
    "s_and_b32 s57, %1, 1\n" \
    "s_mul_i32 s56, s56, 7\n" \
    "s_mul_i32 s78, s55, 37\n" \
    "s_lshr_b32 s78, s78, 8\n" \
    "s_mul_i32 s77, s78, 7\n" \
    "s_sub_u32 s77, s55, s77\n" \
    "s_mul_i32 s63, s57, 5\n" \
    "s_add_u32 s78, s78, s63\n" \
    "s_lshl2_add_u32 s57, s57, s77\n" \
    "s_cmp_lt_u32 s55, 28\n" \
    "s_cselect_b32 s77, s77, s57\n" \
    "s_cselect_b32 s78, s78, 4\n" \
    "s_cmp_lt_u32 s77, 7\n" \
    "s_cselect_b32 s78, s78, 9\n" \
    "s_add_u32 s77, s77, s56\n" \
    "s_lshl_b32 s55, s78, 7\n" \
    "s_add_u32 s28, s16, s55\n" \
    "s_addc_u32 s29, s17, 0\n" \
    "s_and_b32 s29, s29, 0xffff\n" \
    "s_mov_b32 s30, 0\n" \
    "s_mov_b32 s31, 0x00020000\n" \
    "s_sub_u32 s55, 4718592, s55\n" \
    "s_cmp_lt_u32 s78, 9\n" \
    "s_cselect_b32 s30, s55, 0\n" \
    "s_mov_b32 s79, 0\n" \
    "s_cmp_eq_u32 s52, 3\n" \
    "s_cbranch_scc1 Lmha_pathS\n" \
    "s_add_u32 m0, s62, s73\n" \
    "s_add_u32 s61, s60, s68\n" \
    "buffer_load_dwordx4 v1, s[24:27], s60 offen offset:0 lds\n" \
    "buffer_load_dwordx4 v2, s[24:27], s60 offen offset:1024 lds\n" \
    "buffer_load_dwordx4 v3, s[24:27], s60 offen offset:2048 lds\n" \
    "buffer_load_dwordx4 v4, s[24:27], s60 offen offset:3072 lds\n" \
    "s_add_u32 m0, m0, 4096\n" \
    "s_lshl1_add_u32 s60, s68, s60\n" \
    "buffer_load_dwordx4 v1, s[24:27], s61 offen offset:0 lds\n" \
    "buffer_load_dwordx4 v2, s[24:27], s61 offen offset:1024 lds\n" \
    "s_waitcnt vmcnt(7)\n" \
    "s_barrier\n" \
    "ds_read_b128 v[80:83], v14 offset:0\n" \
    "ds_read_b64_tr_b16 v[48:49], v18 offset:0\n" \
    "ds_read_b64_tr_b16 v[50:51], v19 offset:0\n" \
    "ds_read_b128 v[84:87], v14 offset:4096\n" \
    "ds_read_b64_tr_b16 v[52:53], v18 offset:512\n" \
    "ds_read_b64_tr_b16 v[54:55], v19 offset:512\n" \
    "ds_read_b64_tr_b16 v[56:57], v18 offset:1024\n" \
    "ds_read_b64_tr_b16 v[58:59], v19 offset:1024\n" \
    "ds_read_b64_tr_b16 v[60:61], v18 offset:1536\n" \
    "ds_read_b64_tr_b16 v[62:63], v19 offset:1536\n" \
    "s_waitcnt lgkmcnt(7)\n" \
    "v_mfma_f32_32x32x16_bf16 v[128:143], v[48:51], v[80:83], 0\n" \
    "ds_read_b128 v[88:91], v15 offset:0\n" \
    "ds_read_b64_tr_b16 v[64:65], v18 offset:4096\n" \
    "buffer_load_dwordx4 v3, s[24:27], s61 offen offset:2048 lds\n" \
    "buffer_load_dwordx4 v4, s[24:27], s61 offen offset:3072 lds\n" \
    "s_add_u32 m0, m0, 4096\n" \
    "s_lshl1_add_u32 s61, s68, s61\n" \
    "s_waitcnt lgkmcnt(8)\n" \
    "v_mfma_f32_32x32x16_bf16 v[192:207], v[48:51], v[84:87], 0\n" \
    "ds_read_b64_tr_b16 v[66:67], v19 offset:4096\n" \
    "ds_read_b128 v[92:95], v15 offset:4096\n" \
    "buffer_load_dwordx4 v1, s[24:27], s60 offen offset:0 lds\n" \
    "s_waitcnt lgkmcnt(8)\n" \
    "v_mfma_f32_32x32x16_bf16 v[144:159], v[52:55], v[80:83], 0\n" \
    "ds_read_b64_tr_b16 v[68:69], v18 offset:4608\n" \
    "ds_read_b64_tr_b16 v[70:71], v19 offset:4608\n" \
    "buffer_load_dwordx4 v2, s[24:27], s60 offen offset:1024 lds\n" \
    "v_mfma_f32_32x32x16_bf16 v[208:223], v[52:55], v[84:87], 0\n" \
    "ds_read_b64_tr_b16 v[72:73], v18 offset:5120\n" \
    "ds_read_b64_tr_b16 v[74:75], v19 offset:5120\n" \
    "buffer_load_dwordx4 v3, s[24:27], s60 offen offset:2048 lds\n" \
    "buffer_load_dwordx4 v4, s[24:27], s60 offen offset:3072 lds\n" \
    "s_add_u32 m0, m0, 4096\n" \
    "s_waitcnt lgkmcnt(7)\n" \
    "v_mfma_f32_32x32x16_bf16 v[160:175], v[56:59], v[80:83], 0\n" \
    "ds_read_b64_tr_b16 v[76:77], v18 offset:5632\n" \
    "ds_read_b64_tr_b16 v[78:79], v19 offset:5632\n" \
    "buffer_load_dwordx4 v1, s[24:27], s61 offen offset:0 lds\n" \
    "v_mfma_f32_32x32x16_bf16 v[224:239], v[56:59], v[84:87], 0\n" \
    "buffer_load_dwordx4 v2, s[24:27], s61 offen offset:1024 lds\n" \
    "v_mfma_f32_32x32x16_bf16 v[176:191], v[60:63], v[80:83], 0\n" \
    "s_cmp_lg_u32 s74, 0\n" \
    "s_cbranch_scc1 Lmha_skp1\n" \
    "buffer_load_dwordx4 v3, s[24:27], s61 offen offset:2048 lds\n" \
    "buffer_load_dwordx4 v4, s[24:27], s61 offen offset:3072 lds\n" \
    "Lmha_skp1:\n" \
    "v_mfma_f32_32x32x16_bf16 v[240:255], v[60:63], v[84:87], 0\n" \
    "s_add_u32 s60, s60, s72\n" \
    "s_cmp_eq_u32 s60, s98\n" \
    "s_cselect_b32 s60, s99, s60\n" \
    "s_add_u32 s62, s62, s58\n" \
    "s_cmp_eq_u32 s62, 163840\n" \
    "s_cselect_b32 s62, 16384, s62\n" \
    "s_waitcnt lgkmcnt(2)\n" \
    "v_mfma_f32_32x32x16_bf16 v[128:143], v[64:67], v[88:91], v[128:143]\n" \
    "ds_read_b128 v[80:83], v14 offset:512\n" \
    "ds_read_b64_tr_b16 v[48:49], v18 offset:8192\n" \
    "s_add_u32 m0, s62, s73\n" \
    "s_add_u32 s61, s60, s68\n" \
    "v_mfma_f32_32x32x16_bf16 v[192:207], v[64:67], v[92:95], v[192:207]\n" \
    "ds_read_b64_tr_b16 v[50:51], v19 offset:8192\n" \
    "ds_read_b128 v[84:87], v14 offset:4608\n" \
    "buffer_load_dwordx4 v1, s[24:27], s60 offen offset:0 lds\n" \
    "v_mfma_f32_32x32x16_bf16 v[144:159], v[68:71], v[88:91], v[144:159]\n" \
    "ds_read_b64_tr_b16 v[52:53], v18 offset:8704\n" \
    "ds_read_b64_tr_b16 v[54:55], v19 offset:8704\n" \
    "buffer_load_dwordx4 v2, s[24:27], s60 offen offset:1024 lds\n" \
    "v_mfma_f32_32x32x16_bf16 v[208:223], v[68:71], v[92:95], v[208:223]\n" \
    "ds_read_b64_tr_b16 v[56:57], v18 offset:9216\n" \
    "ds_read_b64_tr_b16 v[58:59], v19 offset:9216\n" \
    "buffer_load_dwordx4 v3, s[24:27], s60 offen offset:2048 lds\n" \
    "buffer_load_dwordx4 v4, s[24:27], s60 offen offset:3072 lds\n" \
    "s_add_u32 m0, m0, 4096\n" \
    "s_lshl1_add_u32 s60, s68, s60\n" \
    "v_mfma_f32_32x32x16_bf16 v[160:175], v[72:75], v[88:91], v[160:175]\n" \
    "ds_read_b64_tr_b16 v[60:61], v18 offset:9728\n" \
    "ds_read_b64_tr_b16 v[62:63], v19 offset:9728\n" \
    "buffer_load_dwordx4 v1, s[24:27], s61 offen offset:0 lds\n" \
    "v_mfma_f32_32x32x16_bf16 v[224:239], v[72:75], v[92:95], v[224:239]\n" \
    "buffer_load_dwordx4 v2, s[24:27], s61 offen offset:1024 lds\n" \
    "s_waitcnt lgkmcnt(7)\n" \
    "v_mfma_f32_32x32x16_bf16 v[176:191], v[76:79], v[88:91], v[176:191]\n" \
    "buffer_load_dwordx4 v3, s[24:27], s61 offen offset:2048 lds\n" \
    "v_mfma_f32_32x32x16_bf16 v[240:255], v[76:79], v[92:95], v[240:255]\n" \
    "buffer_load_dwordx4 v4, s[24:27], s61 offen offset:3072 lds\n" \
    "s_add_u32 m0, m0, 4096\n" \
    "s_lshl1_add_u32 s61, s68, s61\n" \
    "buffer_load_dwordx4 v1, s[24:27], s60 offen offset:0 lds\n" \
    "s_add_u32 s76, s76, s58\n" \
    "s_cmp_eq_u32 s76, 147456\n" \
    "s_cselect_b32 s64, -98304, s58\n" \
    "s_cselect_b32 s76, 0, s76\n" \
    "v_mfma_f32_32x32x16_bf16 v[128:143], v[48:51], v[80:83], v[128:143]\n" \
    "ds_read_b128 v[88:91], v15 offset:512\n" \
    "ds_read_b64_tr_b16 v[64:65], v18 offset:12288\n" \
    "buffer_load_dwordx4 v2, s[24:27], s60 offen offset:1024 lds\n" \
    "s_waitcnt lgkmcnt(3)\n" \
    "v_mfma_f32_32x32x16_bf16 v[192:207], v[48:51], v[84:87], v[192:207]\n" \
    "ds_read_b64_tr_b16 v[66:67], v19 offset:12288\n" \
    "ds_read_b128 v[92:95], v15 offset:4608\n" \
    "buffer_load_dwordx4 v3, s[24:27], s60 offen offset:2048 lds\n" \
    "v_mfma_f32_32x32x16_bf16 v[144:159], v[52:55], v[80:83], v[144:159]\n" \
    "ds_read_b64_tr_b16 v[68:69], v18 offset:12800\n" \
    "ds_read_b64_tr_b16 v[70:71], v19 offset:12800\n" \
    "buffer_load_dwordx4 v4, s[24:27], s60 offen offset:3072 lds\n" \
    "s_add_u32 m0, m0, 4096\n" \
    "v_mfma_f32_32x32x16_bf16 v[208:223], v[52:55], v[84:87], v[208:223]\n" \
    "ds_read_b64_tr_b16 v[72:73], v18 offset:13312\n" \
    "ds_read_b64_tr_b16 v[74:75], v19 offset:13312\n" \
    "buffer_load_dwordx4 v1, s[24:27], s61 offen offset:0 lds\n" \
    "buffer_load_dwordx4 v2, s[24:27], s61 offen offset:1024 lds\n" \
    "v_mfma_f32_32x32x16_bf16 v[160:175], v[56:59], v[80:83], v[160:175]\n" \
    "ds_read_b64_tr_b16 v[76:77], v18 offset:13824\n" \
    "ds_read_b64_tr_b16 v[78:79], v19 offset:13824\n" \
    "s_cmp_lg_u32 s74, 0\n" \
    "s_cbranch_scc1 Lmha_skp2\n" \
    "buffer_load_dwordx4 v3, s[24:27], s61 offen offset:2048 lds\n" \
    "buffer_load_dwordx4 v4, s[24:27], s61 offen offset:3072 lds\n" \
    "Lmha_skp2:\n" \
    "v_mfma_f32_32x32x16_bf16 v[224:239], v[56:59], v[84:87], v[224:239]\n" \
    "s_add_u32 s60, s60, s72\n" \
    "s_cmp_eq_u32 s60, s98\n" \
    "s_cselect_b32 s60, s99, s60\n" \
    "s_waitcnt lgkmcnt(4)\n" \
    "v_mfma_f32_32x32x16_bf16 v[176:191], v[60:63], v[80:83], v[176:191]\n" \
    "s_add_u32 s62, s62, s58\n" \
    "s_cmp_eq_u32 s62, 163840\n" \
    "s_cselect_b32 s62, 16384, s62\n" \
    "v_add_u32 v14, s64, v14\n" \
    "v_add_u32 v15, s64, v15\n" \
    "v_add_u32 v18, s64, v18\n" \
    "v_add_u32 v19, s64, v19\n" \
    "v_mfma_f32_32x32x16_bf16 v[240:255], v[60:63], v[84:87], v[240:255]\n" \
    "s_mov_b32 s63, 6\n" \
    "s_waitcnt vmcnt(14)\n" \
    "s_waitcnt lgkmcnt(0)\n" \
    "s_barrier\n" \
    ".p2align 6\n" \
    "s_nop 0\n" \
    "Lmha_mqp:\n" \
    "v_mfma_f32_32x32x16_bf16 v[128:143], v[64:67], v[88:91], v[128:143]\n" \
    "ds_read_b128 v[80:83], v14 offset:0\n" \
    "ds_read_b64_tr_b16 v[48:49], v18 offset:0\n" \
    "s_add_u32 m0, s62, s73\n" \
    "s_add_u32 s61, s60, s68\n" \
    "v_mfma_f32_32x32x16_bf16 v[192:207], v[64:67], v[92:95], v[192:207]\n" \
    "ds_read_b64_tr_b16 v[50:51], v19 offset:0\n" \
    "ds_read_b128 v[84:87], v14 offset:4096\n" \
    "buffer_load_dwordx4 v1, s[24:27], s60 offen offset:0 lds\n" \
    "v_mfma_f32_32x32x16_bf16 v[144:159], v[68:71], v[88:91], v[144:159]\n" \
    "ds_read_b64_tr_b16 v[52:53], v18 offset:512\n" \
    "ds_read_b64_tr_b16 v[54:55], v19 offset:512\n" \
    "v_mfma_f32_32x32x16_bf16 v[208:223], v[68:71], v[92:95], v[208:223]\n" \
    "ds_read_b64_tr_b16 v[56:57], v18 offset:1024\n" \
    "ds_read_b64_tr_b16 v[58:59], v19 offset:1024\n" \
    "buffer_load_dwordx4 v2, s[24:27], s60 offen offset:1024 lds\n" \
    "v_mfma_f32_32x32x16_bf16 v[160:175], v[72:75], v[88:91], v[160:175]\n" \
    "ds_read_b64_tr_b16 v[60:61], v18 offset:1536\n" \
    "ds_read_b64_tr_b16 v[62:63], v19 offset:1536\n" \
    "v_mfma_f32_32x32x16_bf16 v[224:239], v[72:75], v[92:95], v[224:239]\n" \
    "buffer_load_dwordx4 v3, s[24:27], s60 offen offset:2048 lds\n" \
    "v_mfma_f32_32x32x16_bf16 v[176:191], v[76:79], v[88:91], v[176:191]\n" \
    "buffer_load_dwordx4 v4, s[24:27], s60 offen offset:3072 lds\n" \
    "s_add_u32 m0, m0, 4096\n" \
    "s_lshl1_add_u32 s60, s68, s60\n" \
    "v_mfma_f32_32x32x16_bf16 v[240:255], v[76:79], v[92:95], v[240:255]\n" \
    "s_waitcnt lgkmcnt(7)\n" \
    "v_mfma_f32_32x32x16_bf16 v[128:143], v[48:51], v[80:83], v[128:143]\n" \
    "ds_read_b128 v[88:91], v15 offset:0\n" \
    "ds_read_b64_tr_b16 v[64:65], v18 offset:4096\n" \
    "buffer_load_dwordx4 v1, s[24:27], s61 offen offset:0 lds\n" \
    "s_waitcnt lgkmcnt(7)\n" \
    "v_mfma_f32_32x32x16_bf16 v[192:207], v[48:51], v[84:87], v[192:207]\n" \
    "ds_read_b64_tr_b16 v[66:67], v19 offset:4096\n" \
    "ds_read_b128 v[92:95], v15 offset:4096\n" \
    "s_waitcnt lgkmcnt(7)\n" \
    "v_mfma_f32_32x32x16_bf16 v[144:159], v[52:55], v[80:83], v[144:159]\n" \
    "ds_read_b64_tr_b16 v[68:69], v18 offset:4608\n" \
    "ds_read_b64_tr_b16 v[70:71], v19 offset:4608\n" \
    "buffer_load_dwordx4 v2, s[24:27], s61 offen offset:1024 lds\n" \
    "v_mfma_f32_32x32x16_bf16 v[208:223], v[52:55], v[84:87], v[208:223]\n" \
    "ds_read_b64_tr_b16 v[72:73], v18 offset:5120\n" \
    "ds_read_b64_tr_b16 v[74:75], v19 offset:5120\n" \
    "s_waitcnt lgkmcnt(8)\n" \
    "v_mfma_f32_32x32x16_bf16 v[160:175], v[56:59], v[80:83], v[160:175]\n" \
    "ds_read_b64_tr_b16 v[76:77], v18 offset:5632\n" \
    "ds_read_b64_tr_b16 v[78:79], v19 offset:5632\n" \
    "buffer_load_dwordx4 v3, s[24:27], s61 offen offset:2048 lds\n" \
    "v_mfma_f32_32x32x16_bf16 v[224:239], v[56:59], v[84:87], v[224:239]\n" \
    "buffer_load_dwordx4 v4, s[24:27], s61 offen offset:3072 lds\n" \
    "s_add_u32 m0, m0, 4096\n" \
    "s_lshl1_add_u32 s61, s68, s61\n" \
    "v_mfma_f32_32x32x16_bf16 v[176:191], v[60:63], v[80:83], v[176:191]\n" \
    "v_mfma_f32_32x32x16_bf16 v[240:255], v[60:63], v[84:87], v[240:255]\n" \
    "buffer_load_dwordx4 v1, s[24:27], s60 offen offset:0 lds\n" \
    "s_waitcnt lgkmcnt(7)\n" \
    "v_mfma_f32_32x32x16_bf16 v[128:143], v[64:67], v[88:91], v[128:143]\n" \
    "ds_read_b128 v[80:83], v14 offset:512\n" \
    "ds_read_b64_tr_b16 v[48:49], v18 offset:8192\n" \
    "s_waitcnt lgkmcnt(7)\n" \
    "v_mfma_f32_32x32x16_bf16 v[192:207], v[64:67], v[92:95], v[192:207]\n" \
    "ds_read_b64_tr_b16 v[50:51], v19 offset:8192\n" \
    "ds_read_b128 v[84:87], v14 offset:4608\n" \
    "buffer_load_dwordx4 v2, s[24:27], s60 offen offset:1024 lds\n" \
    "s_waitcnt lgkmcnt(7)\n" \
    "v_mfma_f32_32x32x16_bf16 v[144:159], v[68:71], v[88:91], v[144:159]\n" \
    "ds_read_b64_tr_b16 v[52:53], v18 offset:8704\n" \
    "ds_read_b64_tr_b16 v[54:55], v19 offset:8704\n" \
    "buffer_load_dwordx4 v3, s[24:27], s60 offen offset:2048 lds\n" \
    "v_mfma_f32_32x32x16_bf16 v[208:223], v[68:71], v[92:95], v[208:223]\n" \
    "ds_read_b64_tr_b16 v[56:57], v18 offset:9216\n" \
    "ds_read_b64_tr_b16 v[58:59], v19 offset:9216\n" \
    "s_waitcnt lgkmcnt(8)\n" \
    "v_mfma_f32_32x32x16_bf16 v[160:175], v[72:75], v[88:91], v[160:175]\n" \
    "ds_read_b64_tr_b16 v[60:61], v18 offset:9728\n" \
    "ds_read_b64_tr_b16 v[62:63], v19 offset:9728\n" \
    "buffer_load_dwordx4 v4, s[24:27], s60 offen offset:3072 lds\n" \
    "s_add_u32 m0, m0, 4096\n" \
    "v_mfma_f32_32x32x16_bf16 v[224:239], v[72:75], v[92:95], v[224:239]\n" \
    "v_mfma_f32_32x32x16_bf16 v[176:191], v[76:79], v[88:91], v[176:191]\n" \
    "buffer_load_dwordx4 v1, s[24:27], s61 offen offset:0 lds\n" \
    "v_mfma_f32_32x32x16_bf16 v[240:255], v[76:79], v[92:95], v[240:255]\n" \
    "s_add_u32 s76, s76, s58\n" \
    "s_cmp_eq_u32 s76, 147456\n" \
    "s_cselect_b32 s64, -98304, s58\n" \
    "s_cselect_b32 s76, 0, s76\n" \
    "s_waitcnt lgkmcnt(6)\n" \
    "v_mfma_f32_32x32x16_bf16 v[128:143], v[48:51], v[80:83], v[128:143]\n" \
    "ds_read_b128 v[88:91], v15 offset:512\n" \
    "ds_read_b64_tr_b16 v[64:65], v18 offset:12288\n" \
    "buffer_load_dwordx4 v2, s[24:27], s61 offen offset:1024 lds\n" \
    "v_mfma_f32_32x32x16_bf16 v[192:207], v[48:51], v[84:87], v[192:207]\n" \
    "ds_read_b64_tr_b16 v[66:67], v19 offset:12288\n" \
    "ds_read_b128 v[92:95], v15 offset:4608\n" \
    "s_cmp_lg_u32 s74, 0\n" \
    "s_cbranch_scc1 Lmha_sklp\n" \
    "buffer_load_dwordx4 v3, s[24:27], s61 offen offset:2048 lds\n" \
    "buffer_load_dwordx4 v4, s[24:27], s61 offen offset:3072 lds\n" \
    "Lmha_sklp:\n" \
    "s_waitcnt lgkmcnt(4)\n" \
    "v_mfma_f32_32x32x16_bf16 v[144:159], v[52:55], v[80:83], v[144:159]\n" \
    "ds_read_b64_tr_b16 v[68:69], v18 offset:12800\n" \
    "ds_read_b64_tr_b16 v[70:71], v19 offset:12800\n" \
    "v_mfma_f32_32x32x16_bf16 v[208:223], v[52:55], v[84:87], v[208:223]\n" \
    "ds_read_b64_tr_b16 v[72:73], v18 offset:13312\n" \
    "ds_read_b64_tr_b16 v[74:75], v19 offset:13312\n" \
    "s_add_u32 s60, s60, s72\n" \
    "s_cmp_eq_u32 s60, s98\n" \
    "s_cselect_b32 s60, s99, s60\n" \
    "v_mfma_f32_32x32x16_bf16 v[160:175], v[56:59], v[80:83], v[160:175]\n" \
    "ds_read_b64_tr_b16 v[76:77], v18 offset:13824\n" \
    "ds_read_b64_tr_b16 v[78:79], v19 offset:13824\n" \
    "v_mfma_f32_32x32x16_bf16 v[224:239], v[56:59], v[84:87], v[224:239]\n" \
    "s_add_u32 s62, s62, s58\n" \
    "s_cmp_eq_u32 s62, 163840\n" \
    "s_cselect_b32 s62, 16384, s62\n" \
    "v_mfma_f32_32x32x16_bf16 v[176:191], v[60:63], v[80:83], v[176:191]\n" \
    "v_add_u32 v14, s64, v14\n" \
    "v_add_u32 v15, s64, v15\n" \
    "v_add_u32 v18, s64, v18\n" \
    "v_add_u32 v19, s64, v19\n" \
    "v_mfma_f32_32x32x16_bf16 v[240:255], v[60:63], v[84:87], v[240:255]\n" \
    "s_sub_u32 s63, s63, 1\n" \
    "s_cmp_lg_u32 s63, 0\n" \
    "s_waitcnt vmcnt(14)\n" \
    "s_waitcnt lgkmcnt(0)\n" \
    "s_barrier\n" \
    "s_cbranch_scc1 Lmha_mqp\n" \
    "v_mfma_f32_32x32x16_bf16 v[128:143], v[64:67], v[88:91], v[128:143]\n" \
    "ds_read_b128 v[80:83], v14 offset:0\n" \
    "ds_read_b64_tr_b16 v[48:49], v18 offset:0\n" \
    "v_mfma_f32_32x32x16_bf16 v[192:207], v[64:67], v[92:95], v[192:207]\n" \
    "ds_read_b64_tr_b16 v[50:51], v19 offset:0\n" \
    "ds_read_b128 v[84:87], v14 offset:4096\n" \
    "v_mfma_f32_32x32x16_bf16 v[144:159], v[68:71], v[88:91], v[144:159]\n" \
    "ds_read_b64_tr_b16 v[52:53], v18 offset:512\n" \
    "ds_read_b64_tr_b16 v[54:55], v19 offset:512\n" \
    "v_mfma_f32_32x32x16_bf16 v[208:223], v[68:71], v[92:95], v[208:223]\n" \
    "ds_read_b64_tr_b16 v[56:57], v18 offset:1024\n" \
    "ds_read_b64_tr_b16 v[58:59], v19 offset:1024\n" \
    "v_mfma_f32_32x32x16_bf16 v[160:175], v[72:75], v[88:91], v[160:175]\n" \
    "ds_read_b64_tr_b16 v[60:61], v18 offset:1536\n" \
    "ds_read_b64_tr_b16 v[62:63], v19 offset:1536\n" \
    "v_mfma_f32_32x32x16_bf16 v[224:239], v[72:75], v[92:95], v[224:239]\n" \
    "v_mfma_f32_32x32x16_bf16 v[176:191], v[76:79], v[88:91], v[176:191]\n" \
    "v_mfma_f32_32x32x16_bf16 v[240:255], v[76:79], v[92:95], v[240:255]\n" \
    "s_waitcnt lgkmcnt(7)\n" \
    "v_mfma_f32_32x32x16_bf16 v[128:143], v[48:51], v[80:83], v[128:143]\n" \
    "ds_read_b128 v[88:91], v15 offset:0\n" \
    "ds_read_b64_tr_b16 v[64:65], v18 offset:4096\n" \
    "s_waitcnt lgkmcnt(8)\n" \
    "v_mfma_f32_32x32x16_bf16 v[192:207], v[48:51], v[84:87], v[192:207]\n" \
    "ds_read_b64_tr_b16 v[66:67], v19 offset:4096\n" \
    "ds_read_b128 v[92:95], v15 offset:4096\n" \
    "s_waitcnt lgkmcnt(8)\n" \
    "v_mfma_f32_32x32x16_bf16 v[144:159], v[52:55], v[80:83], v[144:159]\n" \
    "ds_read_b64_tr_b16 v[68:69], v18 offset:4608\n" \
    "ds_read_b64_tr_b16 v[70:71], v19 offset:4608\n" \
    "v_mfma_f32_32x32x16_bf16 v[208:223], v[52:55], v[84:87], v[208:223]\n" \
    "ds_read_b64_tr_b16 v[72:73], v18 offset:5120\n" \
    "ds_read_b64_tr_b16 v[74:75], v19 offset:5120\n" \
    "s_waitcnt lgkmcnt(10)\n" \
    "v_mfma_f32_32x32x16_bf16 v[160:175], v[56:59], v[80:83], v[160:175]\n" \
    "ds_read_b64_tr_b16 v[76:77], v18 offset:5632\n" \
    "ds_read_b64_tr_b16 v[78:79], v19 offset:5632\n" \
    "v_mfma_f32_32x32x16_bf16 v[224:239], v[56:59], v[84:87], v[224:239]\n" \
    "s_waitcnt lgkmcnt(10)\n" \
    "v_mfma_f32_32x32x16_bf16 v[176:191], v[60:63], v[80:83], v[176:191]\n" \
    "v_mfma_f32_32x32x16_bf16 v[240:255], v[60:63], v[84:87], v[240:255]\n" \
    "s_waitcnt lgkmcnt(7)\n" \
    "v_mfma_f32_32x32x16_bf16 v[128:143], v[64:67], v[88:91], v[128:143]\n" \
    "ds_read_b128 v[80:83], v14 offset:512\n" \
    "ds_read_b64_tr_b16 v[48:49], v18 offset:8192\n" \
    "s_waitcnt lgkmcnt(8)\n" \
    "v_mfma_f32_32x32x16_bf16 v[192:207], v[64:67], v[92:95], v[192:207]\n" \
    "ds_read_b64_tr_b16 v[50:51], v19 offset:8192\n" \
    "ds_read_b128 v[84:87], v14 offset:4608\n" \
    "s_waitcnt lgkmcnt(8)\n" \
    "v_mfma_f32_32x32x16_bf16 v[144:159], v[68:71], v[88:91], v[144:159]\n" \
    "ds_read_b64_tr_b16 v[52:53], v18 offset:8704\n" \
    "ds_read_b64_tr_b16 v[54:55], v19 offset:8704\n" \
    "v_mfma_f32_32x32x16_bf16 v[208:223], v[68:71], v[92:95], v[208:223]\n" \
    "ds_read_b64_tr_b16 v[56:57], v18 offset:9216\n" \
    "ds_read_b64_tr_b16 v[58:59], v19 offset:9216\n" \
    "s_waitcnt lgkmcnt(10)\n" \
    "v_mfma_f32_32x32x16_bf16 v[160:175], v[72:75], v[88:91], v[160:175]\n" \
    "ds_read_b64_tr_b16 v[60:61], v18 offset:9728\n" \
    "ds_read_b64_tr_b16 v[62:63], v19 offset:9728\n" \
    "v_mfma_f32_32x32x16_bf16 v[224:239], v[72:75], v[92:95], v[224:239]\n" \
    "s_waitcnt lgkmcnt(10)\n" \
    "v_mfma_f32_32x32x16_bf16 v[176:191], v[76:79], v[88:91], v[176:191]\n" \
    "v_mfma_f32_32x32x16_bf16 v[240:255], v[76:79], v[92:95], v[240:255]\n" \
    "s_add_u32 s76, s76, s58\n" \
    "s_cmp_eq_u32 s76, 147456\n" \
    "s_cselect_b32 s64, -98304, s58\n" \
    "s_cselect_b32 s76, 0, s76\n" \
    "s_waitcnt lgkmcnt(7)\n" \
    "v_mfma_f32_32x32x16_bf16 v[128:143], v[48:51], v[80:83], v[128:143]\n" \
    "ds_read_b128 v[88:91], v15 offset:512\n" \
    "ds_read_b64_tr_b16 v[64:65], v18 offset:12288\n" \
    "s_waitcnt lgkmcnt(8)\n" \
    "v_mfma_f32_32x32x16_bf16 v[192:207], v[48:51], v[84:87], v[192:207]\n" \
    "ds_read_b64_tr_b16 v[66:67], v19 offset:12288\n" \
    "ds_read_b128 v[92:95], v15 offset:4608\n" \
    "s_waitcnt lgkmcnt(8)\n" \
    "v_mfma_f32_32x32x16_bf16 v[144:159], v[52:55], v[80:83], v[144:159]\n" \
    "ds_read_b64_tr_b16 v[68:69], v18 offset:12800\n" \
    "ds_read_b64_tr_b16 v[70:71], v19 offset:12800\n" \
    "v_mfma_f32_32x32x16_bf16 v[208:223], v[52:55], v[84:87], v[208:223]\n" \
    "ds_read_b64_tr_b16 v[72:73], v18 offset:13312\n" \
    "ds_read_b64_tr_b16 v[74:75], v19 offset:13312\n" \
    "s_waitcnt lgkmcnt(8)\n" \
    "v_mfma_f32_32x32x16_bf16 v[160:175], v[56:59], v[80:83], v[160:175]\n" \
    "ds_read_b64_tr_b16 v[76:77], v18 offset:13824\n" \
    "ds_read_b64_tr_b16 v[78:79], v19 offset:13824\n" \
    "v_mfma_f32_32x32x16_bf16 v[224:239], v[56:59], v[84:87], v[224:239]\n" \
    "v_mfma_f32_32x32x16_bf16 v[176:191], v[60:63], v[80:83], v[176:191]\n" \
    "v_add_u32 v14, s64, v14\n" \
    "v_add_u32 v15, s64, v15\n" \
    "v_add_u32 v18, s64, v18\n" \
    "v_add_u32 v19, s64, v19\n" \
    "v_mfma_f32_32x32x16_bf16 v[240:255], v[60:63], v[84:87], v[240:255]\n" \
    "s_waitcnt vmcnt(0)\n" \
    "s_waitcnt lgkmcnt(0)\n" \
    "s_barrier\n" \
    "v_mfma_f32_32x32x16_bf16 v[128:143], v[64:67], v[88:91], v[128:143]\n" \
    "ds_read_b128 v[96:99], v14 offset:0\n" \
    "ds_read_b64_tr_b16 v[48:49], v18 offset:0\n" \
    "ds_read_b64_tr_b16 v[50:51], v19 offset:0\n" \
    "v_mfma_f32_32x32x16_bf16 v[192:207], v[64:67], v[92:95], v[192:207]\n" \
    "ds_read_b128 v[104:107], v15 offset:0\n" \
    "ds_read_b64_tr_b16 v[52:53], v18 offset:4096\n" \
    "ds_read_b64_tr_b16 v[54:55], v19 offset:4096\n" \
    "v_mfma_f32_32x32x16_bf16 v[144:159], v[68:71], v[88:91], v[144:159]\n" \
    "ds_read_b128 v[112:115], v14 offset:512\n" \
    "ds_read_b64_tr_b16 v[56:57], v18 offset:8192\n" \
    "ds_read_b64_tr_b16 v[58:59], v19 offset:8192\n" \
    "v_mfma_f32_32x32x16_bf16 v[208:223], v[68:71], v[92:95], v[208:223]\n" \
    "ds_read_b128 v[120:123], v15 offset:512\n" \
    "ds_read_b64_tr_b16 v[60:61], v18 offset:12288\n" \
    "ds_read_b64_tr_b16 v[62:63], v19 offset:12288\n" \
    "v_mfma_f32_32x32x16_bf16 v[160:175], v[72:75], v[88:91], v[160:175]\n" \
    "ds_read_b128 v[100:103], v14 offset:4096\n" \
    "ds_read_b128 v[108:111], v15 offset:4096\n" \
    "v_mfma_f32_32x32x16_bf16 v[224:239], v[72:75], v[92:95], v[224:239]\n" \
    "ds_read_b128 v[116:119], v14 offset:4608\n" \
    "ds_read_b128 v[124:127], v15 offset:4608\n" \
    "v_mfma_f32_32x32x16_bf16 v[176:191], v[76:79], v[88:91], v[176:191]\n" \
    "v_mfma_f32_32x32x16_bf16 v[240:255], v[76:79], v[92:95], v[240:255]\n" \
    "s_waitcnt lgkmcnt(13)\n" \
    "v_mfma_f32_32x32x16_bf16 v[128:143], v[48:51], v[96:99], v[128:143]\n" \
    "ds_read_b64_tr_b16 v[64:65], v18 offset:512\n" \
    "ds_read_b64_tr_b16 v[66:67], v19 offset:512\n" \
    "ds_read_b64_tr_b16 v[68:69], v18 offset:4608\n" \
    "s_waitcnt lgkmcnt(13)\n" \
    "v_mfma_f32_32x32x16_bf16 v[128:143], v[52:55], v[104:107], v[128:143]\n" \
    "ds_read_b64_tr_b16 v[70:71], v19 offset:4608\n" \
    "ds_read_b64_tr_b16 v[72:73], v18 offset:8704\n" \
    "ds_read_b64_tr_b16 v[74:75], v19 offset:8704\n" \
    "s_waitcnt lgkmcnt(13)\n" \
    "v_mfma_f32_32x32x16_bf16 v[128:143], v[56:59], v[112:115], v[128:143]\n" \
    "ds_read_b64_tr_b16 v[76:77], v18 offset:12800\n" \
    "ds_read_b64_tr_b16 v[78:79], v19 offset:12800\n" \
    "s_waitcnt lgkmcnt(12)\n" \
    "v_mfma_f32_32x32x16_bf16 v[128:143], v[60:63], v[120:123], v[128:143]\n" \
    "v_readfirstlane_b32 s22, v10\n" \
    "v_readfirstlane_b32 s23, v11\n" \
    "v_mov_b64 v[38:39], 0\n" \
    "s_add_u32 s22, s22, 0x7f4a7c15\n" \
    "s_addc_u32 s23, s23, 0x9e3779b9\n" \
    "s_waitcnt lgkmcnt(8)\n" \
    "v_mfma_f32_32x32x16_bf16 v[192:207], v[48:51], v[100:103], v[192:207]\n" \
    "v_mfma_f32_32x32x16_bf16 v[192:207], v[52:55], v[108:111], v[192:207]\n" \
    "s_nop 4\n" \
    "v_mul_f32 v128, s70, v128\n" \
    "v_mul_f32 v129, s70, v129\n" \
    "v_mul_f32 v130, s70, v130\n" \
    "v_mul_f32 v131, s70, v131\n" \
    "v_mul_f32 v132, s70, v132\n" \
    "v_mul_f32 v133, s70, v133\n" \
    "v_mul_f32 v134, s70, v134\n" \
    "v_mfma_f32_32x32x16_bf16 v[192:207], v[56:59], v[116:119], v[192:207]\n" \
    "v_mul_f32 v135, s70, v135\n" \
    "v_cvt_pk_bf16_f32 v40, v128, v129\n" \
    "v_cvt_pk_bf16_f32 v41, v130, v131\n" \
    "v_cvt_pk_bf16_f32 v42, v132, v133\n" \
    "v_cvt_pk_bf16_f32 v43, v134, v135\n" \
    "v_accvgpr_write_b32 a0, v40\n" \
    "v_accvgpr_write_b32 a1, v41\n" \
    "v_mfma_f32_32x32x16_bf16 v[192:207], v[60:63], v[124:127], v[192:207]\n" \
    "v_accvgpr_write_b32 a2, v42\n" \
    "v_accvgpr_write_b32 a3, v43\n" \
    "v_mul_f32 v136, s70, v136\n" \
    "v_mul_f32 v137, s70, v137\n" \
    "v_mul_f32 v138, s70, v138\n" \
    "v_mul_f32 v139, s70, v139\n" \
    "v_mul_f32 v140, s70, v140\n" \
    "s_waitcnt lgkmcnt(0)\n" \
    "v_mfma_f32_32x32x16_bf16 v[144:159], v[64:67], v[96:99], v[144:159]\n" \
    "ds_read_b64_tr_b16 v[48:49], v18 offset:1024\n" \
    "ds_read_b64_tr_b16 v[50:51], v19 offset:1024\n" \
    "v_mul_f32 v141, s70, v141\n" \
    "v_mul_f32 v142, s70, v142\n" \
    "v_mul_f32 v143, s70, v143\n" \
    "v_cvt_pk_bf16_f32 v44, v136, v137\n" \
    "v_cvt_pk_bf16_f32 v45, v138, v139\n" \
    "v_cvt_pk_bf16_f32 v46, v140, v141\n" \
    "v_cvt_pk_bf16_f32 v47, v142, v143\n" \
    "v_mfma_f32_32x32x16_bf16 v[144:159], v[68:71], v[104:107], v[144:159]\n" \
    "ds_read_b64_tr_b16 v[52:53], v18 offset:5120\n" \
    "ds_read_b64_tr_b16 v[54:55], v19 offset:5120\n" \
    "v_accvgpr_write_b32 a4, v44\n" \
    "v_accvgpr_write_b32 a5, v45\n" \
    "v_accvgpr_write_b32 a6, v46\n" \
    "v_accvgpr_write_b32 a7, v47\n" \
    "v_mul_f32 v192, s70, v192\n" \
    "v_mul_f32 v193, s70, v193\n" \
    "v_mul_f32 v194, s70, v194\n" \
    "v_mfma_f32_32x32x16_bf16 v[144:159], v[72:75], v[112:115], v[144:159]\n" \
    "ds_read_b64_tr_b16 v[56:57], v18 offset:9216\n" \
    "ds_read_b64_tr_b16 v[58:59], v19 offset:9216\n" \
    "v_mul_f32 v195, s70, v195\n" \
    "v_mul_f32 v196, s70, v196\n" \
    "v_mul_f32 v197, s70, v197\n" \
    "v_mul_f32 v198, s70, v198\n" \
    "v_mul_f32 v199, s70, v199\n" \
    "v_cvt_pk_bf16_f32 v40, v192, v193\n" \
    "v_cvt_pk_bf16_f32 v41, v194, v195\n" \
    "v_mfma_f32_32x32x16_bf16 v[144:159], v[76:79], v[120:123], v[144:159]\n" \
    "ds_read_b64_tr_b16 v[60:61], v18 offset:13312\n" \
    "ds_read_b64_tr_b16 v[62:63], v19 offset:13312\n" \
    "v_cvt_pk_bf16_f32 v42, v196, v197\n" \
    "v_cvt_pk_bf16_f32 v43, v198, v199\n" \
    "v_accvgpr_write_b32 a32, v40\n" \
    "v_accvgpr_write_b32 a33, v41\n" \
    "v_accvgpr_write_b32 a34, v42\n" \
    "v_accvgpr_write_b32 a35, v43\n" \
    "v_mul_f32 v200, s70, v200\n" \
    "v_mfma_f32_32x32x16_bf16 v[208:223], v[64:67], v[100:103], v[208:223]\n" \
    "v_mul_f32 v201, s70, v201\n" \
    "v_mul_f32 v202, s70, v202\n" \
    "v_mul_f32 v203, s70, v203\n" \
    "v_mul_f32 v204, s70, v204\n" \
    "v_mul_f32 v205, s70, v205\n" \
    "v_mul_f32 v206, s70, v206\n" \
    "v_mul_f32 v207, s70, v207\n" \
    "v_mfma_f32_32x32x16_bf16 v[208:223], v[68:71], v[108:111], v[208:223]\n" \
    "v_cvt_pk_bf16_f32 v44, v200, v201\n" \
    "v_cvt_pk_bf16_f32 v45, v202, v203\n" \
    "v_cvt_pk_bf16_f32 v46, v204, v205\n" \
    "v_cvt_pk_bf16_f32 v47, v206, v207\n" \
    "v_accvgpr_write_b32 a36, v44\n" \
    "v_accvgpr_write_b32 a37, v45\n" \
    "v_accvgpr_write_b32 a38, v46\n" \
    "v_mfma_f32_32x32x16_bf16 v[208:223], v[72:75], v[116:119], v[208:223]\n" \
    "v_accvgpr_write_b32 a39, v47\n" \
    "v_mul_f32 v144, s70, v144\n" \
    "v_mul_f32 v145, s70, v145\n" \
    "v_mul_f32 v146, s70, v146\n" \
    "v_mul_f32 v147, s70, v147\n" \
    "v_mul_f32 v148, s70, v148\n" \
    "v_mul_f32 v149, s70, v149\n" \
    "v_mfma_f32_32x32x16_bf16 v[208:223], v[76:79], v[124:127], v[208:223]\n" \
    "v_mul_f32 v150, s70, v150\n" \
    "v_mul_f32 v151, s70, v151\n" \
    "v_cvt_pk_bf16_f32 v40, v144, v145\n" \
    "v_cvt_pk_bf16_f32 v41, v146, v147\n" \
    "v_cvt_pk_bf16_f32 v42, v148, v149\n" \
    "v_cvt_pk_bf16_f32 v43, v150, v151\n" \
    "v_accvgpr_write_b32 a8, v40\n" \
    "s_waitcnt lgkmcnt(0)\n" \
    "v_mfma_f32_32x32x16_bf16 v[160:175], v[48:51], v[96:99], v[160:175]\n" \
    "ds_read_b64_tr_b16 v[64:65], v18 offset:1536\n" \
    "ds_read_b64_tr_b16 v[66:67], v19 offset:1536\n" \
    "v_accvgpr_write_b32 a9, v41\n" \
    "v_accvgpr_write_b32 a10, v42\n" \
    "v_accvgpr_write_b32 a11, v43\n" \
    "v_mul_f32 v152, s70, v152\n" \
    "v_mul_f32 v153, s70, v153\n" \
    "v_mul_f32 v154, s70, v154\n" \
    "v_mul_f32 v155, s70, v155\n" \
    "v_mfma_f32_32x32x16_bf16 v[160:175], v[52:55], v[104:107], v[160:175]\n" \
    "ds_read_b64_tr_b16 v[68:69], v18 offset:5632\n" \
    "ds_read_b64_tr_b16 v[70:71], v19 offset:5632\n" \
    "v_mul_f32 v156, s70, v156\n" \
    "v_mul_f32 v157, s70, v157\n" \
    "v_mul_f32 v158, s70, v158\n" \
    "v_mul_f32 v159, s70, v159\n" \
    "v_cvt_pk_bf16_f32 v44, v152, v153\n" \
    "v_cvt_pk_bf16_f32 v45, v154, v155\n" \
    "v_cvt_pk_bf16_f32 v46, v156, v157\n" \
    "v_mfma_f32_32x32x16_bf16 v[160:175], v[56:59], v[112:115], v[160:175]\n" \
    "ds_read_b64_tr_b16 v[72:73], v18 offset:9728\n" \
    "ds_read_b64_tr_b16 v[74:75], v19 offset:9728\n" \
    "v_cvt_pk_bf16_f32 v47, v158, v159\n" \
    "v_accvgpr_write_b32 a12, v44\n" \
    "v_accvgpr_write_b32 a13, v45\n" \
    "v_accvgpr_write_b32 a14, v46\n" \
    "v_accvgpr_write_b32 a15, v47\n" \
    "v_mul_f32 v208, s70, v208\n" \
    "v_mul_f32 v209, s70, v209\n" \
    "v_mfma_f32_32x32x16_bf16 v[160:175], v[60:63], v[120:123], v[160:175]\n" \
    "ds_read_b64_tr_b16 v[76:77], v18 offset:13824\n" \
    "ds_read_b64_tr_b16 v[78:79], v19 offset:13824\n" \
    "v_mul_f32 v210, s70, v210\n" \
    "v_mul_f32 v211, s70, v211\n" \
    "v_mul_f32 v212, s70, v212\n" \
    "v_mul_f32 v213, s70, v213\n" \
    "v_mul_f32 v214, s70, v214\n" \
    "v_mul_f32 v215, s70, v215\n" \
    "v_cvt_pk_bf16_f32 v40, v208, v209\n" \
    "v_mfma_f32_32x32x16_bf16 v[224:239], v[48:51], v[100:103], v[224:239]\n" \
    "v_cvt_pk_bf16_f32 v41, v210, v211\n" \
    "v_cvt_pk_bf16_f32 v42, v212, v213\n" \
    "v_cvt_pk_bf16_f32 v43, v214, v215\n" \
    "v_accvgpr_write_b32 a40, v40\n" \
    "v_accvgpr_write_b32 a41, v41\n" \
    "v_accvgpr_write_b32 a42, v42\n" \
    "v_accvgpr_write_b32 a43, v43\n" \
    "v_mfma_f32_32x32x16_bf16 v[224:239], v[52:55], v[108:111], v[224:239]\n" \
    "v_mul_f32 v216, s70, v216\n" \
    "v_mul_f32 v217, s70, v217\n" \
    "v_mul_f32 v218, s70, v218\n" \
    "v_mul_f32 v219, s70, v219\n" \
    "v_mul_f32 v220, s70, v220\n" \
    "v_mul_f32 v221, s70, v221\n" \
    "v_mul_f32 v222, s70, v222\n" \
    "v_mfma_f32_32x32x16_bf16 v[224:239], v[56:59], v[116:119], v[224:239]\n" \
    "v_mul_f32 v223, s70, v223\n" \
    "v_cvt_pk_bf16_f32 v44, v216, v217\n" \
    "v_cvt_pk_bf16_f32 v45, v218, v219\n" \
    "v_cvt_pk_bf16_f32 v46, v220, v221\n" \
    "v_cvt_pk_bf16_f32 v47, v222, v223\n" \
    "v_accvgpr_write_b32 a44, v44\n" \
    "v_accvgpr_write_b32 a45, v45\n" \
    "v_mfma_f32_32x32x16_bf16 v[224:239], v[60:63], v[124:127], v[224:239]\n" \
    "v_accvgpr_write_b32 a46, v46\n" \
    "v_accvgpr_write_b32 a47, v47\n" \
    "v_mul_f32 v160, s70, v160\n" \
    "v_mul_f32 v161, s70, v161\n" \
    "v_mul_f32 v162, s70, v162\n" \
    "v_mul_f32 v163, s70, v163\n" \
    "v_mul_f32 v164, s70, v164\n" \
    "s_waitcnt lgkmcnt(0)\n" \
    "s_barrier\n" \
    "v_mfma_f32_32x32x16_bf16 v[176:191], v[64:67], v[96:99], v[176:191]\n" \
    "ds_read_b128 a[64:67], v20 offset:0\n" \
    "ds_read_b128 a[68:71], v21 offset:0\n" \
    "v_mul_f32 v165, s70, v165\n" \
    "v_mul_f32 v166, s70, v166\n" \
    "v_mul_f32 v167, s70, v167\n" \
    "v_cvt_pk_bf16_f32 v40, v160, v161\n" \
    "v_cvt_pk_bf16_f32 v41, v162, v163\n" \
    "v_cvt_pk_bf16_f32 v42, v164, v165\n" \
    "v_cvt_pk_bf16_f32 v43, v166, v167\n" \
    "v_mfma_f32_32x32x16_bf16 v[176:191], v[68:71], v[104:107], v[176:191]\n" \
    "ds_read_b128 a[72:75], v20 offset:256\n" \
    "ds_read_b128 a[76:79], v21 offset:256\n" \
    "v_accvgpr_write_b32 a16, v40\n" \
    "v_accvgpr_write_b32 a17, v41\n" \
    "v_accvgpr_write_b32 a18, v42\n" \
    "v_accvgpr_write_b32 a19, v43\n" \
    "v_mul_f32 v168, s70, v168\n" \
    "v_mul_f32 v169, s70, v169\n" \
    "v_mul_f32 v170, s70, v170\n" \
    "v_mfma_f32_32x32x16_bf16 v[176:191], v[72:75], v[112:115], v[176:191]\n" \
    "ds_read_b128 a[80:83], v20 offset:512\n" \
    "ds_read_b128 a[84:87], v21 offset:512\n" \
    "v_mul_f32 v171, s70, v171\n" \
    "v_mul_f32 v172, s70, v172\n" \
    "v_mul_f32 v173, s70, v173\n" \
    "v_mul_f32 v174, s70, v174\n" \
    "v_mul_f32 v175, s70, v175\n" \
    "v_cvt_pk_bf16_f32 v44, v168, v169\n" \
    "v_cvt_pk_bf16_f32 v45, v170, v171\n" \
    "v_mfma_f32_32x32x16_bf16 v[176:191], v[76:79], v[120:123], v[176:191]\n" \
    "ds_read_b128 a[88:91], v20 offset:768\n" \
    "ds_read_b128 a[92:95], v21 offset:768\n" \
    "v_cvt_pk_bf16_f32 v46, v172, v173\n" \
    "v_cvt_pk_bf16_f32 v47, v174, v175\n" \
    "v_accvgpr_write_b32 a20, v44\n" \
    "v_accvgpr_write_b32 a21, v45\n" \
    "v_accvgpr_write_b32 a22, v46\n" \
    "v_accvgpr_write_b32 a23, v47\n" \
    "v_mul_f32 v224, s70, v224\n" \
    "v_mfma_f32_32x32x16_bf16 v[240:255], v[64:67], v[100:103], v[240:255]\n" \
    "v_mul_f32 v225, s70, v225\n" \
    "v_mul_f32 v226, s70, v226\n" \
    "v_mul_f32 v227, s70, v227\n" \
    "v_mul_f32 v228, s70, v228\n" \
    "v_mul_f32 v229, s70, v229\n" \
    "v_mul_f32 v230, s70, v230\n" \
    "v_mul_f32 v231, s70, v231\n" \
    "v_mfma_f32_32x32x16_bf16 v[240:255], v[68:71], v[108:111], v[240:255]\n" \
    "v_cvt_pk_bf16_f32 v40, v224, v225\n" \
    "v_cvt_pk_bf16_f32 v41, v226, v227\n" \
    "v_cvt_pk_bf16_f32 v42, v228, v229\n" \
    "v_cvt_pk_bf16_f32 v43, v230, v231\n" \
    "v_accvgpr_write_b32 a48, v40\n" \
    "v_accvgpr_write_b32 a49, v41\n" \
    "v_accvgpr_write_b32 a50, v42\n" \
    "v_mfma_f32_32x32x16_bf16 v[240:255], v[72:75], v[116:119], v[240:255]\n" \
    "v_accvgpr_write_b32 a51, v43\n" \
    "v_mul_f32 v232, s70, v232\n" \
    "v_mul_f32 v233, s70, v233\n" \
    "v_mul_f32 v234, s70, v234\n" \
    "v_mul_f32 v235, s70, v235\n" \
    "v_mul_f32 v236, s70, v236\n" \
    "v_mul_f32 v237, s70, v237\n" \
    "v_mfma_f32_32x32x16_bf16 v[240:255], v[76:79], v[124:127], v[240:255]\n" \
    "v_mul_f32 v238, s70, v238\n" \
    "v_mul_f32 v239, s70, v239\n" \
    "v_cvt_pk_bf16_f32 v44, v232, v233\n" \
    "v_cvt_pk_bf16_f32 v45, v234, v235\n" \
    "v_cvt_pk_bf16_f32 v46, v236, v237\n" \
    "v_cvt_pk_bf16_f32 v47, v238, v239\n" \
    "v_accvgpr_write_b32 a52, v44\n" \
    "s_waitcnt lgkmcnt(0)\n" \
    "v_mfma_f32_32x32x16_bf16 v[96:111], a[64:67], a[0:3], 0\n" \
    "v_accvgpr_write_b32 a53, v45\n" \
    "v_accvgpr_write_b32 a54, v46\n" \
    "v_accvgpr_write_b32 a55, v47\n" \
    "v_mul_f32 v176, s70, v176\n" \
    "v_mul_f32 v177, s70, v177\n" \
    "v_mul_f32 v178, s70, v178\n" \
    "v_mul_f32 v179, s70, v179\n" \
    "s_add_u32 s71, s71, s59\n" \
    "s_and_b32 s71, s71, 131071\n" \
    "s_cselect_b32 s67, s59, -114688\n" \
    "v_mfma_f32_32x32x16_bf16 v[96:111], a[68:71], a[4:7], v[96:111]\n" \
    "v_mul_f32 v180, s70, v180\n" \
    "v_mul_f32 v181, s70, v181\n" \
    "v_mul_f32 v182, s70, v182\n" \
    "v_mul_f32 v183, s70, v183\n" \
    "v_cvt_pk_bf16_f32 v40, v176, v177\n" \
    "v_cvt_pk_bf16_f32 v41, v178, v179\n" \
    "v_cvt_pk_bf16_f32 v42, v180, v181\n" \
    "ds_read_b64_tr_b16 a[96:97], v22 offset:0\n" \
    "ds_read_b64_tr_b16 a[98:99], v23 offset:0\n" \
    "v_mfma_f32_32x32x16_bf16 v[96:111], a[72:75], a[8:11], v[96:111]\n" \
    "v_cvt_pk_bf16_f32 v43, v182, v183\n" \
    "v_accvgpr_write_b32 a24, v40\n" \
    "v_accvgpr_write_b32 a25, v41\n" \
    "v_accvgpr_write_b32 a26, v42\n" \
    "v_accvgpr_write_b32 a27, v43\n" \
    "v_mul_f32 v184, s70, v184\n" \
    "v_mul_f32 v185, s70, v185\n" \
    "ds_read_b64_tr_b16 a[100:101], v22 offset:256\n" \
    "ds_read_b64_tr_b16 a[102:103], v23 offset:256\n" \
    "v_mfma_f32_32x32x16_bf16 v[96:111], a[76:79], a[12:15], v[96:111]\n" \
    "v_mul_f32 v186, s70, v186\n" \
    "v_mul_f32 v187, s70, v187\n" \
    "v_mul_f32 v188, s70, v188\n" \
    "v_mul_f32 v189, s70, v189\n" \
    "v_mul_f32 v190, s70, v190\n" \
    "v_mul_f32 v191, s70, v191\n" \
    "v_cvt_pk_bf16_f32 v44, v184, v185\n" \
    "ds_read_b64_tr_b16 a[104:105], v22 offset:512\n" \
    "ds_read_b64_tr_b16 a[106:107], v23 offset:512\n" \
    "v_mfma_f32_32x32x16_bf16 v[96:111], a[80:83], a[16:19], v[96:111]\n" \
    "v_cvt_pk_bf16_f32 v45, v186, v187\n" \
    "v_cvt_pk_bf16_f32 v46, v188, v189\n" \
    "v_cvt_pk_bf16_f32 v47, v190, v191\n" \
    "v_accvgpr_write_b32 a28, v44\n" \
    "v_accvgpr_write_b32 a29, v45\n" \
    "v_accvgpr_write_b32 a30, v46\n" \
    "v_accvgpr_write_b32 a31, v47\n" \
    "ds_read_b64_tr_b16 a[108:109], v22 offset:768\n" \
    "ds_read_b64_tr_b16 a[110:111], v23 offset:768\n" \
    "v_mfma_f32_32x32x16_bf16 v[96:111], a[84:87], a[20:23], v[96:111]\n" \
    "v_mul_f32 v240, s70, v240\n" \
    "v_mul_f32 v241, s70, v241\n" \
    "v_mul_f32 v242, s70, v242\n" \
    "v_mul_f32 v243, s70, v243\n" \
    "v_mul_f32 v244, s70, v244\n" \
    "v_mul_f32 v245, s70, v245\n" \
    "v_mul_f32 v246, s70, v246\n" \
    "ds_read_b64_tr_b16 a[112:113], v22 offset:4096\n" \
    "ds_read_b64_tr_b16 a[114:115], v23 offset:4096\n" \
    "v_mfma_f32_32x32x16_bf16 v[96:111], a[88:91], a[24:27], v[96:111]\n" \
    "v_mul_f32 v247, s70, v247\n" \
    "v_cvt_pk_bf16_f32 v40, v240, v241\n" \
    "v_cvt_pk_bf16_f32 v41, v242, v243\n" \
    "v_cvt_pk_bf16_f32 v42, v244, v245\n" \
    "v_cvt_pk_bf16_f32 v43, v246, v247\n" \
    "v_accvgpr_write_b32 a56, v40\n" \
    "ds_read_b64_tr_b16 a[116:117], v22 offset:4352\n" \
    "ds_read_b64_tr_b16 a[118:119], v23 offset:4352\n" \
    "v_mfma_f32_32x32x16_bf16 v[96:111], a[92:95], a[28:31], v[96:111]\n" \
    "v_accvgpr_write_b32 a57, v41\n" \
    "v_accvgpr_write_b32 a58, v42\n" \
    "v_accvgpr_write_b32 a59, v43\n" \
    "v_mul_f32 v248, s70, v248\n" \
    "v_mul_f32 v249, s70, v249\n" \
    "v_mul_f32 v250, s70, v250\n" \
    "v_mul_f32 v251, s70, v251\n" \
    "ds_read_b64_tr_b16 a[120:121], v22 offset:4608\n" \
    "ds_read_b64_tr_b16 a[122:123], v23 offset:4608\n" \
    "v_mfma_f32_32x32x16_bf16 v[112:127], a[64:67], a[32:35], 0\n" \
    "v_mul_f32 v252, s70, v252\n" \
    "v_mul_f32 v253, s70, v253\n" \
    "v_mul_f32 v254, s70, v254\n" \
    "v_mul_f32 v255, s70, v255\n" \
    "v_cvt_pk_bf16_f32 v44, v248, v249\n" \
    "v_cvt_pk_bf16_f32 v45, v250, v251\n" \
    "ds_read_b64_tr_b16 a[124:125], v22 offset:4864\n" \
    "ds_read_b64_tr_b16 a[126:127], v23 offset:4864\n" \
    "v_mfma_f32_32x32x16_bf16 v[112:127], a[68:71], a[36:39], v[112:127]\n" \
    "v_cvt_pk_bf16_f32 v46, v252, v253\n" \
    "v_cvt_pk_bf16_f32 v47, v254, v255\n" \
    "v_accvgpr_write_b32 a60, v44\n" \
    "v_accvgpr_write_b32 a61, v45\n" \
    "v_accvgpr_write_b32 a62, v46\n" \
    "v_accvgpr_write_b32 a63, v47\n" \
    "v_add_u32 v20, s67, v20\n" \
    "v_add_u32 v21, s67, v21\n" \
    "v_mfma_f32_32x32x16_bf16 v[112:127], a[72:75], a[40:43], v[112:127]\n" \
    "v_max3_f32 v46, v96, v97, v98\n" \
    "v_max3_f32 v47, v99, v100, v101\n" \
    "v_max3_f32 v46, v46, v102, v103\n" \
    "v_max3_f32 v47, v47, v104, v105\n" \
    "v_max3_f32 v46, v46, v106, v107\n" \
    "v_max3_f32 v47, v47, v108, v109\n" \
    "v_max3_f32 v46, v46, v110, v111\n" \
    "v_max_f32 v46, v46, v47\n" \
    "v_mfma_f32_32x32x16_bf16 v[112:127], a[76:79], a[44:47], v[112:127]\n" \
    "v_mov_b32 v47, v46\n" \
    "s_nop 1\n" \
    "v_permlane32_swap_b32 v46, v47\n" \
    "v_max_f32 v46, v46, v47\n" \
    "v_sub_f32 v32, 0, v46\n" \
    "v_sub_f32 v33, 0, v46\n" \
    "v_pk_add_f32 v[96:97], v[96:97], v[32:33] op_sel_hi:[1,0]\n" \
    "v_pk_add_f32 v[98:99], v[98:99], v[32:33] op_sel_hi:[1,0]\n" \
    "v_mfma_f32_32x32x16_bf16 v[112:127], a[80:83], a[48:51], v[112:127]\n" \
    "v_pk_add_f32 v[100:101], v[100:101], v[32:33] op_sel_hi:[1,0]\n" \
    "v_pk_add_f32 v[102:103], v[102:103], v[32:33] op_sel_hi:[1,0]\n" \
    "v_pk_add_f32 v[104:105], v[104:105], v[32:33] op_sel_hi:[1,0]\n" \
    "v_pk_add_f32 v[106:107], v[106:107], v[32:33] op_sel_hi:[1,0]\n" \
    "v_pk_add_f32 v[108:109], v[108:109], v[32:33] op_sel_hi:[1,0]\n" \
    "v_pk_add_f32 v[110:111], v[110:111], v[32:33] op_sel_hi:[1,0]\n" \
    "v_mov_b64 v[64:65], v[32:33]\n" \
    "v_mfma_f32_32x32x16_bf16 v[112:127], a[84:87], a[52:55], v[112:127]\n" \
    "v_mov_b64 v[66:67], v[32:33]\n" \
    "v_mov_b64 v[68:69], v[32:33]\n" \
    "v_mov_b64 v[70:71], v[32:33]\n" \
    "v_mov_b64 v[72:73], v[32:33]\n" \
    "v_mov_b64 v[74:75], v[32:33]\n" \
    "v_mov_b64 v[76:77], v[32:33]\n" \
    "v_mov_b64 v[78:79], v[32:33]\n" \
    "v_mfma_f32_32x32x16_bf16 v[112:127], a[88:91], a[56:59], v[112:127]\n" \
    "v_exp_f32 v40, v96\n" \
    "v_exp_f32 v41, v97\n" \
    "v_exp_f32 v42, v98\n" \
    "v_exp_f32 v43, v99\n" \
    "v_add_f32 v36, v40, v41\n" \
    "v_mfma_f32_32x32x16_bf16 v[112:127], a[92:95], a[60:63], v[112:127]\n" \
    "v_cvt_pk_bf16_f32 v48, v40, v41\n" \
    "v_exp_f32 v44, v100\n" \
    "v_exp_f32 v45, v101\n" \
    "v_add_f32 v36, v36, v42\n" \
    "v_add_f32 v36, v36, v43\n" \
    "v_cvt_pk_bf16_f32 v49, v42, v43\n" \
    "v_exp_f32 v46, v102\n" \
    "v_exp_f32 v47, v103\n" \
    "v_add_f32 v36, v36, v44\n" \
    "v_add_f32 v36, v36, v45\n" \
    "v_cvt_pk_bf16_f32 v50, v44, v45\n" \
    "v_exp_f32 v40, v104\n" \
    "v_exp_f32 v41, v105\n" \
    "v_add_f32 v36, v36, v46\n" \
    "v_add_f32 v36, v36, v47\n" \
    "v_cvt_pk_bf16_f32 v51, v46, v47\n" \
    "v_exp_f32 v42, v106\n" \
    "v_exp_f32 v43, v107\n" \
    "v_add_f32 v36, v36, v40\n" \
    "v_add_f32 v36, v36, v41\n" \
    "v_cvt_pk_bf16_f32 v52, v40, v41\n" \
    "v_exp_f32 v44, v108\n" \
    "v_exp_f32 v45, v109\n" \
    "v_add_f32 v36, v36, v42\n" \
    "v_add_f32 v36, v36, v43\n" \
    "v_cvt_pk_bf16_f32 v53, v42, v43\n" \
    "v_exp_f32 v46, v110\n" \
    "v_exp_f32 v47, v111\n" \
    "v_add_f32 v36, v36, v44\n" \
    "v_add_f32 v36, v36, v45\n" \
    "v_cvt_pk_bf16_f32 v54, v44, v45\n" \
    "v_add_f32 v36, v36, v46\n" \
    "v_add_f32 v36, v36, v47\n" \
    "v_cvt_pk_bf16_f32 v55, v46, v47\n" \
    "v_cmp_lt_f32 vcc, s69, v36\n" \
    "s_cbranch_vccnz Lmha_stub_mf0\n" \
    "Lmha_back_mf0:\n" \
    "s_waitcnt lgkmcnt(0)\n" \
    "v_mfma_f32_32x32x16_bf16 v[128:143], a[96:99], v[48:51], 0\n" \
    "v_add_f32 v38, v38, v36\n" \
    "v_max3_f32 v46, v112, v113, v114\n" \
    "v_max3_f32 v47, v115, v116, v117\n" \
    "v_max3_f32 v46, v46, v118, v119\n" \
    "v_max3_f32 v47, v47, v120, v121\n" \
    "v_max3_f32 v46, v46, v122, v123\n" \
    "v_max3_f32 v47, v47, v124, v125\n" \
    "ds_read_b128 a[64:67], v20 offset:0\n" \
    "ds_read_b128 a[68:71], v21 offset:0\n" \
    "v_mfma_f32_32x32x16_bf16 v[144:159], a[100:103], v[48:51], 0\n" \
    "v_max3_f32 v46, v46, v126, v127\n" \
    "v_max_f32 v46, v46, v47\n" \
    "v_mov_b32 v47, v46\n" \
    "s_nop 1\n" \
    "v_permlane32_swap_b32 v46, v47\n" \
    "v_max_f32 v46, v46, v47\n" \
    "v_sub_f32 v32, 0, v46\n" \
    "ds_read_b128 a[72:75], v20 offset:256\n" \
    "ds_read_b128 a[76:79], v21 offset:256\n" \
    "v_mfma_f32_32x32x16_bf16 v[160:175], a[104:107], v[48:51], 0\n" \
    "v_sub_f32 v33, 0, v46\n" \
    "v_pk_add_f32 v[112:113], v[112:113], v[32:33] op_sel_hi:[1,0]\n" \
    "v_pk_add_f32 v[114:115], v[114:115], v[32:33] op_sel_hi:[1,0]\n" \
    "v_pk_add_f32 v[116:117], v[116:117], v[32:33] op_sel_hi:[1,0]\n" \
    "v_pk_add_f32 v[118:119], v[118:119], v[32:33] op_sel_hi:[1,0]\n" \
    "v_pk_add_f32 v[120:121], v[120:121], v[32:33] op_sel_hi:[1,0]\n" \
    "ds_read_b128 a[80:83], v20 offset:512\n" \
    "ds_read_b128 a[84:87], v21 offset:512\n" \
    "v_mfma_f32_32x32x16_bf16 v[176:191], a[108:111], v[48:51], 0\n" \
    "v_pk_add_f32 v[122:123], v[122:123], v[32:33] op_sel_hi:[1,0]\n" \
    "v_pk_add_f32 v[124:125], v[124:125], v[32:33] op_sel_hi:[1,0]\n" \
    "v_pk_add_f32 v[126:127], v[126:127], v[32:33] op_sel_hi:[1,0]\n" \
    "v_mov_b64 v[80:81], v[32:33]\n" \
    "v_mov_b64 v[82:83], v[32:33]\n" \
    "v_mov_b64 v[84:85], v[32:33]\n" \
    "ds_read_b128 a[88:91], v20 offset:768\n" \
    "ds_read_b128 a[92:95], v21 offset:768\n" \
    "v_mfma_f32_32x32x16_bf16 v[128:143], a[112:115], v[52:55], v[128:143]\n" \
    "v_mov_b64 v[86:87], v[32:33]\n" \
    "v_mov_b64 v[88:89], v[32:33]\n" \
    "v_mov_b64 v[90:91], v[32:33]\n" \
    "v_mov_b64 v[92:93], v[32:33]\n" \
    "v_mov_b64 v[94:95], v[32:33]\n" \
    "v_mfma_f32_32x32x16_bf16 v[144:159], a[116:119], v[52:55], v[144:159]\n" \
    "v_exp_f32 v40, v112\n" \
    "v_exp_f32 v41, v113\n" \
    "v_exp_f32 v42, v114\n" \
    "v_exp_f32 v43, v115\n" \
    "v_mfma_f32_32x32x16_bf16 v[160:175], a[120:123], v[52:55], v[160:175]\n" \
    "v_add_f32 v37, v40, v41\n" \
    "v_cvt_pk_bf16_f32 v56, v40, v41\n" \
    "v_exp_f32 v44, v116\n" \
    "v_exp_f32 v45, v117\n" \
    "v_mfma_f32_32x32x16_bf16 v[176:191], a[124:127], v[52:55], v[176:191]\n" \
    "v_add_f32 v37, v37, v42\n" \
    "v_add_f32 v37, v37, v43\n" \
    "v_cvt_pk_bf16_f32 v57, v42, v43\n" \
    "v_add_f32 v37, v37, v44\n" \
    "v_add_u32 v18, 65536, v16\n" \
    "v_add_u32 v19, 65536, v17\n" \
    "v_add_u32 v14, 131072, v16\n" \
    "v_add_u32 v15, 131072, v17\n" \
    "v_xor_b32 v1, 512, v16\n" \
    "v_xor_b32 v2, 512, v17\n" \
    "v_xor_b32 v3, 512, v18\n" \
    "v_xor_b32 v4, 512, v19\n" \
    "v_xor_b32 v10, 512, v14\n" \
    "v_xor_b32 v11, 512, v15\n" \
    "s_mov_b32 s63, 2\n" \
    "s_getpc_b64 s[98:99]\n" \
    "Lmha_jpc:\n" \
    "s_add_u32 s98, s98, Lmha_h0-Lmha_jpc\n" \
    "s_addc_u32 s99, s99, 0\n" \
    "s_waitcnt lgkmcnt(0)\n" \
    "v_mfma_f32_32x32x16_bf16 v[96:111], a[64:67], a[0:3], v[64:79]\n" \
    "v_add_f32 v37, v37, v45\n" \
    "v_cvt_pk_bf16_f32 v58, v44, v45\n" \
    "v_exp_f32 v46, v118\n" \
    "v_exp_f32 v47, v119\n" \
    "v_mfma_f32_32x32x16_bf16 v[96:111], a[68:71], a[4:7], v[96:111]\n" \
    "v_add_f32 v37, v37, v46\n" \
    "v_add_f32 v37, v37, v47\n" \
    "v_cvt_pk_bf16_f32 v59, v46, v47\n" \
    "v_exp_f32 v40, v120\n" \
    "v_mfma_f32_32x32x16_bf16 v[96:111], a[72:75], a[8:11], v[96:111]\n" \
    "v_add_f32 v37, v37, v40\n" \
    "v_exp_f32 v41, v121\n" \
    "v_exp_f32 v42, v122\n" \
    "v_mfma_f32_32x32x16_bf16 v[96:111], a[76:79], a[12:15], v[96:111]\n" \
    "v_add_f32 v37, v37, v41\n" \
    "v_cvt_pk_bf16_f32 v60, v40, v41\n" \
    "v_add_f32 v37, v37, v42\n" \
    "v_exp_f32 v43, v123\n" \
    "v_mfma_f32_32x32x16_bf16 v[96:111], a[80:83], a[16:19], v[96:111]\n" \
    "v_add_f32 v37, v37, v43\n" \
    "v_cvt_pk_bf16_f32 v61, v42, v43\n" \
    "v_exp_f32 v44, v124\n" \
    "v_exp_f32 v45, v125\n" \
    "v_mfma_f32_32x32x16_bf16 v[96:111], a[84:87], a[20:23], v[96:111]\n" \
    "v_add_f32 v37, v37, v44\n" \
    "v_add_f32 v37, v37, v45\n" \
    "v_cvt_pk_bf16_f32 v62, v44, v45\n" \
    "v_exp_f32 v46, v126\n" \
    "v_mfma_f32_32x32x16_bf16 v[96:111], a[88:91], a[24:27], v[96:111]\n" \
    "v_add_u32 v22, s67, v22\n" \
    "v_add_u32 v23, s67, v23\n" \
    "v_add_f32 v37, v37, v46\n" \
    "v_exp_f32 v47, v127\n" \
    "v_mfma_f32_32x32x16_bf16 v[96:111], a[92:95], a[28:31], v[96:111]\n" \
    "v_add_f32 v37, v37, v47\n" \
    "v_cvt_pk_bf16_f32 v63, v46, v47\n" \
    "v_cmp_lt_f32 vcc, s69, v37\n" \
    "s_cbranch_vccnz Lmha_stub_mPa\n" \
    "Lmha_back_mPa:\n" \
    "v_mfma_f32_32x32x16_bf16 v[192:207], a[96:99], v[56:59], 0\n" \
    "s_add_u32 s71, s71, s59\n" \
    "s_and_b32 s71, s71, 131071\n" \
    "s_cselect_b32 s67, s59, -114688\n" \
    "ds_read_b64_tr_b16 a[96:97], v22 offset:0\n" \
    "ds_read_b64_tr_b16 a[98:99], v23 offset:0\n" \
    "v_mfma_f32_32x32x16_bf16 v[208:223], a[100:103], v[56:59], 0\n" \
    "v_add_f32 v39, v39, v37\n" \
    "ds_read_b64_tr_b16 a[100:101], v22 offset:256\n" \
    "ds_read_b64_tr_b16 a[102:103], v23 offset:256\n" \
    "v_mfma_f32_32x32x16_bf16 v[224:239], a[104:107], v[56:59], 0\n" \
    "v_add_u32 v20, s67, v20\n" \
    "v_add_u32 v21, s67, v21\n" \
    "ds_read_b64_tr_b16 a[104:105], v22 offset:512\n" \
    "ds_read_b64_tr_b16 a[106:107], v23 offset:512\n" \
    "v_mfma_f32_32x32x16_bf16 v[240:255], a[108:111], v[56:59], 0\n" \
    "ds_read_b64_tr_b16 a[108:109], v22 offset:768\n" \
    "v_exp_f32 v40, v96\n" \
    "v_exp_f32 v41, v97\n" \
    "v_mfma_f32_32x32x16_bf16 v[192:207], a[112:115], v[60:63], v[192:207]\n" \
    "ds_read_b64_tr_b16 a[110:111], v23 offset:768\n" \
    "v_add_f32 v36, v40, v41\n" \
    "v_cvt_pk_bf16_f32 v48, v40, v41\n" \
    "v_exp_f32 v42, v98\n" \
    "v_mfma_f32_32x32x16_bf16 v[208:223], a[116:119], v[60:63], v[208:223]\n" \
    "ds_read_b64_tr_b16 a[112:113], v22 offset:4096\n" \
    "v_add_f32 v36, v36, v42\n" \
    "v_exp_f32 v43, v99\n" \
    "v_mfma_f32_32x32x16_bf16 v[224:239], a[120:123], v[60:63], v[224:239]\n" \
    "ds_read_b64_tr_b16 a[114:115], v23 offset:4096\n" \
    "v_add_f32 v36, v36, v43\n" \
    "v_cvt_pk_bf16_f32 v49, v42, v43\n" \
    "v_exp_f32 v44, v100\n" \
    "v_mfma_f32_32x32x16_bf16 v[240:255], a[124:127], v[60:63], v[240:255]\n" \
    "ds_read_b64_tr_b16 a[116:117], v22 offset:4352\n" \
    "v_add_f32 v36, v36, v44\n" \
    "v_exp_f32 v45, v101\n" \
    "v_mfma_f32_32x32x16_bf16 v[112:127], a[64:67], a[32:35], v[80:95]\n" \
    "v_add_f32 v36, v36, v45\n" \
    "v_cvt_pk_bf16_f32 v50, v44, v45\n" \
    "v_exp_f32 v46, v102\n" \
    "v_exp_f32 v47, v103\n" \
    "v_mfma_f32_32x32x16_bf16 v[112:127], a[68:71], a[36:39], v[112:127]\n" \
    "ds_read_b64_tr_b16 a[118:119], v23 offset:4352\n" \
    "v_add_f32 v36, v36, v46\n" \
    "v_add_f32 v36, v36, v47\n" \
    "v_cvt_pk_bf16_f32 v51, v46, v47\n" \
    "v_exp_f32 v40, v104\n" \
    "v_mfma_f32_32x32x16_bf16 v[112:127], a[72:75], a[40:43], v[112:127]\n" \
    "v_add_f32 v36, v36, v40\n" \
    "v_exp_f32 v41, v105\n" \
    "v_exp_f32 v42, v106\n" \
    "v_mfma_f32_32x32x16_bf16 v[112:127], a[76:79], a[44:47], v[112:127]\n" \
    "ds_read_b64_tr_b16 a[120:121], v22 offset:4608\n" \
    "v_add_f32 v36, v36, v41\n" \
    "v_cvt_pk_bf16_f32 v52, v40, v41\n" \
    "v_add_f32 v36, v36, v42\n" \
    "v_exp_f32 v43, v107\n" \
    "v_mfma_f32_32x32x16_bf16 v[112:127], a[80:83], a[48:51], v[112:127]\n" \
    "v_add_f32 v36, v36, v43\n" \
    "v_cvt_pk_bf16_f32 v53, v42, v43\n" \
    "v_exp_f32 v44, v108\n" \
    "v_exp_f32 v45, v109\n" \
    "v_mfma_f32_32x32x16_bf16 v[112:127], a[84:87], a[52:55], v[112:127]\n" \
    "ds_read_b64_tr_b16 a[122:123], v23 offset:4608\n" \
    "v_add_f32 v36, v36, v44\n" \
    "v_add_f32 v36, v36, v45\n" \
    "v_cvt_pk_bf16_f32 v54, v44, v45\n" \
    "v_exp_f32 v46, v110\n" \
    "v_mfma_f32_32x32x16_bf16 v[112:127], a[88:91], a[56:59], v[112:127]\n" \
    "ds_read_b64_tr_b16 a[124:125], v22 offset:4864\n" \
    "ds_read_b64_tr_b16 a[126:127], v23 offset:4864\n" \
    "v_add_f32 v36, v36, v46\n" \
    "v_exp_f32 v47, v111\n" \
    "v_mfma_f32_32x32x16_bf16 v[112:127], a[92:95], a[60:63], v[112:127]\n" \
    "v_add_f32 v36, v36, v47\n" \
    "v_cvt_pk_bf16_f32 v55, v46, v47\n" \
    "v_cmp_lt_f32 vcc, s69, v36\n" \
    "s_cbranch_vccnz Lmha_stub_mPb\n" \
    "Lmha_back_mPb:\n" \
    "s_waitcnt lgkmcnt(3)\n" \
    "v_mfma_f32_32x32x16_bf16 v[128:143], a[96:99], v[48:51], v[128:143]\n" \
    "v_add_f32 v38, v38, v36\n" \
    "ds_read_b128 a[64:67], v20 offset:0\n" \
    "ds_read_b128 a[68:71], v21 offset:0\n" \
    "ds_read_b128 a[72:75], v20 offset:256\n" \
    "v_mfma_f32_32x32x16_bf16 v[144:159], a[100:103], v[48:51], v[144:159]\n" \
    "ds_read_b128 a[76:79], v21 offset:256\n" \
    "ds_read_b128 a[80:83], v20 offset:512\n" \
    "ds_read_b128 a[84:87], v21 offset:512\n" \
    "v_mfma_f32_32x32x16_bf16 v[160:175], a[104:107], v[48:51], v[160:175]\n" \
    "s_waitcnt lgkmcnt(6)\n" \
    "s_barrier\n" \
    "v_mfma_f32_32x32x16_bf16 v[176:191], a[108:111], v[48:51], v[176:191]\n" \
    "ds_read_b128 a[88:91], v20 offset:768\n" \
    "v_exp_f32 v40, v112\n" \
    "v_exp_f32 v41, v113\n" \
    "v_mfma_f32_32x32x16_bf16 v[128:143], a[112:115], v[52:55], v[128:143]\n" \
    "v_add_f32 v37, v40, v41\n" \
    "v_cvt_pk_bf16_f32 v56, v40, v41\n" \
    "v_exp_f32 v42, v114\n" \
    "v_mfma_f32_32x32x16_bf16 v[144:159], a[116:119], v[52:55], v[144:159]\n" \
    "ds_read_b128 a[92:95], v21 offset:768\n" \
    "v_add_f32 v37, v37, v42\n" \
    "v_exp_f32 v43, v115\n" \
    "v_mfma_f32_32x32x16_bf16 v[160:175], a[120:123], v[52:55], v[160:175]\n" \
    "v_add_f32 v37, v37, v43\n" \
    "v_cvt_pk_bf16_f32 v57, v42, v43\n" \
    "v_exp_f32 v44, v116\n" \
    "v_mfma_f32_32x32x16_bf16 v[176:191], a[124:127], v[52:55], v[176:191]\n" \
    "v_add_f32 v37, v37, v44\n" \
    "v_exp_f32 v45, v117\n" \
    ".p2align 6\n" \
    "s_nop 0\n" \
    "Lmha_u1:\n" \
    "s_waitcnt lgkmcnt(2)\n" \
    "v_mfma_f32_32x32x16_bf16 v[96:111], a[64:67], a[0:3], v[64:79]\n" \
    "v_add_f32 v37, v37, v45\n" \
    "v_cvt_pk_bf16_f32 v58, v44, v45\n" \
    "v_exp_f32 v46, v118\n" \
    "v_exp_f32 v47, v119\n" \
    "v_mfma_f32_32x32x16_bf16 v[96:111], a[68:71], a[4:7], v[96:111]\n" \
    "v_add_f32 v37, v37, v46\n" \
    "v_add_f32 v37, v37, v47\n" \
    "v_cvt_pk_bf16_f32 v59, v46, v47\n" \
    "v_exp_f32 v40, v120\n" \
    "v_mfma_f32_32x32x16_bf16 v[96:111], a[72:75], a[8:11], v[96:111]\n" \
    "v_add_f32 v37, v37, v40\n" \
    "v_exp_f32 v41, v121\n" \
    "v_exp_f32 v42, v122\n" \
    "v_mfma_f32_32x32x16_bf16 v[96:111], a[76:79], a[12:15], v[96:111]\n" \
    "v_add_f32 v37, v37, v41\n" \
    "v_cvt_pk_bf16_f32 v60, v40, v41\n" \
    "v_add_f32 v37, v37, v42\n" \
    "v_exp_f32 v43, v123\n" \
    "v_mfma_f32_32x32x16_bf16 v[96:111], a[80:83], a[16:19], v[96:111]\n" \
    "v_add_f32 v37, v37, v43\n" \
    "v_cvt_pk_bf16_f32 v61, v42, v43\n" \
    "v_exp_f32 v44, v124\n" \
    "v_exp_f32 v45, v125\n" \
    "v_mfma_f32_32x32x16_bf16 v[96:111], a[84:87], a[20:23], v[96:111]\n" \
    "v_add_f32 v37, v37, v44\n" \
    "v_add_f32 v37, v37, v45\n" \
    "v_cvt_pk_bf16_f32 v62, v44, v45\n" \
    "v_exp_f32 v46, v126\n" \
    "s_waitcnt lgkmcnt(0)\n" \
    "v_mfma_f32_32x32x16_bf16 v[96:111], a[88:91], a[24:27], v[96:111]\n" \
    "v_add_u32 v22, s67, v22\n" \
    "v_add_u32 v23, s67, v23\n" \
    "v_add_f32 v37, v37, v46\n" \
    "v_exp_f32 v47, v127\n" \
    "s_waitcnt lgkmcnt(0)\n" \
    "v_mfma_f32_32x32x16_bf16 v[96:111], a[92:95], a[28:31], v[96:111]\n" \
    "v_add_f32 v37, v37, v47\n" \
    "v_cvt_pk_bf16_f32 v63, v46, v47\n" \
    "v_cmp_lt_f32 vcc, s69, v37\n" \
    "s_cbranch_vccnz Lmha_stub_mAa\n" \
    "Lmha_back_mAa:\n" \
    "Lmha_rest:\n" \
    "v_mfma_f32_32x32x16_bf16 v[192:207], a[96:99], v[56:59], v[192:207]\n" \
    "s_add_u32 s71, s71, s59\n" \
    "s_and_b32 s71, s71, 131071\n" \
    "s_cselect_b32 s67, s59, -114688\n" \
    "ds_read_b64_tr_b16 a[96:97], v22 offset:0\n" \
    "ds_read_b64_tr_b16 a[98:99], v23 offset:0\n" \
    "v_mfma_f32_32x32x16_bf16 v[208:223], a[100:103], v[56:59], v[208:223]\n" \
    "v_add_f32 v39, v39, v37\n" \
    "ds_read_b64_tr_b16 a[100:101], v22 offset:256\n" \
    "ds_read_b64_tr_b16 a[102:103], v23 offset:256\n" \
    "v_mfma_f32_32x32x16_bf16 v[224:239], a[104:107], v[56:59], v[224:239]\n" \
    "v_add_u32 v20, s67, v20\n" \
    "v_add_u32 v21, s67, v21\n" \
    "ds_read_b64_tr_b16 a[104:105], v22 offset:512\n" \
    "ds_read_b64_tr_b16 a[106:107], v23 offset:512\n" \
    "v_mfma_f32_32x32x16_bf16 v[240:255], a[108:111], v[56:59], v[240:255]\n" \
    "ds_read_b64_tr_b16 a[108:109], v22 offset:768\n" \
    "v_exp_f32 v40, v96\n" \
    "v_exp_f32 v41, v97\n" \
    "v_mfma_f32_32x32x16_bf16 v[192:207], a[112:115], v[60:63], v[192:207]\n" \
    "ds_read_b64_tr_b16 a[110:111], v23 offset:768\n" \
    "v_add_f32 v36, v40, v41\n" \
    "v_cvt_pk_bf16_f32 v48, v40, v41\n" \
    "v_exp_f32 v42, v98\n" \
    "v_mfma_f32_32x32x16_bf16 v[208:223], a[116:119], v[60:63], v[208:223]\n" \
    "ds_read_b64_tr_b16 a[112:113], v22 offset:4096\n" \
    "v_add_f32 v36, v36, v42\n" \
    "v_exp_f32 v43, v99\n" \
    "v_mfma_f32_32x32x16_bf16 v[224:239], a[120:123], v[60:63], v[224:239]\n" \
    "ds_read_b64_tr_b16 a[114:115], v23 offset:4096\n" \
    "v_add_f32 v36, v36, v43\n" \
    "v_cvt_pk_bf16_f32 v49, v42, v43\n" \
    "v_exp_f32 v44, v100\n" \
    "v_mfma_f32_32x32x16_bf16 v[240:255], a[124:127], v[60:63], v[240:255]\n" \
    "ds_read_b64_tr_b16 a[116:117], v22 offset:4352\n" \
    "v_add_f32 v36, v36, v44\n" \
    "v_exp_f32 v45, v101\n" \
    "v_mfma_f32_32x32x16_bf16 v[112:127], a[64:67], a[32:35], v[80:95]\n" \
    "v_add_f32 v36, v36, v45\n" \
    "v_cvt_pk_bf16_f32 v50, v44, v45\n" \
    "v_exp_f32 v46, v102\n" \
    "v_exp_f32 v47, v103\n" \
    "v_mfma_f32_32x32x16_bf16 v[112:127], a[68:71], a[36:39], v[112:127]\n" \
    "ds_read_b64_tr_b16 a[118:119], v23 offset:4352\n" \
    "v_add_f32 v36, v36, v46\n" \
    "v_add_f32 v36, v36, v47\n" \
    "v_cvt_pk_bf16_f32 v51, v46, v47\n" \
    "v_exp_f32 v40, v104\n" \
    "v_mfma_f32_32x32x16_bf16 v[112:127], a[72:75], a[40:43], v[112:127]\n" \
    "v_add_f32 v36, v36, v40\n" \
    "v_exp_f32 v41, v105\n" \
    "v_exp_f32 v42, v106\n" \
    "v_mfma_f32_32x32x16_bf16 v[112:127], a[76:79], a[44:47], v[112:127]\n" \
    "ds_read_b64_tr_b16 a[120:121], v22 offset:4608\n" \
    "v_add_f32 v36, v36, v41\n" \
    "v_cvt_pk_bf16_f32 v52, v40, v41\n" \
    "v_add_f32 v36, v36, v42\n" \
    "v_exp_f32 v43, v107\n" \
    "v_mfma_f32_32x32x16_bf16 v[112:127], a[80:83], a[48:51], v[112:127]\n" \
    "v_add_f32 v36, v36, v43\n" \
    "v_cvt_pk_bf16_f32 v53, v42, v43\n" \
    "v_exp_f32 v44, v108\n" \
    "v_exp_f32 v45, v109\n" \
    "v_mfma_f32_32x32x16_bf16 v[112:127], a[84:87], a[52:55], v[112:127]\n" \
    "ds_read_b64_tr_b16 a[122:123], v23 offset:4608\n" \
    "v_add_f32 v36, v36, v44\n" \
    "v_add_f32 v36, v36, v45\n" \
    "v_cvt_pk_bf16_f32 v54, v44, v45\n" \
    "v_exp_f32 v46, v110\n" \
    "v_mfma_f32_32x32x16_bf16 v[112:127], a[88:91], a[56:59], v[112:127]\n" \
    "ds_read_b64_tr_b16 a[124:125], v22 offset:4864\n" \
    "ds_read_b64_tr_b16 a[126:127], v23 offset:4864\n" \
    "v_add_f32 v36, v36, v46\n" \
    "v_exp_f32 v47, v111\n" \
    "v_mfma_f32_32x32x16_bf16 v[112:127], a[92:95], a[60:63], v[112:127]\n" \
    "v_add_f32 v36, v36, v47\n" \
    "v_cvt_pk_bf16_f32 v55, v46, v47\n" \
    "v_cmp_lt_f32 vcc, s69, v36\n" \
    "s_cbranch_vccnz Lmha_stub_mAb\n" \
    "Lmha_back_mAb:\n" \
    "s_waitcnt lgkmcnt(3)\n" \
    "v_mfma_f32_32x32x16_bf16 v[128:143], a[96:99], v[48:51], v[128:143]\n" \
    "v_add_f32 v38, v38, v36\n" \
    "ds_read_b128 a[64:67], v20 offset:0\n" \
    "ds_read_b128 a[68:71], v21 offset:0\n" \
    "ds_read_b128 a[72:75], v20 offset:256\n" \
    "v_mfma_f32_32x32x16_bf16 v[144:159], a[100:103], v[48:51], v[144:159]\n" \
    "ds_read_b128 a[76:79], v21 offset:256\n" \
    "ds_read_b128 a[80:83], v20 offset:512\n" \
    "ds_read_b128 a[84:87], v21 offset:512\n" \
    "s_add_u32 s63, s63, 1\n" \
    "v_mfma_f32_32x32x16_bf16 v[160:175], a[104:107], v[48:51], v[160:175]\n" \
    "s_waitcnt lgkmcnt(6)\n" \
    "s_barrier\n" \
    "v_mfma_f32_32x32x16_bf16 v[176:191], a[108:111], v[48:51], v[176:191]\n" \
    "ds_read_b128 a[88:91], v20 offset:768\n" \
    "v_exp_f32 v40, v112\n" \
    "v_exp_f32 v41, v113\n" \
    "v_mfma_f32_32x32x16_bf16 v[128:143], a[112:115], v[52:55], v[128:143]\n" \
    "v_add_f32 v37, v40, v41\n" \
    "v_cvt_pk_bf16_f32 v56, v40, v41\n" \
    "v_exp_f32 v42, v114\n" \
    "v_mfma_f32_32x32x16_bf16 v[144:159], a[116:119], v[52:55], v[144:159]\n" \
    "ds_read_b128 a[92:95], v21 offset:768\n" \
    "v_add_f32 v37, v37, v42\n" \
    "v_exp_f32 v43, v115\n" \
    "v_mfma_f32_32x32x16_bf16 v[160:175], a[120:123], v[52:55], v[160:175]\n" \
    "s_bitcmp1_b32 0x4920, s63\n" \
    "v_add_f32 v37, v37, v43\n" \
    "v_cvt_pk_bf16_f32 v57, v42, v43\n" \
    "v_exp_f32 v44, v116\n" \
    "v_mfma_f32_32x32x16_bf16 v[176:191], a[124:127], v[52:55], v[176:191]\n" \
    "v_add_f32 v37, v37, v44\n" \
    "v_exp_f32 v45, v117\n" \
    "s_cbranch_scc1 Lmha_u1\n" \
    "s_setpc_b64 s[98:99]\n" \
    "Lmha_h0:\n" \
    "s_add_u32 s98, s98, Lmha_h1-Lmha_h0\n" \
    "s_addc_u32 s99, s99, 0\n" \
    "s_waitcnt lgkmcnt(1)\n" \
    "v_mfma_f32_32x32x16_bf16 v[96:111], a[64:67], a[0:3], v[64:79]\n" \
    "ds_read_b64_tr_b16 a[128:129], v14 offset:0\n" \
    "v_add_f32 v37, v37, v45\n" \
    "v_cvt_pk_bf16_f32 v58, v44, v45\n" \
    "v_exp_f32 v46, v118\n" \
    "v_exp_f32 v47, v119\n" \
    "v_mfma_f32_32x32x16_bf16 v[96:111], a[68:71], a[4:7], v[96:111]\n" \
    "ds_read_b64_tr_b16 a[130:131], v15 offset:0\n" \
    "v_add_f32 v37, v37, v46\n" \
    "v_add_f32 v37, v37, v47\n" \
    "v_cvt_pk_bf16_f32 v59, v46, v47\n" \
    "v_exp_f32 v40, v120\n" \
    "v_mfma_f32_32x32x16_bf16 v[96:111], a[72:75], a[8:11], v[96:111]\n" \
    "ds_read_b64_tr_b16 a[132:133], v10 offset:0\n" \
    "v_add_f32 v37, v37, v40\n" \
    "v_exp_f32 v41, v121\n" \
    "v_exp_f32 v42, v122\n" \
    "v_mfma_f32_32x32x16_bf16 v[96:111], a[76:79], a[12:15], v[96:111]\n" \
    "ds_read_b64_tr_b16 a[134:135], v11 offset:0\n" \
    "v_add_f32 v37, v37, v41\n" \
    "v_cvt_pk_bf16_f32 v60, v40, v41\n" \
    "v_add_f32 v37, v37, v42\n" \
    "v_exp_f32 v43, v123\n" \
    "v_mfma_f32_32x32x16_bf16 v[96:111], a[80:83], a[16:19], v[96:111]\n" \
    "ds_read_b64_tr_b16 a[136:137], v14 offset:2048\n" \
    "v_add_f32 v37, v37, v43\n" \
    "v_cvt_pk_bf16_f32 v61, v42, v43\n" \
    "v_exp_f32 v44, v124\n" \
    "v_exp_f32 v45, v125\n" \
    "v_mfma_f32_32x32x16_bf16 v[96:111], a[84:87], a[20:23], v[96:111]\n" \
    "ds_read_b64_tr_b16 a[138:139], v15 offset:2048\n" \
    "v_add_f32 v37, v37, v44\n" \
    "v_add_f32 v37, v37, v45\n" \
    "v_cvt_pk_bf16_f32 v62, v44, v45\n" \
    "v_exp_f32 v46, v126\n" \
    "v_mfma_f32_32x32x16_bf16 v[96:111], a[88:91], a[24:27], v[96:111]\n" \
    "ds_read_b64_tr_b16 a[140:141], v10 offset:2048\n" \
    "v_add_u32 v22, s67, v22\n" \
    "v_add_u32 v23, s67, v23\n" \
    "v_add_f32 v37, v37, v46\n" \
    "v_exp_f32 v47, v127\n" \
    "s_waitcnt lgkmcnt(3)\n" \
    "v_mfma_f32_32x32x16_bf16 v[96:111], a[92:95], a[28:31], v[96:111]\n" \
    "ds_read_b64_tr_b16 a[142:143], v11 offset:2048\n" \
    "v_add_f32 v37, v37, v47\n" \
    "v_cvt_pk_bf16_f32 v63, v46, v47\n" \
    "v_cmp_lt_f32 vcc, s69, v37\n" \
    "s_cbranch_vccnz Lmha_stub_mh0\n" \
    "Lmha_back_mh0:\n" \
    "s_branch Lmha_rest\n" \
    "Lmha_h1:\n" \
    "s_add_u32 s98, s98, Lmha_h2-Lmha_h1\n" \
    "s_addc_u32 s99, s99, 0\n" \
    "s_waitcnt lgkmcnt(1)\n" \
    "v_mfma_f32_32x32x16_bf16 v[96:111], a[64:67], a[0:3], v[64:79]\n" \
    "ds_read_b64_tr_b16 a[144:145], v14 offset:16384\n" \
    "v_add_f32 v37, v37, v45\n" \
    "v_cvt_pk_bf16_f32 v58, v44, v45\n" \
    "v_exp_f32 v46, v118\n" \
    "v_exp_f32 v47, v119\n" \
    "v_mfma_f32_32x32x16_bf16 v[96:111], a[68:71], a[4:7], v[96:111]\n" \
    "ds_read_b64_tr_b16 a[146:147], v15 offset:16384\n" \
    "v_add_f32 v37, v37, v46\n" \
    "v_add_f32 v37, v37, v47\n" \
    "v_cvt_pk_bf16_f32 v59, v46, v47\n" \
    "v_exp_f32 v40, v120\n" \
    "v_mfma_f32_32x32x16_bf16 v[96:111], a[72:75], a[8:11], v[96:111]\n" \
    "ds_read_b64_tr_b16 a[148:149], v10 offset:16384\n" \
    "v_add_f32 v37, v37, v40\n" \
    "v_exp_f32 v41, v121\n" \
    "v_exp_f32 v42, v122\n" \
    "v_mfma_f32_32x32x16_bf16 v[96:111], a[76:79], a[12:15], v[96:111]\n" \
    "ds_read_b64_tr_b16 a[150:151], v11 offset:16384\n" \
    "v_add_f32 v37, v37, v41\n" \
    "v_cvt_pk_bf16_f32 v60, v40, v41\n" \
    "v_add_f32 v37, v37, v42\n" \
    "v_exp_f32 v43, v123\n" \
    "v_mfma_f32_32x32x16_bf16 v[96:111], a[80:83], a[16:19], v[96:111]\n" \
    "ds_read_b64_tr_b16 a[152:153], v14 offset:18432\n" \
    "v_add_f32 v37, v37, v43\n" \
    "v_cvt_pk_bf16_f32 v61, v42, v43\n" \
    "v_exp_f32 v44, v124\n" \
    "v_exp_f32 v45, v125\n" \
    "v_mfma_f32_32x32x16_bf16 v[96:111], a[84:87], a[20:23], v[96:111]\n" \
    "ds_read_b64_tr_b16 a[154:155], v15 offset:18432\n" \
    "v_add_f32 v37, v37, v44\n" \
    "v_add_f32 v37, v37, v45\n" \
    "v_cvt_pk_bf16_f32 v62, v44, v45\n" \
    "v_exp_f32 v46, v126\n" \
    "v_mfma_f32_32x32x16_bf16 v[96:111], a[88:91], a[24:27], v[96:111]\n" \
    "ds_read_b64_tr_b16 a[156:157], v10 offset:18432\n" \
    "v_add_u32 v22, s67, v22\n" \
    "v_add_u32 v23, s67, v23\n" \
    "v_add_f32 v37, v37, v46\n" \
    "v_exp_f32 v47, v127\n" \
    "s_waitcnt lgkmcnt(3)\n" \
    "v_mfma_f32_32x32x16_bf16 v[96:111], a[92:95], a[28:31], v[96:111]\n" \
    "ds_read_b64_tr_b16 a[158:159], v11 offset:18432\n" \
    "v_add_f32 v37, v37, v47\n" \
    "v_cvt_pk_bf16_f32 v63, v46, v47\n" \
    "v_cmp_lt_f32 vcc, s69, v37\n" \
    "s_cbranch_vccnz Lmha_stub_mh1\n" \
    "Lmha_back_mh1:\n" \
    "s_branch Lmha_rest\n" \
    "Lmha_h2:\n" \
    "s_add_u32 s98, s98, Lmha_h3-Lmha_h2\n" \
    "s_addc_u32 s99, s99, 0\n" \
    "s_waitcnt lgkmcnt(1)\n" \
    "v_mfma_f32_32x32x16_bf16 v[96:111], a[64:67], a[0:3], v[64:79]\n" \
    "ds_read_b64_tr_b16 a[160:161], v14 offset:0\n" \
    "v_add_f32 v37, v37, v45\n" \
    "v_cvt_pk_bf16_f32 v58, v44, v45\n" \
    "v_exp_f32 v46, v118\n" \
    "v_exp_f32 v47, v119\n" \
    "v_mfma_f32_32x32x16_bf16 v[96:111], a[68:71], a[4:7], v[96:111]\n" \
    "ds_read_b64_tr_b16 a[162:163], v15 offset:0\n" \
    "v_add_f32 v37, v37, v46\n" \
    "v_add_f32 v37, v37, v47\n" \
    "v_cvt_pk_bf16_f32 v59, v46, v47\n" \
    "v_exp_f32 v40, v120\n" \
    "v_mfma_f32_32x32x16_bf16 v[96:111], a[72:75], a[8:11], v[96:111]\n" \
    "ds_read_b64_tr_b16 a[164:165], v10 offset:0\n" \
    "v_add_f32 v37, v37, v40\n" \
    "v_exp_f32 v41, v121\n" \
    "v_exp_f32 v42, v122\n" \
    "v_mfma_f32_32x32x16_bf16 v[96:111], a[76:79], a[12:15], v[96:111]\n" \
    "ds_read_b64_tr_b16 a[166:167], v11 offset:0\n" \
    "v_add_f32 v37, v37, v41\n" \
    "v_cvt_pk_bf16_f32 v60, v40, v41\n" \
    "v_add_f32 v37, v37, v42\n" \
    "v_exp_f32 v43, v123\n" \
    "v_mfma_f32_32x32x16_bf16 v[96:111], a[80:83], a[16:19], v[96:111]\n" \
    "ds_read_b64_tr_b16 a[168:169], v14 offset:2048\n" \
    "v_add_f32 v37, v37, v43\n" \
    "v_cvt_pk_bf16_f32 v61, v42, v43\n" \
    "v_exp_f32 v44, v124\n" \
    "v_exp_f32 v45, v125\n" \
    "v_mfma_f32_32x32x16_bf16 v[96:111], a[84:87], a[20:23], v[96:111]\n" \
    "ds_read_b64_tr_b16 a[170:171], v15 offset:2048\n" \
    "v_add_f32 v37, v37, v44\n" \
    "v_add_f32 v37, v37, v45\n" \
    "v_cvt_pk_bf16_f32 v62, v44, v45\n" \
    "v_exp_f32 v46, v126\n" \
    "v_mfma_f32_32x32x16_bf16 v[96:111], a[88:91], a[24:27], v[96:111]\n" \
    "ds_read_b64_tr_b16 a[172:173], v10 offset:2048\n" \
    "v_add_u32 v22, s67, v22\n" \
    "v_add_u32 v23, s67, v23\n" \
    "v_add_f32 v37, v37, v46\n" \
    "v_exp_f32 v47, v127\n" \
    "s_waitcnt lgkmcnt(3)\n" \
    "v_mfma_f32_32x32x16_bf16 v[96:111], a[92:95], a[28:31], v[96:111]\n" \
    "ds_read_b64_tr_b16 a[174:175], v11 offset:2048\n" \
    "v_add_f32 v37, v37, v47\n" \
    "v_cvt_pk_bf16_f32 v63, v46, v47\n" \
    "v_cmp_lt_f32 vcc, s69, v37\n" \
    "s_cbranch_vccnz Lmha_stub_mh2\n" \
    "Lmha_back_mh2:\n" \
    "s_branch Lmha_rest\n" \
    "Lmha_h3:\n" \
    "s_add_u32 s98, s98, Lmha_h4-Lmha_h3\n" \
    "s_addc_u32 s99, s99, 0\n" \
    "s_waitcnt lgkmcnt(1)\n" \
    "v_mfma_f32_32x32x16_bf16 v[96:111], a[64:67], a[0:3], v[64:79]\n" \
    "ds_read_b64_tr_b16 a[176:177], v14 offset:16384\n" \
    "v_add_f32 v37, v37, v45\n" \
    "v_cvt_pk_bf16_f32 v58, v44, v45\n" \
    "v_exp_f32 v46, v118\n" \
    "v_exp_f32 v47, v119\n" \
    "v_mfma_f32_32x32x16_bf16 v[96:111], a[68:71], a[4:7], v[96:111]\n" \
    "ds_read_b64_tr_b16 a[178:179], v15 offset:16384\n" \
    "v_add_f32 v37, v37, v46\n" \
    "v_add_f32 v37, v37, v47\n" \
    "v_cvt_pk_bf16_f32 v59, v46, v47\n" \
    "v_exp_f32 v40, v120\n" \
    "v_mfma_f32_32x32x16_bf16 v[96:111], a[72:75], a[8:11], v[96:111]\n" \
    "ds_read_b64_tr_b16 a[180:181], v10 offset:16384\n" \
    "v_add_f32 v37, v37, v40\n" \
    "v_exp_f32 v41, v121\n" \
    "v_exp_f32 v42, v122\n" \
    "v_mfma_f32_32x32x16_bf16 v[96:111], a[76:79], a[12:15], v[96:111]\n" \
    "ds_read_b64_tr_b16 a[182:183], v11 offset:16384\n" \
    "v_add_f32 v37, v37, v41\n" \
    "v_cvt_pk_bf16_f32 v60, v40, v41\n" \
    "v_add_f32 v37, v37, v42\n" \
    "v_exp_f32 v43, v123\n" \
    "v_mfma_f32_32x32x16_bf16 v[96:111], a[80:83], a[16:19], v[96:111]\n" \
    "ds_read_b64_tr_b16 a[184:185], v14 offset:18432\n" \
    "v_add_f32 v37, v37, v43\n" \
    "v_cvt_pk_bf16_f32 v61, v42, v43\n" \
    "v_exp_f32 v44, v124\n" \
    "v_exp_f32 v45, v125\n" \
    "v_mfma_f32_32x32x16_bf16 v[96:111], a[84:87], a[20:23], v[96:111]\n" \
    "ds_read_b64_tr_b16 a[186:187], v15 offset:18432\n" \
    "v_add_f32 v37, v37, v44\n" \
    "v_add_f32 v37, v37, v45\n" \
    "v_cvt_pk_bf16_f32 v62, v44, v45\n" \
    "v_exp_f32 v46, v126\n" \
    "v_mfma_f32_32x32x16_bf16 v[96:111], a[88:91], a[24:27], v[96:111]\n" \
    "ds_read_b64_tr_b16 a[188:189], v10 offset:18432\n" \
    "v_add_u32 v22, s67, v22\n" \
    "v_add_u32 v23, s67, v23\n" \
    "v_add_f32 v37, v37, v46\n" \
    "v_exp_f32 v47, v127\n" \
    "s_waitcnt lgkmcnt(3)\n" \
    "v_mfma_f32_32x32x16_bf16 v[96:111], a[92:95], a[28:31], v[96:111]\n" \
    "ds_read_b64_tr_b16 a[190:191], v11 offset:18432\n" \
    "v_add_f32 v37, v37, v47\n" \
    "v_cvt_pk_bf16_f32 v63, v46, v47\n" \
    "v_cmp_lt_f32 vcc, s69, v37\n" \
    "s_cbranch_vccnz Lmha_stub_mh3\n" \
    "Lmha_back_mh3:\n" \
    "s_branch Lmha_rest\n" \
    "Lmha_h4:\n" \
    "s_add_u32 s98, s98, Lmha_h5-Lmha_h4\n" \
    "s_addc_u32 s99, s99, 0\n" \
    "s_waitcnt lgkmcnt(1)\n" \
    "v_mfma_f32_32x32x16_bf16 v[96:111], a[64:67], a[0:3], v[64:79]\n" \
    "ds_read_b64_tr_b16 a[192:193], v14 offset:0\n" \
    "v_add_f32 v37, v37, v45\n" \
    "v_cvt_pk_bf16_f32 v58, v44, v45\n" \
    "v_exp_f32 v46, v118\n" \
    "v_exp_f32 v47, v119\n" \
    "v_mfma_f32_32x32x16_bf16 v[96:111], a[68:71], a[4:7], v[96:111]\n" \
    "ds_read_b64_tr_b16 a[194:195], v15 offset:0\n" \
    "v_add_f32 v37, v37, v46\n" \
    "v_add_f32 v37, v37, v47\n" \
    "v_cvt_pk_bf16_f32 v59, v46, v47\n" \
    "v_exp_f32 v40, v120\n" \
    "v_mfma_f32_32x32x16_bf16 v[96:111], a[72:75], a[8:11], v[96:111]\n" \
    "ds_read_b64_tr_b16 a[196:197], v10 offset:0\n" \
    "v_add_f32 v37, v37, v40\n" \
    "v_exp_f32 v41, v121\n" \
    "v_exp_f32 v42, v122\n" \
    "v_mfma_f32_32x32x16_bf16 v[96:111], a[76:79], a[12:15], v[96:111]\n" \
    "ds_read_b64_tr_b16 a[198:199], v11 offset:0\n" \
    "v_add_f32 v37, v37, v41\n" \
    "v_cvt_pk_bf16_f32 v60, v40, v41\n" \
    "v_add_f32 v37, v37, v42\n" \
    "v_exp_f32 v43, v123\n" \
    "v_mfma_f32_32x32x16_bf16 v[96:111], a[80:83], a[16:19], v[96:111]\n" \
    "ds_read_b64_tr_b16 a[200:201], v14 offset:2048\n" \
    "v_add_f32 v37, v37, v43\n" \
    "v_cvt_pk_bf16_f32 v61, v42, v43\n" \
    "v_exp_f32 v44, v124\n" \
    "v_exp_f32 v45, v125\n" \
    "v_mfma_f32_32x32x16_bf16 v[96:111], a[84:87], a[20:23], v[96:111]\n" \
    "ds_read_b64_tr_b16 a[202:203], v15 offset:2048\n" \
    "v_add_f32 v37, v37, v44\n" \
    "v_add_f32 v37, v37, v45\n" \
    "v_cvt_pk_bf16_f32 v62, v44, v45\n" \
    "v_exp_f32 v46, v126\n" \
    "v_mfma_f32_32x32x16_bf16 v[96:111], a[88:91], a[24:27], v[96:111]\n" \
    "ds_read_b64_tr_b16 a[204:205], v10 offset:2048\n" \
    "v_add_u32 v22, s67, v22\n" \
    "v_add_u32 v23, s67, v23\n" \
    "v_add_f32 v37, v37, v46\n" \
    "v_exp_f32 v47, v127\n" \
    "s_waitcnt lgkmcnt(3)\n" \
    "v_mfma_f32_32x32x16_bf16 v[96:111], a[92:95], a[28:31], v[96:111]\n" \
    "ds_read_b64_tr_b16 a[206:207], v11 offset:2048\n" \
    "v_add_f32 v37, v37, v47\n" \
    "v_cvt_pk_bf16_f32 v63, v46, v47\n" \
    "v_cmp_lt_f32 vcc, s69, v37\n" \
    "s_cbranch_vccnz Lmha_stub_mh4\n" \
    "Lmha_back_mh4:\n" \
    "s_branch Lmha_rest\n" \
    "Lmha_h5:\n" \
    "s_add_u32 s98, s98, Lmha_h6-Lmha_h5\n" \
    "s_addc_u32 s99, s99, 0\n" \
    "s_waitcnt lgkmcnt(1)\n" \
    "v_mfma_f32_32x32x16_bf16 v[96:111], a[64:67], a[0:3], v[64:79]\n" \
    "ds_read_b64_tr_b16 a[208:209], v14 offset:16384\n" \
    "v_add_f32 v37, v37, v45\n" \
    "v_cvt_pk_bf16_f32 v58, v44, v45\n" \
    "v_exp_f32 v46, v118\n" \
    "v_exp_f32 v47, v119\n" \
    "v_mfma_f32_32x32x16_bf16 v[96:111], a[68:71], a[4:7], v[96:111]\n" \
    "ds_read_b64_tr_b16 a[210:211], v15 offset:16384\n" \
    "v_add_f32 v37, v37, v46\n" \
    "v_add_f32 v37, v37, v47\n" \
    "v_cvt_pk_bf16_f32 v59, v46, v47\n" \
    "v_exp_f32 v40, v120\n" \
    "v_mfma_f32_32x32x16_bf16 v[96:111], a[72:75], a[8:11], v[96:111]\n" \
    "ds_read_b64_tr_b16 a[212:213], v10 offset:16384\n" \
    "v_add_f32 v37, v37, v40\n" \
    "v_exp_f32 v41, v121\n" \
    "v_exp_f32 v42, v122\n" \
    "v_mfma_f32_32x32x16_bf16 v[96:111], a[76:79], a[12:15], v[96:111]\n" \
    "ds_read_b64_tr_b16 a[214:215], v11 offset:16384\n" \
    "v_add_f32 v37, v37, v41\n" \
    "v_cvt_pk_bf16_f32 v60, v40, v41\n" \
    "v_add_f32 v37, v37, v42\n" \
    "v_exp_f32 v43, v123\n" \
    "v_mfma_f32_32x32x16_bf16 v[96:111], a[80:83], a[16:19], v[96:111]\n" \
    "ds_read_b64_tr_b16 a[216:217], v14 offset:18432\n" \
    "v_add_f32 v37, v37, v43\n" \
    "v_cvt_pk_bf16_f32 v61, v42, v43\n" \
    "v_exp_f32 v44, v124\n" \
    "v_exp_f32 v45, v125\n" \
    "v_mfma_f32_32x32x16_bf16 v[96:111], a[84:87], a[20:23], v[96:111]\n" \
    "ds_read_b64_tr_b16 a[218:219], v15 offset:18432\n" \
    "v_add_f32 v37, v37, v44\n" \
    "v_add_f32 v37, v37, v45\n" \
    "v_cvt_pk_bf16_f32 v62, v44, v45\n" \
    "v_exp_f32 v46, v126\n" \
    "v_mfma_f32_32x32x16_bf16 v[96:111], a[88:91], a[24:27], v[96:111]\n" \
    "ds_read_b64_tr_b16 a[220:221], v10 offset:18432\n" \
    "v_add_u32 v22, s67, v22\n" \
    "v_add_u32 v23, s67, v23\n" \
    "v_add_f32 v37, v37, v46\n" \
    "v_exp_f32 v47, v127\n" \
    "s_waitcnt lgkmcnt(3)\n" \
    "v_mfma_f32_32x32x16_bf16 v[96:111], a[92:95], a[28:31], v[96:111]\n" \
    "ds_read_b64_tr_b16 a[222:223], v11 offset:18432\n" \
    "v_add_f32 v37, v37, v47\n" \
    "v_cvt_pk_bf16_f32 v63, v46, v47\n" \
    "v_cmp_lt_f32 vcc, s69, v37\n" \
    "s_cbranch_vccnz Lmha_stub_mh5\n" \
    "Lmha_back_mh5:\n" \
    "s_branch Lmha_rest\n" \
    "Lmha_h6:\n" \
    "s_add_u32 s98, s98, Lmha_h7-Lmha_h6\n" \
    "s_addc_u32 s99, s99, 0\n" \
    "s_waitcnt lgkmcnt(1)\n" \
    "v_mfma_f32_32x32x16_bf16 v[96:111], a[64:67], a[0:3], v[64:79]\n" \
    "ds_read_b64_tr_b16 a[224:225], v14 offset:0\n" \
    "v_add_f32 v37, v37, v45\n" \
    "v_cvt_pk_bf16_f32 v58, v44, v45\n" \
    "v_exp_f32 v46, v118\n" \
    "v_exp_f32 v47, v119\n" \
    "v_mfma_f32_32x32x16_bf16 v[96:111], a[68:71], a[4:7], v[96:111]\n" \
    "ds_read_b64_tr_b16 a[226:227], v15 offset:0\n" \
    "v_add_f32 v37, v37, v46\n" \
    "v_add_f32 v37, v37, v47\n" \
    "v_cvt_pk_bf16_f32 v59, v46, v47\n" \
    "v_exp_f32 v40, v120\n" \
    "v_mfma_f32_32x32x16_bf16 v[96:111], a[72:75], a[8:11], v[96:111]\n" \
    "ds_read_b64_tr_b16 a[228:229], v10 offset:0\n" \
    "v_add_f32 v37, v37, v40\n" \
    "v_exp_f32 v41, v121\n" \
    "v_exp_f32 v42, v122\n" \
    "v_mfma_f32_32x32x16_bf16 v[96:111], a[76:79], a[12:15], v[96:111]\n" \
    "ds_read_b64_tr_b16 a[230:231], v11 offset:0\n" \
    "v_add_f32 v37, v37, v41\n" \
    "v_cvt_pk_bf16_f32 v60, v40, v41\n" \
    "v_add_f32 v37, v37, v42\n" \
    "v_exp_f32 v43, v123\n" \
    "v_mfma_f32_32x32x16_bf16 v[96:111], a[80:83], a[16:19], v[96:111]\n" \
    "ds_read_b64_tr_b16 a[232:233], v14 offset:2048\n" \
    "v_add_f32 v37, v37, v43\n" \
    "v_cvt_pk_bf16_f32 v61, v42, v43\n" \
    "v_exp_f32 v44, v124\n" \
    "v_exp_f32 v45, v125\n" \
    "v_mfma_f32_32x32x16_bf16 v[96:111], a[84:87], a[20:23], v[96:111]\n" \
    "ds_read_b64_tr_b16 a[234:235], v15 offset:2048\n" \
    "v_add_f32 v37, v37, v44\n" \
    "v_add_f32 v37, v37, v45\n" \
    "v_cvt_pk_bf16_f32 v62, v44, v45\n" \
    "v_exp_f32 v46, v126\n" \
    "v_mfma_f32_32x32x16_bf16 v[96:111], a[88:91], a[24:27], v[96:111]\n" \
    "ds_read_b64_tr_b16 a[236:237], v10 offset:2048\n" \
    "v_add_u32 v22, s67, v22\n" \
    "v_add_u32 v23, s67, v23\n" \
    "v_add_f32 v37, v37, v46\n" \
    "v_exp_f32 v47, v127\n" \
    "s_waitcnt lgkmcnt(3)\n" \
    "v_mfma_f32_32x32x16_bf16 v[96:111], a[92:95], a[28:31], v[96:111]\n" \
    "ds_read_b64_tr_b16 a[238:239], v11 offset:2048\n" \
    "v_add_f32 v37, v37, v47\n" \
    "v_cvt_pk_bf16_f32 v63, v46, v47\n" \
    "v_cmp_lt_f32 vcc, s69, v37\n" \
    "s_cbranch_vccnz Lmha_stub_mh6\n" \
    "Lmha_back_mh6:\n" \
    "s_branch Lmha_rest\n" \
    "Lmha_h7:\n" \
    "s_add_u32 s98, s98, Lmha_h8-Lmha_h7\n" \
    "s_addc_u32 s99, s99, 0\n" \
    "s_waitcnt lgkmcnt(1)\n" \
    "v_mfma_f32_32x32x16_bf16 v[96:111], a[64:67], a[0:3], v[64:79]\n" \
    "ds_read_b64_tr_b16 a[240:241], v14 offset:16384\n" \
    "v_add_f32 v37, v37, v45\n" \
    "v_cvt_pk_bf16_f32 v58, v44, v45\n" \
    "v_exp_f32 v46, v118\n" \
    "v_exp_f32 v47, v119\n" \
    "v_mfma_f32_32x32x16_bf16 v[96:111], a[68:71], a[4:7], v[96:111]\n" \
    "ds_read_b64_tr_b16 a[242:243], v15 offset:16384\n" \
    "v_add_f32 v37, v37, v46\n" \
    "v_add_f32 v37, v37, v47\n" \
    "v_cvt_pk_bf16_f32 v59, v46, v47\n" \
    "v_exp_f32 v40, v120\n" \
    "v_mfma_f32_32x32x16_bf16 v[96:111], a[72:75], a[8:11], v[96:111]\n" \
    "ds_read_b64_tr_b16 a[244:245], v10 offset:16384\n" \
    "v_add_f32 v37, v37, v40\n" \
    "v_exp_f32 v41, v121\n" \
    "v_exp_f32 v42, v122\n" \
    "v_mfma_f32_32x32x16_bf16 v[96:111], a[76:79], a[12:15], v[96:111]\n" \
    "ds_read_b64_tr_b16 a[246:247], v11 offset:16384\n" \
    "v_add_f32 v37, v37, v41\n" \
    "v_cvt_pk_bf16_f32 v60, v40, v41\n" \
    "v_add_f32 v37, v37, v42\n" \
    "v_exp_f32 v43, v123\n" \
    "v_mfma_f32_32x32x16_bf16 v[96:111], a[80:83], a[16:19], v[96:111]\n" \
    "ds_read_b64_tr_b16 a[248:249], v14 offset:18432\n" \
    "v_add_f32 v37, v37, v43\n" \
    "v_cvt_pk_bf16_f32 v61, v42, v43\n" \
    "v_exp_f32 v44, v124\n" \
    "v_exp_f32 v45, v125\n" \
    "v_mfma_f32_32x32x16_bf16 v[96:111], a[84:87], a[20:23], v[96:111]\n" \
    "ds_read_b64_tr_b16 a[250:251], v15 offset:18432\n" \
    "v_add_f32 v37, v37, v44\n" \
    "v_add_f32 v37, v37, v45\n" \
    "v_cvt_pk_bf16_f32 v62, v44, v45\n" \
    "v_exp_f32 v46, v126\n" \
    "v_mfma_f32_32x32x16_bf16 v[96:111], a[88:91], a[24:27], v[96:111]\n" \
    "ds_read_b64_tr_b16 a[252:253], v10 offset:18432\n" \
    "v_add_u32 v22, s67, v22\n" \
    "v_add_u32 v23, s67, v23\n" \
    "v_add_f32 v37, v37, v46\n" \
    "v_exp_f32 v47, v127\n" \
    "s_waitcnt lgkmcnt(3)\n" \
    "v_mfma_f32_32x32x16_bf16 v[96:111], a[92:95], a[28:31], v[96:111]\n" \
    "ds_read_b64_tr_b16 a[254:255], v11 offset:18432\n" \
    "v_add_f32 v37, v37, v47\n" \
    "v_cvt_pk_bf16_f32 v63, v46, v47\n" \
    "v_cmp_lt_f32 vcc, s69, v37\n" \
    "s_cbranch_vccnz Lmha_stub_mh7\n" \
    "Lmha_back_mh7:\n" \
    "s_branch Lmha_rest\n" \
    "Lmha_h8:\n" \
    "s_waitcnt lgkmcnt(1)\n" \
    "v_mfma_f32_32x32x16_bf16 v[96:111], a[64:67], a[0:3], v[64:79]\n" \
    "v_add_f32 v37, v37, v45\n" \
    "v_cvt_pk_bf16_f32 v58, v44, v45\n" \
    "v_exp_f32 v46, v118\n" \
    "v_exp_f32 v47, v119\n" \
    "v_mfma_f32_32x32x16_bf16 v[96:111], a[68:71], a[4:7], v[96:111]\n" \
    "v_add_f32 v37, v37, v46\n" \
    "v_add_f32 v37, v37, v47\n" \
    "v_cvt_pk_bf16_f32 v59, v46, v47\n" \
    "v_exp_f32 v40, v120\n" \
    "v_mfma_f32_32x32x16_bf16 v[96:111], a[72:75], a[8:11], v[96:111]\n" \
    "v_add_f32 v37, v37, v40\n" \
    "v_exp_f32 v41, v121\n" \
    "v_exp_f32 v42, v122\n" \
    "v_mfma_f32_32x32x16_bf16 v[96:111], a[76:79], a[12:15], v[96:111]\n" \
    "v_add_f32 v37, v37, v41\n" \
    "v_cvt_pk_bf16_f32 v60, v40, v41\n" \
    "v_add_f32 v37, v37, v42\n" \
    "v_exp_f32 v43, v123\n" \
    "v_mfma_f32_32x32x16_bf16 v[96:111], a[80:83], a[16:19], v[96:111]\n" \
    "v_add_f32 v37, v37, v43\n" \
    "v_cvt_pk_bf16_f32 v61, v42, v43\n" \
    "v_exp_f32 v44, v124\n" \
    "v_exp_f32 v45, v125\n" \
    "v_mfma_f32_32x32x16_bf16 v[96:111], a[84:87], a[20:23], v[96:111]\n" \
    "v_add_f32 v37, v37, v44\n" \
    "v_add_f32 v37, v37, v45\n" \
    "v_cvt_pk_bf16_f32 v62, v44, v45\n" \
    "v_exp_f32 v46, v126\n" \
    "v_mfma_f32_32x32x16_bf16 v[96:111], a[88:91], a[24:27], v[96:111]\n" \
    "v_add_u32 v22, s67, v22\n" \
    "v_add_u32 v23, s67, v23\n" \
    "v_add_f32 v37, v37, v46\n" \
    "v_exp_f32 v47, v127\n" \
    "s_waitcnt lgkmcnt(0)\n" \
    "v_mfma_f32_32x32x16_bf16 v[96:111], a[92:95], a[28:31], v[96:111]\n" \
    "v_add_f32 v37, v37, v47\n" \
    "v_cvt_pk_bf16_f32 v63, v46, v47\n" \
    "v_cmp_lt_f32 vcc, s69, v37\n" \
    "s_cbranch_vccnz Lmha_stub_mCa\n" \
    "Lmha_back_mCa:\n" \
    "v_mfma_f32_32x32x16_bf16 v[192:207], a[96:99], v[56:59], v[192:207]\n" \
    "ds_read_b64_tr_b16 a[96:97], v22 offset:0\n" \
    "ds_read_b64_tr_b16 a[98:99], v23 offset:0\n" \
    "v_mfma_f32_32x32x16_bf16 v[208:223], a[100:103], v[56:59], v[208:223]\n" \
    "v_add_f32 v39, v39, v37\n" \
    "ds_read_b64_tr_b16 a[100:101], v22 offset:256\n" \
    "ds_read_b64_tr_b16 a[102:103], v23 offset:256\n" \
    "v_mfma_f32_32x32x16_bf16 v[224:239], a[104:107], v[56:59], v[224:239]\n" \
    "ds_read_b64_tr_b16 a[104:105], v22 offset:512\n" \
    "ds_read_b64_tr_b16 a[106:107], v23 offset:512\n" \
    "v_mfma_f32_32x32x16_bf16 v[240:255], a[108:111], v[56:59], v[240:255]\n" \
    "ds_read_b64_tr_b16 a[108:109], v22 offset:768\n" \
    "v_exp_f32 v40, v96\n" \
    "v_exp_f32 v41, v97\n" \
    "v_mfma_f32_32x32x16_bf16 v[192:207], a[112:115], v[60:63], v[192:207]\n" \
    "ds_read_b64_tr_b16 a[110:111], v23 offset:768\n" \
    "v_add_f32 v36, v40, v41\n" \
    "v_cvt_pk_bf16_f32 v48, v40, v41\n" \
    "v_exp_f32 v42, v98\n" \
    "v_mfma_f32_32x32x16_bf16 v[208:223], a[116:119], v[60:63], v[208:223]\n" \
    "ds_read_b64_tr_b16 a[112:113], v22 offset:4096\n" \
    "v_add_f32 v36, v36, v42\n" \
    "v_exp_f32 v43, v99\n" \
    "v_mfma_f32_32x32x16_bf16 v[224:239], a[120:123], v[60:63], v[224:239]\n" \
    "ds_read_b64_tr_b16 a[114:115], v23 offset:4096\n" \
    "v_add_f32 v36, v36, v43\n" \
    "v_cvt_pk_bf16_f32 v49, v42, v43\n" \
    "v_exp_f32 v44, v100\n" \
    "v_mfma_f32_32x32x16_bf16 v[240:255], a[124:127], v[60:63], v[240:255]\n" \
    "ds_read_b64_tr_b16 a[116:117], v22 offset:4352\n" \
    "v_add_f32 v36, v36, v44\n" \
    "v_exp_f32 v45, v101\n" \
    "v_mfma_f32_32x32x16_bf16 v[112:127], a[64:67], a[32:35], v[80:95]\n" \
    "v_add_f32 v36, v36, v45\n" \
    "v_cvt_pk_bf16_f32 v50, v44, v45\n" \
    "v_exp_f32 v46, v102\n" \
    "v_exp_f32 v47, v103\n" \
    "v_mfma_f32_32x32x16_bf16 v[112:127], a[68:71], a[36:39], v[112:127]\n" \
    "ds_read_b64_tr_b16 a[118:119], v23 offset:4352\n" \
    "v_add_f32 v36, v36, v46\n" \
    "v_add_f32 v36, v36, v47\n" \
    "v_cvt_pk_bf16_f32 v51, v46, v47\n" \
    "v_exp_f32 v40, v104\n" \
    "v_mfma_f32_32x32x16_bf16 v[112:127], a[72:75], a[40:43], v[112:127]\n" \
    "v_add_f32 v36, v36, v40\n" \
    "v_exp_f32 v41, v105\n" \
    "v_exp_f32 v42, v106\n" \
    "v_mfma_f32_32x32x16_bf16 v[112:127], a[76:79], a[44:47], v[112:127]\n" \
    "ds_read_b64_tr_b16 a[120:121], v22 offset:4608\n" \
    "v_add_f32 v36, v36, v41\n" \
    "v_cvt_pk_bf16_f32 v52, v40, v41\n" \
    "v_add_f32 v36, v36, v42\n" \
    "v_exp_f32 v43, v107\n" \
    "v_mfma_f32_32x32x16_bf16 v[112:127], a[80:83], a[48:51], v[112:127]\n" \
    "v_add_f32 v36, v36, v43\n" \
    "v_cvt_pk_bf16_f32 v53, v42, v43\n" \
    "v_exp_f32 v44, v108\n" \
    "v_exp_f32 v45, v109\n" \
    "v_mfma_f32_32x32x16_bf16 v[112:127], a[84:87], a[52:55], v[112:127]\n" \
    "ds_read_b64_tr_b16 a[122:123], v23 offset:4608\n" \
    "v_add_f32 v36, v36, v44\n" \
    "v_add_f32 v36, v36, v45\n" \
    "v_cvt_pk_bf16_f32 v54, v44, v45\n" \
    "v_exp_f32 v46, v110\n" \
    "v_mfma_f32_32x32x16_bf16 v[112:127], a[88:91], a[56:59], v[112:127]\n" \
    "ds_read_b64_tr_b16 a[124:125], v22 offset:4864\n" \
    "ds_read_b64_tr_b16 a[126:127], v23 offset:4864\n" \
    "v_add_f32 v36, v36, v46\n" \
    "v_exp_f32 v47, v111\n" \
    "v_mfma_f32_32x32x16_bf16 v[112:127], a[92:95], a[60:63], v[112:127]\n" \
    "v_add_f32 v36, v36, v47\n" \
    "v_cvt_pk_bf16_f32 v55, v46, v47\n" \
    "v_cmp_lt_f32 vcc, s69, v36\n" \
    "s_cbranch_vccnz Lmha_stub_mCb\n" \
    "Lmha_back_mCb:\n" \
    "s_waitcnt lgkmcnt(3)\n" \
    "v_mfma_f32_32x32x16_bf16 v[128:143], a[96:99], v[48:51], v[128:143]\n" \
    "v_add_f32 v38, v38, v36\n" \
    "ds_read_b64_tr_b16 a[0:1], v16 offset:0\n" \
    "ds_read_b64_tr_b16 a[2:3], v17 offset:0\n" \
    "ds_read_b64_tr_b16 a[4:5], v1 offset:0\n" \
    "ds_read_b64_tr_b16 a[6:7], v2 offset:0\n" \
    "ds_read_b64_tr_b16 a[8:9], v16 offset:2048\n" \
    "ds_read_b64_tr_b16 a[10:11], v17 offset:2048\n" \
    "v_mfma_f32_32x32x16_bf16 v[144:159], a[100:103], v[48:51], v[144:159]\n" \
    "ds_read_b64_tr_b16 a[12:13], v1 offset:2048\n" \
    "ds_read_b64_tr_b16 a[14:15], v2 offset:2048\n" \
    "ds_read_b64_tr_b16 a[16:17], v16 offset:16384\n" \
    "ds_read_b64_tr_b16 a[18:19], v17 offset:16384\n" \
    "ds_read_b64_tr_b16 a[20:21], v1 offset:16384\n" \
    "ds_read_b64_tr_b16 a[22:23], v2 offset:16384\n" \
    "v_mfma_f32_32x32x16_bf16 v[160:175], a[104:107], v[48:51], v[160:175]\n" \
    "ds_read_b64_tr_b16 a[24:25], v16 offset:18432\n" \
    "ds_read_b64_tr_b16 a[26:27], v17 offset:18432\n" \
    "ds_read_b64_tr_b16 a[28:29], v1 offset:18432\n" \
    "ds_read_b64_tr_b16 a[30:31], v2 offset:18432\n" \
    "v_mfma_f32_32x32x16_bf16 v[176:191], a[108:111], v[48:51], v[176:191]\n" \
    "v_exp_f32 v40, v112\n" \
    "v_exp_f32 v41, v113\n" \
    "v_exp_f32 v42, v114\n" \
    "v_exp_f32 v43, v115\n" \
    "v_add_f32 v37, v40, v41\n" \
    "v_cvt_pk_bf16_f32 v56, v40, v41\n" \
    "v_mfma_f32_32x32x16_bf16 v[128:143], a[112:115], v[52:55], v[128:143]\n" \
    "v_exp_f32 v44, v116\n" \
    "v_exp_f32 v45, v117\n" \
    "v_add_f32 v37, v37, v42\n" \
    "v_add_f32 v37, v37, v43\n" \
    "v_cvt_pk_bf16_f32 v57, v42, v43\n" \
    "v_exp_f32 v46, v118\n" \
    "v_mfma_f32_32x32x16_bf16 v[144:159], a[116:119], v[52:55], v[144:159]\n" \
    "v_exp_f32 v47, v119\n" \
    "v_add_f32 v37, v37, v44\n" \
    "v_add_f32 v37, v37, v45\n" \
    "v_cvt_pk_bf16_f32 v58, v44, v45\n" \
    "v_exp_f32 v40, v120\n" \
    "v_exp_f32 v41, v121\n" \
    "s_waitcnt lgkmcnt(0)\n" \
    "v_mfma_f32_32x32x16_bf16 v[160:175], a[120:123], v[52:55], v[160:175]\n" \
    "v_add_f32 v37, v37, v46\n" \
    "v_add_f32 v37, v37, v47\n" \
    "v_cvt_pk_bf16_f32 v59, v46, v47\n" \
    "v_exp_f32 v42, v122\n" \
    "v_exp_f32 v43, v123\n" \
    "v_add_f32 v37, v37, v40\n" \
    "v_mfma_f32_32x32x16_bf16 v[176:191], a[124:127], v[52:55], v[176:191]\n" \
    "v_add_f32 v37, v37, v41\n" \
    "v_cvt_pk_bf16_f32 v60, v40, v41\n" \
    "v_exp_f32 v44, v124\n" \
    "v_exp_f32 v45, v125\n" \
    "v_add_f32 v37, v37, v42\n" \
    "v_add_f32 v37, v37, v43\n" \
    "v_cvt_pk_bf16_f32 v61, v42, v43\n" \
    "v_exp_f32 v46, v126\n" \
    "v_exp_f32 v47, v127\n" \
    "v_add_f32 v37, v37, v44\n" \
    "v_add_f32 v37, v37, v45\n" \
    "v_cvt_pk_bf16_f32 v62, v44, v45\n" \
    "v_add_f32 v37, v37, v46\n" \
    "v_add_f32 v37, v37, v47\n" \
    "v_cvt_pk_bf16_f32 v63, v46, v47\n" \
    "v_cmp_lt_f32 vcc, s69, v37\n" \
    "s_cbranch_vccnz Lmha_stub_me1\n" \
    "Lmha_back_me1:\n" \
    "v_mfma_f32_32x32x16_bf16 v[192:207], a[96:99], v[56:59], v[192:207]\n" \
    "v_add_f32 v39, v39, v37\n" \
    "v_mov_b32 v47, v38\n" \
    "s_nop 1\n" \
    "v_permlane32_swap_b32 v38, v47\n" \
    "v_add_f32 v38, v38, v47\n" \
    "v_rcp_f32 v32, v38\n" \
    "s_lshl_b32 s55, s52, 19\n" \
    "s_add_u32 s55, s55, 0\n" \
    "v_mfma_f32_32x32x16_bf16 v[192:207], a[112:115], v[60:63], v[192:207]\n" \
    "s_add_u32 s56, s55, 4096\n" \
    "v_pk_mul_f32 v[128:129], v[128:129], v[32:33] op_sel_hi:[1,0]\n" \
    "v_pk_mul_f32 v[130:131], v[130:131], v[32:33] op_sel_hi:[1,0]\n" \
    "v_pk_mul_f32 v[132:133], v[132:133], v[32:33] op_sel_hi:[1,0]\n" \
    "v_pk_mul_f32 v[134:135], v[134:135], v[32:33] op_sel_hi:[1,0]\n" \
    "v_cvt_pk_bf16_f32 v96, v128, v129\n" \
    "v_cvt_pk_bf16_f32 v97, v130, v131\n" \
    "v_mfma_f32_32x32x16_bf16 v[208:223], a[100:103], v[56:59], v[208:223]\n" \
    "v_cvt_pk_bf16_f32 v98, v132, v133\n" \
    "v_cvt_pk_bf16_f32 v99, v134, v135\n" \
    "buffer_store_dwordx4 v[96:99], v24, s[40:43], s55 offen offset:0 sc1\n" \
    "v_pk_mul_f32 v[136:137], v[136:137], v[32:33] op_sel_hi:[1,0]\n" \
    "v_pk_mul_f32 v[138:139], v[138:139], v[32:33] op_sel_hi:[1,0]\n" \
    "v_pk_mul_f32 v[140:141], v[140:141], v[32:33] op_sel_hi:[1,0]\n" \
    "v_pk_mul_f32 v[142:143], v[142:143], v[32:33] op_sel_hi:[1,0]\n" \
    "v_mfma_f32_32x32x16_bf16 v[208:223], a[116:119], v[60:63], v[208:223]\n" \
    "v_cvt_pk_bf16_f32 v100, v136, v137\n" \
    "v_cvt_pk_bf16_f32 v101, v138, v139\n" \
    "v_cvt_pk_bf16_f32 v102, v140, v141\n" \
    "v_cvt_pk_bf16_f32 v103, v142, v143\n" \
    "buffer_store_dwordx4 v[100:103], v24, s[40:43], s55 offen offset:1024 sc1\n" \
    "v_pk_mul_f32 v[144:145], v[144:145], v[32:33] op_sel_hi:[1,0]\n" \
    "v_pk_mul_f32 v[146:147], v[146:147], v[32:33] op_sel_hi:[1,0]\n" \
    "v_mfma_f32_32x32x16_bf16 v[224:239], a[104:107], v[56:59], v[224:239]\n" \
    "v_pk_mul_f32 v[148:149], v[148:149], v[32:33] op_sel_hi:[1,0]\n" \
    "v_pk_mul_f32 v[150:151], v[150:151], v[32:33] op_sel_hi:[1,0]\n" \
    "v_cvt_pk_bf16_f32 v104, v144, v145\n" \
    "v_cvt_pk_bf16_f32 v105, v146, v147\n" \
    "v_cvt_pk_bf16_f32 v106, v148, v149\n" \
    "v_cvt_pk_bf16_f32 v107, v150, v151\n" \
    "buffer_store_dwordx4 v[104:107], v24, s[40:43], s55 offen offset:2048 sc1\n" \
    "v_mfma_f32_32x32x16_bf16 v[224:239], a[120:123], v[60:63], v[224:239]\n" \
    "v_pk_mul_f32 v[152:153], v[152:153], v[32:33] op_sel_hi:[1,0]\n" \
    "v_pk_mul_f32 v[154:155], v[154:155], v[32:33] op_sel_hi:[1,0]\n" \
    "v_pk_mul_f32 v[156:157], v[156:157], v[32:33] op_sel_hi:[1,0]\n" \
    "v_pk_mul_f32 v[158:159], v[158:159], v[32:33] op_sel_hi:[1,0]\n" \
    "v_cvt_pk_bf16_f32 v108, v152, v153\n" \
    "v_cvt_pk_bf16_f32 v109, v154, v155\n" \
    "v_cvt_pk_bf16_f32 v110, v156, v157\n" \
    "v_mfma_f32_32x32x16_bf16 v[240:255], a[108:111], v[56:59], v[240:255]\n" \
    "v_cvt_pk_bf16_f32 v111, v158, v159\n" \
    "buffer_store_dwordx4 v[108:111], v24, s[40:43], s55 offen offset:3072 sc1\n" \
    "v_pk_mul_f32 v[160:161], v[160:161], v[32:33] op_sel_hi:[1,0]\n" \
    "v_pk_mul_f32 v[162:163], v[162:163], v[32:33] op_sel_hi:[1,0]\n" \
    "v_pk_mul_f32 v[164:165], v[164:165], v[32:33] op_sel_hi:[1,0]\n" \
    "v_pk_mul_f32 v[166:167], v[166:167], v[32:33] op_sel_hi:[1,0]\n" \
    "v_cvt_pk_bf16_f32 v64, v160, v161\n" \
    "v_mfma_f32_32x32x16_bf16 v[240:255], a[124:127], v[60:63], v[240:255]\n" \
    "v_cvt_pk_bf16_f32 v65, v162, v163\n" \
    "v_cvt_pk_bf16_f32 v66, v164, v165\n" \
    "v_cvt_pk_bf16_f32 v67, v166, v167\n" \
    "buffer_store_dwordx4 v[64:67], v24, s[40:43], s56 offen offset:0 sc1\n" \
    "v_pk_mul_f32 v[168:169], v[168:169], v[32:33] op_sel_hi:[1,0]\n" \
    "v_pk_mul_f32 v[170:171], v[170:171], v[32:33] op_sel_hi:[1,0]\n" \
    "v_pk_mul_f32 v[172:173], v[172:173], v[32:33] op_sel_hi:[1,0]\n" \
    "v_pk_mul_f32 v[174:175], v[174:175], v[32:33] op_sel_hi:[1,0]\n" \
    "v_cvt_pk_bf16_f32 v68, v168, v169\n" \
    "v_cvt_pk_bf16_f32 v69, v170, v171\n" \
    "v_cvt_pk_bf16_f32 v70, v172, v173\n" \
    "v_cvt_pk_bf16_f32 v71, v174, v175\n" \
    "buffer_store_dwordx4 v[68:71], v24, s[40:43], s56 offen offset:1024 sc1\n" \
    "v_pk_mul_f32 v[176:177], v[176:177], v[32:33] op_sel_hi:[1,0]\n" \
    "v_pk_mul_f32 v[178:179], v[178:179], v[32:33] op_sel_hi:[1,0]\n" \
    "v_pk_mul_f32 v[180:181], v[180:181], v[32:33] op_sel_hi:[1,0]\n" \
    "v_pk_mul_f32 v[182:183], v[182:183], v[32:33] op_sel_hi:[1,0]\n" \
    "v_cvt_pk_bf16_f32 v72, v176, v177\n" \
    "v_cvt_pk_bf16_f32 v73, v178, v179\n" \
    "v_cvt_pk_bf16_f32 v74, v180, v181\n" \
    "v_cvt_pk_bf16_f32 v75, v182, v183\n" \
    "buffer_store_dwordx4 v[72:75], v24, s[40:43], s56 offen offset:2048 sc1\n" \
    "v_pk_mul_f32 v[184:185], v[184:185], v[32:33] op_sel_hi:[1,0]\n" \
    "v_pk_mul_f32 v[186:187], v[186:187], v[32:33] op_sel_hi:[1,0]\n" \
    "v_pk_mul_f32 v[188:189], v[188:189], v[32:33] op_sel_hi:[1,0]\n" \
    "v_pk_mul_f32 v[190:191], v[190:191], v[32:33] op_sel_hi:[1,0]\n" \
    "v_cvt_pk_bf16_f32 v76, v184, v185\n" \
    "v_cvt_pk_bf16_f32 v77, v186, v187\n" \
    "v_cvt_pk_bf16_f32 v78, v188, v189\n" \
    "v_cvt_pk_bf16_f32 v79, v190, v191\n" \
    "buffer_store_dwordx4 v[76:79], v24, s[40:43], s56 offen offset:3072 sc1\n" \
    "v_mov_b32 v47, v39\n" \
    "s_nop 1\n" \
    "v_permlane32_swap_b32 v39, v47\n" \
    "v_add_f32 v39, v39, v47\n" \
    "v_rcp_f32 v32, v39\n" \
    "s_lshl_b32 s55, s52, 19\n" \
    "s_add_u32 s55, s55, 262144\n" \
    "s_add_u32 s56, s55, 4096\n" \
    "v_pk_mul_f32 v[192:193], v[192:193], v[32:33] op_sel_hi:[1,0]\n" \
    "v_pk_mul_f32 v[194:195], v[194:195], v[32:33] op_sel_hi:[1,0]\n" \
    "v_pk_mul_f32 v[196:197], v[196:197], v[32:33] op_sel_hi:[1,0]\n" \
    "v_pk_mul_f32 v[198:199], v[198:199], v[32:33] op_sel_hi:[1,0]\n" \
    "v_cvt_pk_bf16_f32 v112, v192, v193\n" \
    "v_cvt_pk_bf16_f32 v113, v194, v195\n" \
    "v_cvt_pk_bf16_f32 v114, v196, v197\n" \
    "v_cvt_pk_bf16_f32 v115, v198, v199\n" \
    "buffer_store_dwordx4 v[112:115], v24, s[40:43], s55 offen offset:0 sc1\n" \
    "v_pk_mul_f32 v[200:201], v[200:201], v[32:33] op_sel_hi:[1,0]\n" \
    "v_pk_mul_f32 v[202:203], v[202:203], v[32:33] op_sel_hi:[1,0]\n" \
    "v_pk_mul_f32 v[204:205], v[204:205], v[32:33] op_sel_hi:[1,0]\n" \
    "v_pk_mul_f32 v[206:207], v[206:207], v[32:33] op_sel_hi:[1,0]\n" \
    "v_cvt_pk_bf16_f32 v116, v200, v201\n" \
    "v_cvt_pk_bf16_f32 v117, v202, v203\n" \
    "v_cvt_pk_bf16_f32 v118, v204, v205\n" \
    "v_cvt_pk_bf16_f32 v119, v206, v207\n" \
    "buffer_store_dwordx4 v[116:119], v24, s[40:43], s55 offen offset:1024 sc1\n" \
    "v_pk_mul_f32 v[208:209], v[208:209], v[32:33] op_sel_hi:[1,0]\n" \
    "v_pk_mul_f32 v[210:211], v[210:211], v[32:33] op_sel_hi:[1,0]\n" \
    "v_pk_mul_f32 v[212:213], v[212:213], v[32:33] op_sel_hi:[1,0]\n" \
    "v_pk_mul_f32 v[214:215], v[214:215], v[32:33] op_sel_hi:[1,0]\n" \
    "v_cvt_pk_bf16_f32 v120, v208, v209\n" \
    "v_cvt_pk_bf16_f32 v121, v210, v211\n" \
    "v_cvt_pk_bf16_f32 v122, v212, v213\n" \
    "v_cvt_pk_bf16_f32 v123, v214, v215\n" \
    "buffer_store_dwordx4 v[120:123], v24, s[40:43], s55 offen offset:2048 sc1\n" \
    "v_pk_mul_f32 v[216:217], v[216:217], v[32:33] op_sel_hi:[1,0]\n" \
    "v_pk_mul_f32 v[218:219], v[218:219], v[32:33] op_sel_hi:[1,0]\n" \
    "v_pk_mul_f32 v[220:221], v[220:221], v[32:33] op_sel_hi:[1,0]\n" \
    "v_pk_mul_f32 v[222:223], v[222:223], v[32:33] op_sel_hi:[1,0]\n" \
    "v_cvt_pk_bf16_f32 v124, v216, v217\n" \
    "v_cvt_pk_bf16_f32 v125, v218, v219\n" \
    "v_cvt_pk_bf16_f32 v126, v220, v221\n" \
    "v_cvt_pk_bf16_f32 v127, v222, v223\n" \
    "buffer_store_dwordx4 v[124:127], v24, s[40:43], s55 offen offset:3072 sc1\n" \
    "v_pk_mul_f32 v[224:225], v[224:225], v[32:33] op_sel_hi:[1,0]\n" \
    "v_pk_mul_f32 v[226:227], v[226:227], v[32:33] op_sel_hi:[1,0]\n" \
    "v_pk_mul_f32 v[228:229], v[228:229], v[32:33] op_sel_hi:[1,0]\n" \
    "v_pk_mul_f32 v[230:231], v[230:231], v[32:33] op_sel_hi:[1,0]\n" \
    "v_cvt_pk_bf16_f32 v80, v224, v225\n" \
    "v_cvt_pk_bf16_f32 v81, v226, v227\n" \
    "v_cvt_pk_bf16_f32 v82, v228, v229\n" \
    "v_cvt_pk_bf16_f32 v83, v230, v231\n" \
    "buffer_store_dwordx4 v[80:83], v24, s[40:43], s56 offen offset:0 sc1\n" \
    "v_pk_mul_f32 v[232:233], v[232:233], v[32:33] op_sel_hi:[1,0]\n" \
    "v_pk_mul_f32 v[234:235], v[234:235], v[32:33] op_sel_hi:[1,0]\n" \
    "v_pk_mul_f32 v[236:237], v[236:237], v[32:33] op_sel_hi:[1,0]\n" \
    "v_pk_mul_f32 v[238:239], v[238:239], v[32:33] op_sel_hi:[1,0]\n" \
    "v_cvt_pk_bf16_f32 v84, v232, v233\n" \
    "v_cvt_pk_bf16_f32 v85, v234, v235\n" \
    "v_cvt_pk_bf16_f32 v86, v236, v237\n" \
    "v_cvt_pk_bf16_f32 v87, v238, v239\n" \
    "buffer_store_dwordx4 v[84:87], v24, s[40:43], s56 offen offset:1024 sc1\n" \
    "v_pk_mul_f32 v[240:241], v[240:241], v[32:33] op_sel_hi:[1,0]\n" \
    "v_pk_mul_f32 v[242:243], v[242:243], v[32:33] op_sel_hi:[1,0]\n" \
    "v_pk_mul_f32 v[244:245], v[244:245], v[32:33] op_sel_hi:[1,0]\n" \
    "v_pk_mul_f32 v[246:247], v[246:247], v[32:33] op_sel_hi:[1,0]\n" \
    "v_cvt_pk_bf16_f32 v88, v240, v241\n" \
    "v_cvt_pk_bf16_f32 v89, v242, v243\n" \
    "v_cvt_pk_bf16_f32 v90, v244, v245\n" \
    "v_cvt_pk_bf16_f32 v91, v246, v247\n" \
    "buffer_store_dwordx4 v[88:91], v24, s[40:43], s56 offen offset:2048 sc1\n" \
    "v_pk_mul_f32 v[248:249], v[248:249], v[32:33] op_sel_hi:[1,0]\n" \
    "v_pk_mul_f32 v[250:251], v[250:251], v[32:33] op_sel_hi:[1,0]\n" \
    "v_pk_mul_f32 v[252:253], v[252:253], v[32:33] op_sel_hi:[1,0]\n" \
    "v_pk_mul_f32 v[254:255], v[254:255], v[32:33] op_sel_hi:[1,0]\n" \
    "v_cvt_pk_bf16_f32 v92, v248, v249\n" \
    "v_cvt_pk_bf16_f32 v93, v250, v251\n" \
    "v_cvt_pk_bf16_f32 v94, v252, v253\n" \
    "v_cvt_pk_bf16_f32 v95, v254, v255\n" \
    "buffer_store_dwordx4 v[92:95], v24, s[40:43], s56 offen offset:3072 sc1\n" \
    "ds_read_b64_tr_b16 a[32:33], v16 offset:32768\n" \
    "ds_read_b64_tr_b16 a[34:35], v17 offset:32768\n" \
    "ds_read_b64_tr_b16 a[36:37], v1 offset:32768\n" \
    "ds_read_b64_tr_b16 a[38:39], v2 offset:32768\n" \
    "ds_read_b64_tr_b16 a[40:41], v16 offset:34816\n" \
    "ds_read_b64_tr_b16 a[42:43], v17 offset:34816\n" \
    "ds_read_b64_tr_b16 a[44:45], v1 offset:34816\n" \
    "ds_read_b64_tr_b16 a[46:47], v2 offset:34816\n" \
    "ds_read_b64_tr_b16 a[48:49], v16 offset:49152\n" \
    "ds_read_b64_tr_b16 a[50:51], v17 offset:49152\n" \
    "ds_read_b64_tr_b16 a[52:53], v1 offset:49152\n" \
    "ds_read_b64_tr_b16 a[54:55], v2 offset:49152\n" \
    "ds_read_b64_tr_b16 a[56:57], v16 offset:51200\n" \
    "ds_read_b64_tr_b16 a[58:59], v17 offset:51200\n" \
    "ds_read_b64_tr_b16 a[60:61], v1 offset:51200\n" \
    "ds_read_b64_tr_b16 a[62:63], v2 offset:51200\n" \
    "ds_read_b64_tr_b16 a[64:65], v18 offset:0\n" \
    "ds_read_b64_tr_b16 a[66:67], v19 offset:0\n" \
    "ds_read_b64_tr_b16 a[68:69], v3 offset:0\n" \
    "ds_read_b64_tr_b16 a[70:71], v4 offset:0\n" \
    "ds_read_b64_tr_b16 a[72:73], v18 offset:2048\n" \
    "ds_read_b64_tr_b16 a[74:75], v19 offset:2048\n" \
    "ds_read_b64_tr_b16 a[76:77], v3 offset:2048\n" \
    "ds_read_b64_tr_b16 a[78:79], v4 offset:2048\n" \
    "ds_read_b64_tr_b16 a[80:81], v14 offset:0\n" \
    "ds_read_b64_tr_b16 a[82:83], v15 offset:0\n" \
    "ds_read_b64_tr_b16 a[84:85], v10 offset:0\n" \
    "ds_read_b64_tr_b16 a[86:87], v11 offset:0\n" \
    "ds_read_b64_tr_b16 a[88:89], v14 offset:2048\n" \
    "ds_read_b64_tr_b16 a[90:91], v15 offset:2048\n" \
    "ds_read_b64_tr_b16 a[92:93], v10 offset:2048\n" \
    "ds_read_b64_tr_b16 a[94:95], v11 offset:2048\n" \
    "s_mov_b32 s55, 16777216\n" \
    "s_add_u32 s36, s20, s55\n" \
    "s_addc_u32 s37, s21, 0\n" \
    "s_and_b32 s37, s37, 0xffff\n" \
    "s_mov_b32 s38, 131080\n" \
    "s_mov_b32 s39, 0x00020000\n" \
    "s_lshl_b32 s55, s53, 5\n" \
    "s_add_u32 s55, s55, s54\n" \
    "s_lshl2_add_u32 s55, s55, s52\n" \
    "s_lshl_b32 s55, s55, 7\n" \
    "v_mov_b32 v28, s55\n" \
    "v_mov_b32 v30, s22\n" \
    "v_mov_b32 v31, s23\n" \
    "s_waitcnt lgkmcnt(0)\n" \
    "s_barrier\n" \
    "ds_read_b64_tr_b16 a[96:97], v14 offset:16384\n" \
    "ds_read_b64_tr_b16 a[98:99], v15 offset:16384\n" \
    "ds_read_b64_tr_b16 a[100:101], v10 offset:16384\n" \
    "ds_read_b64_tr_b16 a[102:103], v11 offset:16384\n" \
    "ds_read_b64_tr_b16 a[104:105], v14 offset:18432\n" \
    "ds_read_b64_tr_b16 a[106:107], v15 offset:18432\n" \
    "ds_read_b64_tr_b16 a[108:109], v10 offset:18432\n" \
    "ds_read_b64_tr_b16 a[110:111], v11 offset:18432\n" \
    "ds_read_b64_tr_b16 a[112:113], v18 offset:16384\n" \
    "ds_read_b64_tr_b16 a[114:115], v19 offset:16384\n" \
    "ds_read_b64_tr_b16 a[116:117], v3 offset:16384\n" \
    "ds_read_b64_tr_b16 a[118:119], v4 offset:16384\n" \
    "ds_read_b64_tr_b16 a[120:121], v18 offset:18432\n" \
    "ds_read_b64_tr_b16 a[122:123], v19 offset:18432\n" \
    "ds_read_b64_tr_b16 a[124:125], v3 offset:18432\n" \
    "ds_read_b64_tr_b16 a[126:127], v4 offset:18432\n" \
    "s_waitcnt vmcnt(0)\n" \
    "s_mov_b64 exec, 1\n" \
    "buffer_store_dwordx2 v[30:31], v28, s[36:39], 0 offen offset:0 sc1\n" \
    "s_mov_b64 exec, -1\n" \
    "Lmha_join:\n" \
    "s_cmp_lt_u32 s78, 9\n" \
    "s_cbranch_scc0 Lmha_idle\n" \
    "s_lshl_b32 s73, s52, 12\n" \
    "s_mul_i32 s79, s52, 1179648\n" \
    "s_add_u32 m0, s73, 32768\n" \
    "s_add_u32 s75, s79, 737280\n" \
    "buffer_load_dwordx4 v5, s[28:31], s75 offen offset:0 lds\n" \
    "buffer_load_dwordx4 v6, s[28:31], s75 offen offset:1024 lds\n" \
    "buffer_load_dwordx4 v7, s[28:31], s75 offen offset:2048 lds\n" \
    "buffer_load_dwordx4 v8, s[28:31], s75 offen offset:3072 lds\n" \
    "s_add_u32 m0, s73, 49152\n" \
    "s_add_u32 s75, s79, 774144\n" \
    "buffer_load_dwordx4 v5, s[28:31], s75 offen offset:0 lds\n" \
    "buffer_load_dwordx4 v6, s[28:31], s75 offen offset:1024 lds\n" \
    "buffer_load_dwordx4 v7, s[28:31], s75 offen offset:2048 lds\n" \
    "buffer_load_dwordx4 v8, s[28:31], s75 offen offset:3072 lds\n" \
    "s_add_u32 m0, s73, 65536\n" \
    "s_add_u32 s75, s79, 811008\n" \
    "buffer_load_dwordx4 v5, s[28:31], s75 offen offset:0 lds\n" \
    "buffer_load_dwordx4 v6, s[28:31], s75 offen offset:1024 lds\n" \
    "buffer_load_dwordx4 v7, s[28:31], s75 offen offset:2048 lds\n" \
    "buffer_load_dwordx4 v8, s[28:31], s75 offen offset:3072 lds\n" \
    "s_add_u32 m0, s73, 131072\n" \
    "s_add_u32 s75, s79, 847872\n" \
    "buffer_load_dwordx4 v5, s[28:31], s75 offen offset:0 lds\n" \
    "buffer_load_dwordx4 v6, s[28:31], s75 offen offset:1024 lds\n" \
    "buffer_load_dwordx4 v7, s[28:31], s75 offen offset:2048 lds\n" \
    "buffer_load_dwordx4 v8, s[28:31], s75 offen offset:3072 lds\n" \
    "s_lshl_b32 s61, s77, 1\n" \
    "s_add_u32 s62, s61, 1\n" \
    "s_mul_i32 s57, s61, 37\n" \
    "s_lshr_b32 s57, s57, 8\n" \
    "s_mul_i32 s55, s57, 7\n" \
    "s_sub_u32 s55, s61, s55\n" \
    "s_lshr_b32 s55, s55, 1\n" \
    "s_add_u32 s61, s61, s57\n" \
    "s_lshl_b32 s57, s57, 7\n" \
    "s_add_u32 s55, s55, s57\n" \
    "s_mul_i32 s57, s62, 37\n" \
    "s_lshr_b32 s57, s57, 8\n" \
    "s_mul_i32 s56, s57, 7\n" \
    "s_sub_u32 s56, s62, s56\n" \
    "s_lshr_b32 s56, s56, 1\n" \
    "s_add_u32 s62, s62, s57\n" \
    "s_lshl_b32 s57, s57, 7\n" \
    "s_add_u32 s56, s56, s57\n" \
    "s_sub_u32 s56, s56, s55\n" \
    "s_lshl_b32 s57, s52, 5\n" \
    "s_add_u32 s55, s55, s57\n" \
    "v_lshrrev_b32 v29, 4, v24\n" \
    "v_bfe_u32 v28, v29, 3, 1\n" \
    "v_mul_u32_u24 v28, s56, v28\n" \
    "v_and_b32 v27, 7, v29\n" \
    "v_lshl_add_u32 v28, v27, 2, v28\n" \
    "v_add_u32 v28, s55, v28\n" \
    "v_lshlrev_b32 v28, 7, v28\n" \
    "s_mov_b32 s55, 0\n" \
    "s_add_u32 s44, s20, s55\n" \
    "s_addc_u32 s45, s21, 0\n" \
    "s_and_b32 s45, s45, 0xffff\n" \
    "s_mov_b32 s46, 16777216\n" \
    "s_mov_b32 s47, 0x00020000\n" \
    "s_lshl_b32 s55, s52, 16\n" \
    "s_lshl_b32 s68, s61, 18\n" \
    "s_lshl_b32 s76, s62, 18\n" \
    "s_add_u32 s68, s68, s55\n" \
    "s_add_u32 s76, s76, s55\n" \
    "s_cmp_lt_u32 s52, 2\n" \
    "s_cselect_b32 s61, s68, s76\n" \
    "s_cselect_b32 s62, s76, s68\n" \
    "s_mov_b64 exec, 0xffff\n" \
    "s_mov_b32 s63, 400000\n" \
    "buffer_load_dwordx2 v[30:31], v28, s[36:39], 0 offen offset:0 sc1\n" \
    "s_mov_b64 exec, -1\n" \
    "s_add_u32 m0, s73, 147456\n" \
    "s_add_u32 s75, s79, 884736\n" \
    "s_waitcnt lgkmcnt(0)\n" \
    "buffer_load_dwordx4 v5, s[28:31], s75 offen offset:0 lds\n" \
    "buffer_load_dwordx4 v6, s[28:31], s75 offen offset:1024 lds\n" \
    "buffer_load_dwordx4 v7, s[28:31], s75 offen offset:2048 lds\n" \
    "buffer_load_dwordx4 v8, s[28:31], s75 offen offset:3072 lds\n" \
    "s_add_u32 m0, s73, 81920\n" \
    "s_add_u32 s75, s79, 921600\n" \
    "buffer_load_dwordx4 v5, s[28:31], s75 offen offset:0 lds\n" \
    "buffer_load_dwordx4 v6, s[28:31], s75 offen offset:1024 lds\n" \
    "buffer_load_dwordx4 v7, s[28:31], s75 offen offset:2048 lds\n" \
    "buffer_load_dwordx4 v8, s[28:31], s75 offen offset:3072 lds\n" \
    "s_mov_b64 exec, 0xffff\n" \
    "s_waitcnt vmcnt(8)\n" \
    "v_cmp_ne_u64 vcc, s[22:23], v[30:31]\n" \
    "s_cbranch_vccz Lmha_okA\n" \
    "Lmha_spA:\n" \
    "s_sleep 1\n" \
    "buffer_load_dwordx2 v[30:31], v28, s[36:39], 0 offen offset:0 sc1\n" \
    "s_waitcnt vmcnt(0)\n" \
    "s_sub_u32 s63, s63, 1\n" \
    "s_cbranch_scc1 Lmha_okA\n" \
    "v_cmp_ne_u64 vcc, s[22:23], v[30:31]\n" \
    "s_cbranch_vccnz Lmha_spA\n" \
    "Lmha_okA:\n" \
    "s_mov_b64 exec, -1\n" \
    "buffer_load_dwordx4 v[128:131], v24, s[44:47], s61 offen offset:0 sc1\n" \
    "buffer_load_dwordx4 v[132:135], v24, s[44:47], s62 offen offset:0 sc1\n" \
    "buffer_load_dwordx4 v[136:139], v24, s[44:47], s61 offen offset:1024 sc1\n" \
    "buffer_load_dwordx4 v[140:143], v24, s[44:47], s62 offen offset:1024 sc1\n" \
    "buffer_load_dwordx4 v[144:147], v24, s[44:47], s61 offen offset:2048 sc1\n" \
    "buffer_load_dwordx4 v[148:151], v24, s[44:47], s62 offen offset:2048 sc1\n" \
    "buffer_load_dwordx4 v[152:155], v24, s[44:47], s61 offen offset:3072 sc1\n" \
    "buffer_load_dwordx4 v[156:159], v24, s[44:47], s62 offen offset:3072 sc1\n" \
    "s_add_u32 s61, s61, 4096\n" \
    "s_add_u32 s62, s62, 4096\n" \
    "buffer_load_dwordx4 v[160:163], v24, s[44:47], s61 offen offset:0 sc1\n" \
    "buffer_load_dwordx4 v[164:167], v24, s[44:47], s62 offen offset:0 sc1\n" \
    "buffer_load_dwordx4 v[168:171], v24, s[44:47], s61 offen offset:1024 sc1\n" \
    "buffer_load_dwordx4 v[172:175], v24, s[44:47], s62 offen offset:1024 sc1\n" \
    "buffer_load_dwordx4 v[176:179], v24, s[44:47], s61 offen offset:2048 sc1\n" \
    "buffer_load_dwordx4 v[180:183], v24, s[44:47], s62 offen offset:2048 sc1\n" \
    "buffer_load_dwordx4 v[184:187], v24, s[44:47], s61 offen offset:3072 sc1\n" \
    "buffer_load_dwordx4 v[188:191], v24, s[44:47], s62 offen offset:3072 sc1\n" \
    "v_add_u32 v20, s73, v24\n" \
    "v_xor_b32 v21, 4096, v20\n" \
    "v_xor_b32 v22, 8192, v20\n" \
    "v_xor_b32 v23, 12288, v20\n" \
    "s_lshr_b32 s55, s52, 1\n" \
    "s_lshl1_add_u32 s55, s77, s55\n" \
    "s_mul_i32 s55, s55, 36864\n" \
    "s_and_b32 s56, s52, 1\n" \
    "s_lshl1_add_u32 s56, s78, s56\n" \
    "s_lshl_b32 s56, s56, 6\n" \
    "s_add_u32 s57, s55, s56\n" \
    "s_mov_b32 s55, 0\n" \
    "s_add_u32 s24, s18, s55\n" \
    "s_addc_u32 s25, s19, 0\n" \
    "s_and_b32 s25, s25, 0xffff\n" \
    "s_mov_b32 s26, 2064384\n" \
    "s_mov_b32 s27, 0x00020000\n" \
    "v_and_b32 v28, 31, v29\n" \
    "v_mul_u32_u24 v28, 1152, v28\n" \
    "v_lshrrev_b32 v29, 5, v29\n" \
    "v_lshl_add_u32 v28, v29, 4, v28\n" \
    "s_waitcnt vmcnt(15)\n" \
    "v_mfma_f32_32x32x16_bf16 v[64:79], a[128:131], v[128:131], 0\n" \
    "v_mfma_f32_32x32x16_bf16 v[80:95], a[132:135], v[128:131], 0\n" \
    "s_waitcnt vmcnt(14)\n" \
    "v_mfma_f32_32x32x16_bf16 v[96:111], a[128:131], v[132:135], 0\n" \
    "v_mfma_f32_32x32x16_bf16 v[112:127], a[132:135], v[132:135], 0\n" \
    "s_add_u32 s61, s61, 4096\n" \
    "s_add_u32 s62, s62, 4096\n" \
    "buffer_load_dwordx4 v[128:131], v24, s[44:47], s61 offen offset:0 sc1\n" \
    "buffer_load_dwordx4 v[132:135], v24, s[44:47], s62 offen offset:0 sc1\n" \
    "s_waitcnt vmcnt(15)\n" \
    "v_mfma_f32_32x32x16_bf16 v[64:79], a[136:139], v[136:139], v[64:79]\n" \
    "v_mfma_f32_32x32x16_bf16 v[80:95], a[140:143], v[136:139], v[80:95]\n" \
    "s_waitcnt vmcnt(14)\n" \
    "v_mfma_f32_32x32x16_bf16 v[96:111], a[136:139], v[140:143], v[96:111]\n" \
    "v_mfma_f32_32x32x16_bf16 v[112:127], a[140:143], v[140:143], v[112:127]\n" \
    "buffer_load_dwordx4 v[136:139], v24, s[44:47], s61 offen offset:1024 sc1\n" \
    "buffer_load_dwordx4 v[140:143], v24, s[44:47], s62 offen offset:1024 sc1\n" \
    "s_waitcnt vmcnt(15)\n" \
    "v_mfma_f32_32x32x16_bf16 v[64:79], a[144:147], v[144:147], v[64:79]\n" \
    "ds_read_b64_tr_b16 a[128:129], v18 offset:32768\n" \
    "ds_read_b64_tr_b16 a[130:131], v19 offset:32768\n" \
    "ds_read_b64_tr_b16 a[132:133], v3 offset:32768\n" \
    "v_mfma_f32_32x32x16_bf16 v[80:95], a[148:151], v[144:147], v[80:95]\n" \
    "ds_read_b64_tr_b16 a[134:135], v4 offset:32768\n" \
    "ds_read_b64_tr_b16 a[136:137], v18 offset:34816\n" \
    "ds_read_b64_tr_b16 a[138:139], v19 offset:34816\n" \
    "s_waitcnt vmcnt(14)\n" \
    "v_mfma_f32_32x32x16_bf16 v[96:111], a[144:147], v[148:151], v[96:111]\n" \
    "ds_read_b64_tr_b16 a[140:141], v3 offset:34816\n" \
    "ds_read_b64_tr_b16 a[142:143], v4 offset:34816\n" \
    "v_mfma_f32_32x32x16_bf16 v[112:127], a[148:151], v[148:151], v[112:127]\n" \
    "buffer_load_dwordx4 v[144:147], v24, s[44:47], s61 offen offset:2048 sc1\n" \
    "buffer_load_dwordx4 v[148:151], v24, s[44:47], s62 offen offset:2048 sc1\n" \
    "s_waitcnt vmcnt(15)\n" \
    "v_mfma_f32_32x32x16_bf16 v[64:79], a[152:155], v[152:155], v[64:79]\n" \
    "s_add_u32 m0, s73, 98304\n" \
    "s_add_u32 s75, s79, 958464\n" \
    "s_waitcnt lgkmcnt(0)\n" \
    "buffer_load_dwordx4 v5, s[28:31], s75 offen offset:0 lds\n" \
    "v_mfma_f32_32x32x16_bf16 v[80:95], a[156:159], v[152:155], v[80:95]\n" \
    "s_waitcnt vmcnt(15)\n" \
    "v_mfma_f32_32x32x16_bf16 v[96:111], a[152:155], v[156:159], v[96:111]\n" \
    "buffer_load_dwordx4 v6, s[28:31], s75 offen offset:1024 lds\n" \
    "v_mfma_f32_32x32x16_bf16 v[112:127], a[156:159], v[156:159], v[112:127]\n" \
    "buffer_load_dwordx4 v[152:155], v24, s[44:47], s61 offen offset:3072 sc1\n" \
    "buffer_load_dwordx4 v[156:159], v24, s[44:47], s62 offen offset:3072 sc1\n" \
    "s_waitcnt vmcnt(17)\n" \
    "v_mfma_f32_32x32x16_bf16 v[64:79], a[160:163], v[160:163], v[64:79]\n" \
    "s_barrier\n" \
    "ds_read_b64_tr_b16 a[144:145], v18 offset:49152\n" \
    "ds_read_b64_tr_b16 a[146:147], v19 offset:49152\n" \
    "ds_read_b64_tr_b16 a[148:149], v3 offset:49152\n" \
    "v_mfma_f32_32x32x16_bf16 v[80:95], a[164:167], v[160:163], v[80:95]\n" \
    "ds_read_b64_tr_b16 a[150:151], v4 offset:49152\n" \
    "ds_read_b64_tr_b16 a[152:153], v18 offset:51200\n" \
    "ds_read_b64_tr_b16 a[154:155], v19 offset:51200\n" \
    "s_waitcnt vmcnt(16)\n" \
    "v_mfma_f32_32x32x16_bf16 v[96:111], a[160:163], v[164:167], v[96:111]\n" \
    "ds_read_b64_tr_b16 a[156:157], v3 offset:51200\n" \
    "ds_read_b64_tr_b16 a[158:159], v4 offset:51200\n" \
    "v_mfma_f32_32x32x16_bf16 v[112:127], a[164:167], v[164:167], v[112:127]\n" \
    "s_add_u32 s61, s61, 4096\n" \
    "s_add_u32 s62, s62, 4096\n" \
    "buffer_load_dwordx4 v[160:163], v24, s[44:47], s61 offen offset:0 sc1\n" \
    "buffer_load_dwordx4 v[164:167], v24, s[44:47], s62 offen offset:0 sc1\n" \
    "s_waitcnt vmcnt(17)\n" \
    "v_mfma_f32_32x32x16_bf16 v[64:79], a[168:171], v[168:171], v[64:79]\n" \
    "buffer_load_dwordx4 v7, s[28:31], s75 offen offset:2048 lds\n" \
    "v_mfma_f32_32x32x16_bf16 v[80:95], a[172:175], v[168:171], v[80:95]\n" \
    "s_waitcnt vmcnt(17)\n" \
    "v_mfma_f32_32x32x16_bf16 v[96:111], a[168:171], v[172:175], v[96:111]\n" \
    "buffer_load_dwordx4 v8, s[28:31], s75 offen offset:3072 lds\n" \
    "v_mfma_f32_32x32x16_bf16 v[112:127], a[172:175], v[172:175], v[112:127]\n" \
    "buffer_load_dwordx4 v[168:171], v24, s[44:47], s61 offen offset:1024 sc1\n" \
    "buffer_load_dwordx4 v[172:175], v24, s[44:47], s62 offen offset:1024 sc1\n" \
    "s_waitcnt vmcnt(19)\n" \
    "v_mfma_f32_32x32x16_bf16 v[64:79], a[176:179], v[176:179], v[64:79]\n" \
    "ds_read_b64_tr_b16 a[160:161], v16 offset:0\n" \
    "ds_read_b64_tr_b16 a[162:163], v17 offset:0\n" \
    "ds_read_b64_tr_b16 a[164:165], v1 offset:0\n" \
    "v_mfma_f32_32x32x16_bf16 v[80:95], a[180:183], v[176:179], v[80:95]\n" \
    "ds_read_b64_tr_b16 a[166:167], v2 offset:0\n" \
    "ds_read_b64_tr_b16 a[168:169], v16 offset:2048\n" \
    "ds_read_b64_tr_b16 a[170:171], v17 offset:2048\n" \
    "s_waitcnt vmcnt(18)\n" \
    "v_mfma_f32_32x32x16_bf16 v[96:111], a[176:179], v[180:183], v[96:111]\n" \
    "ds_read_b64_tr_b16 a[172:173], v1 offset:2048\n" \
    "ds_read_b64_tr_b16 a[174:175], v2 offset:2048\n" \
    "v_mfma_f32_32x32x16_bf16 v[112:127], a[180:183], v[180:183], v[112:127]\n" \
    "buffer_load_dwordx4 v[176:179], v24, s[44:47], s61 offen offset:2048 sc1\n" \
    "buffer_load_dwordx4 v[180:183], v24, s[44:47], s62 offen offset:2048 sc1\n" \
    "s_waitcnt vmcnt(19)\n" \
    "v_mfma_f32_32x32x16_bf16 v[64:79], a[184:187], v[184:187], v[64:79]\n" \
    "s_add_u32 m0, s73, 114688\n" \
    "s_add_u32 s75, s79, 995328\n" \
    "s_waitcnt lgkmcnt(8)\n" \
    "buffer_load_dwordx4 v5, s[28:31], s75 offen offset:0 lds\n" \
    "v_mfma_f32_32x32x16_bf16 v[80:95], a[188:191], v[184:187], v[80:95]\n" \
    "s_waitcnt vmcnt(19)\n" \
    "v_mfma_f32_32x32x16_bf16 v[96:111], a[184:187], v[188:191], v[96:111]\n" \
    "buffer_load_dwordx4 v6, s[28:31], s75 offen offset:1024 lds\n" \
    "v_mfma_f32_32x32x16_bf16 v[112:127], a[188:191], v[188:191], v[112:127]\n" \
    "buffer_load_dwordx4 v[184:187], v24, s[44:47], s61 offen offset:3072 sc1\n" \
    "buffer_load_dwordx4 v[188:191], v24, s[44:47], s62 offen offset:3072 sc1\n" \
    "s_waitcnt vmcnt(21)\n" \
    "v_mfma_f32_32x32x16_bf16 v[64:79], a[192:195], v[128:131], v[64:79]\n" \
    "ds_read_b64_tr_b16 a[176:177], v16 offset:16384\n" \
    "ds_read_b64_tr_b16 a[178:179], v17 offset:16384\n" \
    "ds_read_b64_tr_b16 a[180:181], v1 offset:16384\n" \
    "v_mfma_f32_32x32x16_bf16 v[80:95], a[196:199], v[128:131], v[80:95]\n" \
    "ds_read_b64_tr_b16 a[182:183], v2 offset:16384\n" \
    "ds_read_b64_tr_b16 a[184:185], v16 offset:18432\n" \
    "ds_read_b64_tr_b16 a[186:187], v17 offset:18432\n" \
    "s_waitcnt vmcnt(20)\n" \
    "v_mfma_f32_32x32x16_bf16 v[96:111], a[192:195], v[132:135], v[96:111]\n" \
    "ds_read_b64_tr_b16 a[188:189], v1 offset:18432\n" \
    "ds_read_b64_tr_b16 a[190:191], v2 offset:18432\n" \
    "v_mfma_f32_32x32x16_bf16 v[112:127], a[196:199], v[132:135], v[112:127]\n" \
    "s_add_u32 s61, s61, 4096\n" \
    "s_add_u32 s62, s62, 4096\n" \
    "buffer_load_dwordx4 v[128:131], v24, s[44:47], s61 offen offset:0 sc1\n" \
    "buffer_load_dwordx4 v[132:135], v24, s[44:47], s62 offen offset:0 sc1\n" \
    "s_waitcnt vmcnt(21)\n" \
    "v_mfma_f32_32x32x16_bf16 v[64:79], a[200:203], v[136:139], v[64:79]\n" \
    "buffer_load_dwordx4 v7, s[28:31], s75 offen offset:2048 lds\n" \
    "v_mfma_f32_32x32x16_bf16 v[80:95], a[204:207], v[136:139], v[80:95]\n" \
    "s_waitcnt vmcnt(21)\n" \
    "v_mfma_f32_32x32x16_bf16 v[96:111], a[200:203], v[140:143], v[96:111]\n" \
    "buffer_load_dwordx4 v8, s[28:31], s75 offen offset:3072 lds\n" \
    "v_mfma_f32_32x32x16_bf16 v[112:127], a[204:207], v[140:143], v[112:127]\n" \
    "buffer_load_dwordx4 v[136:139], v24, s[44:47], s61 offen offset:1024 sc1\n" \
    "buffer_load_dwordx4 v[140:143], v24, s[44:47], s62 offen offset:1024 sc1\n" \
    "s_waitcnt vmcnt(23)\n" \
    "v_mfma_f32_32x32x16_bf16 v[64:79], a[208:211], v[144:147], v[64:79]\n" \
    "ds_read_b64_tr_b16 a[192:193], v16 offset:32768\n" \
    "ds_read_b64_tr_b16 a[194:195], v17 offset:32768\n" \
    "ds_read_b64_tr_b16 a[196:197], v1 offset:32768\n" \
    "v_mfma_f32_32x32x16_bf16 v[80:95], a[212:215], v[144:147], v[80:95]\n" \
    "ds_read_b64_tr_b16 a[198:199], v2 offset:32768\n" \
    "ds_read_b64_tr_b16 a[200:201], v16 offset:34816\n" \
    "ds_read_b64_tr_b16 a[202:203], v17 offset:34816\n" \
    "s_waitcnt vmcnt(22)\n" \
    "v_mfma_f32_32x32x16_bf16 v[96:111], a[208:211], v[148:151], v[96:111]\n" \
    "ds_read_b64_tr_b16 a[204:205], v1 offset:34816\n" \
    "ds_read_b64_tr_b16 a[206:207], v2 offset:34816\n" \
    "v_mfma_f32_32x32x16_bf16 v[112:127], a[212:215], v[148:151], v[112:127]\n" \
    "buffer_load_dwordx4 v[144:147], v24, s[44:47], s61 offen offset:2048 sc1\n" \
    "buffer_load_dwordx4 v[148:151], v24, s[44:47], s62 offen offset:2048 sc1\n" \
    "s_waitcnt vmcnt(21)\n" \
    "v_mfma_f32_32x32x16_bf16 v[64:79], a[216:219], v[152:155], v[64:79]\n" \
    "s_add_u32 m0, s73, 0\n" \
    "s_add_u32 s75, s79, 1032192\n" \
    "s_waitcnt lgkmcnt(8)\n" \
    "buffer_load_dwordx4 v5, s[28:31], s75 offen offset:0 lds\n" \
    "v_mfma_f32_32x32x16_bf16 v[80:95], a[220:223], v[152:155], v[80:95]\n" \
    "s_waitcnt vmcnt(21)\n" \
    "v_mfma_f32_32x32x16_bf16 v[96:111], a[216:219], v[156:159], v[96:111]\n" \
    "buffer_load_dwordx4 v6, s[28:31], s75 offen offset:1024 lds\n" \
    "v_mfma_f32_32x32x16_bf16 v[112:127], a[220:223], v[156:159], v[112:127]\n" \
    "buffer_load_dwordx4 v[152:155], v24, s[44:47], s61 offen offset:3072 sc1\n" \
    "buffer_load_dwordx4 v[156:159], v24, s[44:47], s62 offen offset:3072 sc1\n" \
    "s_waitcnt vmcnt(23)\n" \
    "v_mfma_f32_32x32x16_bf16 v[64:79], a[224:227], v[160:163], v[64:79]\n" \
    "ds_read_b64_tr_b16 a[208:209], v16 offset:49152\n" \
    "ds_read_b64_tr_b16 a[210:211], v17 offset:49152\n" \
    "ds_read_b64_tr_b16 a[212:213], v1 offset:49152\n" \
    "v_mfma_f32_32x32x16_bf16 v[80:95], a[228:231], v[160:163], v[80:95]\n" \
    "ds_read_b64_tr_b16 a[214:215], v2 offset:49152\n" \
    "ds_read_b64_tr_b16 a[216:217], v16 offset:51200\n" \
    "ds_read_b64_tr_b16 a[218:219], v17 offset:51200\n" \
    "s_waitcnt vmcnt(22)\n" \
    "v_mfma_f32_32x32x16_bf16 v[96:111], a[224:227], v[164:167], v[96:111]\n" \
    "ds_read_b64_tr_b16 a[220:221], v1 offset:51200\n" \
    "ds_read_b64_tr_b16 a[222:223], v2 offset:51200\n" \
    "v_mfma_f32_32x32x16_bf16 v[112:127], a[228:231], v[164:167], v[112:127]\n" \
    "s_add_u32 s61, s61, 4096\n" \
    "s_add_u32 s62, s62, 4096\n" \
    "buffer_load_dwordx4 v[160:163], v24, s[44:47], s61 offen offset:0 sc1\n" \
    "buffer_load_dwordx4 v[164:167], v24, s[44:47], s62 offen offset:0 sc1\n" \
    "s_waitcnt vmcnt(21)\n" \
    "v_mfma_f32_32x32x16_bf16 v[64:79], a[232:235], v[168:171], v[64:79]\n" \
    "buffer_load_dwordx4 v7, s[28:31], s75 offen offset:2048 lds\n" \
    "v_mfma_f32_32x32x16_bf16 v[80:95], a[236:239], v[168:171], v[80:95]\n" \
    "s_waitcnt vmcnt(21)\n" \
    "v_mfma_f32_32x32x16_bf16 v[96:111], a[232:235], v[172:175], v[96:111]\n" \
    "buffer_load_dwordx4 v8, s[28:31], s75 offen offset:3072 lds\n" \
    "v_mfma_f32_32x32x16_bf16 v[112:127], a[236:239], v[172:175], v[112:127]\n" \
    "buffer_load_dwordx4 v[168:171], v24, s[44:47], s61 offen offset:1024 sc1\n" \
    "buffer_load_dwordx4 v[172:175], v24, s[44:47], s62 offen offset:1024 sc1\n" \
    "s_waitcnt vmcnt(23)\n" \
    "v_mfma_f32_32x32x16_bf16 v[64:79], a[240:243], v[176:179], v[64:79]\n" \
    "s_add_u32 m0, s73, 16384\n" \
    "s_add_u32 s75, s79, 1069056\n" \
    "buffer_load_dwordx4 v5, s[28:31], s75 offen offset:0 lds\n" \
    "v_mfma_f32_32x32x16_bf16 v[80:95], a[244:247], v[176:179], v[80:95]\n" \
    "s_waitcnt vmcnt(23)\n" \
    "v_mfma_f32_32x32x16_bf16 v[96:111], a[240:243], v[180:183], v[96:111]\n" \
    "buffer_load_dwordx4 v6, s[28:31], s75 offen offset:1024 lds\n" \
    "v_mfma_f32_32x32x16_bf16 v[112:127], a[244:247], v[180:183], v[112:127]\n" \
    "buffer_load_dwordx4 v[176:179], v24, s[44:47], s61 offen offset:2048 sc1\n" \
    "buffer_load_dwordx4 v[180:183], v24, s[44:47], s62 offen offset:2048 sc1\n" \
    "s_waitcnt vmcnt(23)\n" \
    "v_mfma_f32_32x32x16_bf16 v[64:79], a[248:251], v[184:187], v[64:79]\n" \
    "buffer_load_dwordx4 v7, s[28:31], s75 offen offset:2048 lds\n" \
    "v_mfma_f32_32x32x16_bf16 v[80:95], a[252:255], v[184:187], v[80:95]\n" \
    "s_waitcnt vmcnt(23)\n" \
    "v_mfma_f32_32x32x16_bf16 v[96:111], a[248:251], v[188:191], v[96:111]\n" \
    "buffer_load_dwordx4 v8, s[28:31], s75 offen offset:3072 lds\n" \
    "v_mfma_f32_32x32x16_bf16 v[112:127], a[252:255], v[188:191], v[112:127]\n" \
    "buffer_load_dwordx4 v[184:187], v24, s[44:47], s61 offen offset:3072 sc1\n" \
    "buffer_load_dwordx4 v[188:191], v24, s[44:47], s62 offen offset:3072 sc1\n" \
    "s_waitcnt vmcnt(25)\n" \
    "v_mfma_f32_32x32x16_bf16 v[64:79], a[0:3], v[128:131], v[64:79]\n" \
    "s_add_u32 m0, s73, 32768\n" \
    "s_add_u32 s75, s79, 1105920\n" \
    "s_waitcnt lgkmcnt(0)\n" \
    "buffer_load_dwordx4 v5, s[28:31], s75 offen offset:0 lds\n" \
    "v_mfma_f32_32x32x16_bf16 v[80:95], a[4:7], v[128:131], v[80:95]\n" \
    "s_waitcnt vmcnt(25)\n" \
    "v_mfma_f32_32x32x16_bf16 v[96:111], a[0:3], v[132:135], v[96:111]\n" \
    "buffer_load_dwordx4 v6, s[28:31], s75 offen offset:1024 lds\n" \
    "v_mfma_f32_32x32x16_bf16 v[112:127], a[4:7], v[132:135], v[112:127]\n" \
    "s_add_u32 s61, s61, 4096\n" \
    "s_add_u32 s62, s62, 4096\n" \
    "buffer_load_dwordx4 v[128:131], v24, s[44:47], s61 offen offset:0 sc1\n" \
    "buffer_load_dwordx4 v[132:135], v24, s[44:47], s62 offen offset:0 sc1\n" \
    "s_waitcnt vmcnt(25)\n" \
    "v_mfma_f32_32x32x16_bf16 v[64:79], a[8:11], v[136:139], v[64:79]\n" \
    "buffer_load_dwordx4 v7, s[28:31], s75 offen offset:2048 lds\n" \
    "v_mfma_f32_32x32x16_bf16 v[80:95], a[12:15], v[136:139], v[80:95]\n" \
    "s_waitcnt vmcnt(25)\n" \
    "v_mfma_f32_32x32x16_bf16 v[96:111], a[8:11], v[140:143], v[96:111]\n" \
    "buffer_load_dwordx4 v8, s[28:31], s75 offen offset:3072 lds\n" \
    "v_mfma_f32_32x32x16_bf16 v[112:127], a[12:15], v[140:143], v[112:127]\n" \
    "buffer_load_dwordx4 v[136:139], v24, s[44:47], s61 offen offset:1024 sc1\n" \
    "buffer_load_dwordx4 v[140:143], v24, s[44:47], s62 offen offset:1024 sc1\n" \
    "s_waitcnt vmcnt(27)\n" \
    "v_mfma_f32_32x32x16_bf16 v[64:79], a[16:19], v[144:147], v[64:79]\n" \
    "s_add_u32 m0, s73, 49152\n" \
    "s_add_u32 s75, s79, 1142784\n" \
    "buffer_load_dwordx4 v5, s[28:31], s75 offen offset:0 lds\n" \
    "v_mfma_f32_32x32x16_bf16 v[80:95], a[20:23], v[144:147], v[80:95]\n" \
    "s_waitcnt vmcnt(27)\n" \
    "v_mfma_f32_32x32x16_bf16 v[96:111], a[16:19], v[148:151], v[96:111]\n" \
    "buffer_load_dwordx4 v6, s[28:31], s75 offen offset:1024 lds\n" \
    "v_mfma_f32_32x32x16_bf16 v[112:127], a[20:23], v[148:151], v[112:127]\n" \
    "buffer_load_dwordx4 v[144:147], v24, s[44:47], s61 offen offset:2048 sc1\n" \
    "buffer_load_dwordx4 v[148:151], v24, s[44:47], s62 offen offset:2048 sc1\n" \
    "s_waitcnt vmcnt(27)\n" \
    "v_mfma_f32_32x32x16_bf16 v[64:79], a[24:27], v[152:155], v[64:79]\n" \
    "buffer_load_dwordx4 v7, s[28:31], s75 offen offset:2048 lds\n" \
    "v_mfma_f32_32x32x16_bf16 v[80:95], a[28:31], v[152:155], v[80:95]\n" \
    "s_waitcnt vmcnt(27)\n" \
    "v_mfma_f32_32x32x16_bf16 v[96:111], a[24:27], v[156:159], v[96:111]\n" \
    "buffer_load_dwordx4 v8, s[28:31], s75 offen offset:3072 lds\n" \
    "v_mfma_f32_32x32x16_bf16 v[112:127], a[28:31], v[156:159], v[112:127]\n" \
    "buffer_load_dwordx4 v[152:155], v24, s[44:47], s61 offen offset:3072 sc1\n" \
    "buffer_load_dwordx4 v[156:159], v24, s[44:47], s62 offen offset:3072 sc1\n" \
    "s_waitcnt vmcnt(29)\n" \
    "v_mfma_f32_32x32x16_bf16 v[64:79], a[32:35], v[160:163], v[64:79]\n" \
    "v_mfma_f32_32x32x16_bf16 v[80:95], a[36:39], v[160:163], v[80:95]\n" \
    "s_waitcnt vmcnt(28)\n" \
    "v_mfma_f32_32x32x16_bf16 v[96:111], a[32:35], v[164:167], v[96:111]\n" \
    "v_mfma_f32_32x32x16_bf16 v[112:127], a[36:39], v[164:167], v[112:127]\n" \
    "s_add_u32 s61, s61, 4096\n" \
    "s_add_u32 s62, s62, 4096\n" \
    "buffer_load_dwordx4 v[160:163], v24, s[44:47], s61 offen offset:0 sc1\n" \
    "buffer_load_dwordx4 v[164:167], v24, s[44:47], s62 offen offset:0 sc1\n" \
    "s_waitcnt vmcnt(27)\n" \
    "v_mfma_f32_32x32x16_bf16 v[64:79], a[40:43], v[168:171], v[64:79]\n" \
    "v_mfma_f32_32x32x16_bf16 v[80:95], a[44:47], v[168:171], v[80:95]\n" \
    "s_waitcnt vmcnt(26)\n" \
    "v_mfma_f32_32x32x16_bf16 v[96:111], a[40:43], v[172:175], v[96:111]\n" \
    "v_mfma_f32_32x32x16_bf16 v[112:127], a[44:47], v[172:175], v[112:127]\n" \
    "buffer_load_dwordx4 v[168:171], v24, s[44:47], s61 offen offset:1024 sc1\n" \
    "buffer_load_dwordx4 v[172:175], v24, s[44:47], s62 offen offset:1024 sc1\n" \
    "s_waitcnt vmcnt(25)\n" \
    "v_mfma_f32_32x32x16_bf16 v[64:79], a[48:51], v[176:179], v[64:79]\n" \
    "v_mfma_f32_32x32x16_bf16 v[80:95], a[52:55], v[176:179], v[80:95]\n" \
    "s_waitcnt vmcnt(24)\n" \
    "v_mfma_f32_32x32x16_bf16 v[96:111], a[48:51], v[180:183], v[96:111]\n" \
    "v_mfma_f32_32x32x16_bf16 v[112:127], a[52:55], v[180:183], v[112:127]\n" \
    "buffer_load_dwordx4 v[176:179], v24, s[44:47], s61 offen offset:2048 sc1\n" \
    "buffer_load_dwordx4 v[180:183], v24, s[44:47], s62 offen offset:2048 sc1\n" \
    "s_waitcnt vmcnt(23)\n" \
    "v_mfma_f32_32x32x16_bf16 v[64:79], a[56:59], v[184:187], v[64:79]\n" \
    "v_mfma_f32_32x32x16_bf16 v[80:95], a[60:63], v[184:187], v[80:95]\n" \
    "s_waitcnt vmcnt(22)\n" \
    "v_mfma_f32_32x32x16_bf16 v[96:111], a[56:59], v[188:191], v[96:111]\n" \
    "v_mfma_f32_32x32x16_bf16 v[112:127], a[60:63], v[188:191], v[112:127]\n" \
    "buffer_load_dwordx4 v[184:187], v24, s[44:47], s61 offen offset:3072 sc1\n" \
    "buffer_load_dwordx4 v[188:191], v24, s[44:47], s62 offen offset:3072 sc1\n" \
    "s_waitcnt vmcnt(21)\n" \
    "v_mfma_f32_32x32x16_bf16 v[64:79], a[64:67], v[128:131], v[64:79]\n" \
    "v_mfma_f32_32x32x16_bf16 v[80:95], a[68:71], v[128:131], v[80:95]\n" \
    "s_waitcnt vmcnt(20)\n" \
    "v_mfma_f32_32x32x16_bf16 v[96:111], a[64:67], v[132:135], v[96:111]\n" \
    "v_mfma_f32_32x32x16_bf16 v[112:127], a[68:71], v[132:135], v[112:127]\n" \
    "s_add_u32 s61, s61, 4096\n" \
    "s_add_u32 s62, s62, 4096\n" \
    "buffer_load_dwordx4 v[128:131], v24, s[44:47], s61 offen offset:0 sc1\n" \
    "buffer_load_dwordx4 v[132:135], v24, s[44:47], s62 offen offset:0 sc1\n" \
    "s_waitcnt vmcnt(19)\n" \
    "v_mfma_f32_32x32x16_bf16 v[64:79], a[72:75], v[136:139], v[64:79]\n" \
    "v_mfma_f32_32x32x16_bf16 v[80:95], a[76:79], v[136:139], v[80:95]\n" \
    "s_waitcnt vmcnt(18)\n" \
    "v_mfma_f32_32x32x16_bf16 v[96:111], a[72:75], v[140:143], v[96:111]\n" \
    "v_mfma_f32_32x32x16_bf16 v[112:127], a[76:79], v[140:143], v[112:127]\n" \
    "buffer_load_dwordx4 v[136:139], v24, s[44:47], s61 offen offset:1024 sc1\n" \
    "buffer_load_dwordx4 v[140:143], v24, s[44:47], s62 offen offset:1024 sc1\n" \
    "s_waitcnt vmcnt(17)\n" \
    "v_mfma_f32_32x32x16_bf16 v[64:79], a[80:83], v[144:147], v[64:79]\n" \
    "v_mfma_f32_32x32x16_bf16 v[80:95], a[84:87], v[144:147], v[80:95]\n" \
    "s_waitcnt vmcnt(16)\n" \
    "v_mfma_f32_32x32x16_bf16 v[96:111], a[80:83], v[148:151], v[96:111]\n" \
    "v_mfma_f32_32x32x16_bf16 v[112:127], a[84:87], v[148:151], v[112:127]\n" \
    "buffer_load_dwordx4 v[144:147], v24, s[44:47], s61 offen offset:2048 sc1\n" \
    "buffer_load_dwordx4 v[148:151], v24, s[44:47], s62 offen offset:2048 sc1\n" \
    "s_waitcnt vmcnt(15)\n" \
    "v_mfma_f32_32x32x16_bf16 v[64:79], a[88:91], v[152:155], v[64:79]\n" \
    "v_mfma_f32_32x32x16_bf16 v[80:95], a[92:95], v[152:155], v[80:95]\n" \
    "s_waitcnt vmcnt(14)\n" \
    "v_mfma_f32_32x32x16_bf16 v[96:111], a[88:91], v[156:159], v[96:111]\n" \
    "v_mfma_f32_32x32x16_bf16 v[112:127], a[92:95], v[156:159], v[112:127]\n" \
    "buffer_load_dwordx4 v[152:155], v24, s[44:47], s61 offen offset:3072 sc1\n" \
    "buffer_load_dwordx4 v[156:159], v24, s[44:47], s62 offen offset:3072 sc1\n" \
    "s_waitcnt vmcnt(15)\n" \
    "v_mfma_f32_32x32x16_bf16 v[64:79], a[96:99], v[160:163], v[64:79]\n" \
    "v_mfma_f32_32x32x16_bf16 v[80:95], a[100:103], v[160:163], v[80:95]\n" \
    "s_waitcnt vmcnt(14)\n" \
    "v_mfma_f32_32x32x16_bf16 v[96:111], a[96:99], v[164:167], v[96:111]\n" \
    "v_mfma_f32_32x32x16_bf16 v[112:127], a[100:103], v[164:167], v[112:127]\n" \
    "s_add_u32 s61, s61, 4096\n" \
    "s_add_u32 s62, s62, 4096\n" \
    "buffer_load_dwordx4 v[160:163], v24, s[44:47], s61 offen offset:0 sc1\n" \
    "buffer_load_dwordx4 v[164:167], v24, s[44:47], s62 offen offset:0 sc1\n" \
    "s_waitcnt vmcnt(15)\n" \
    "v_mfma_f32_32x32x16_bf16 v[64:79], a[104:107], v[168:171], v[64:79]\n" \
    "v_mfma_f32_32x32x16_bf16 v[80:95], a[108:111], v[168:171], v[80:95]\n" \
    "s_waitcnt vmcnt(14)\n" \
    "v_mfma_f32_32x32x16_bf16 v[96:111], a[104:107], v[172:175], v[96:111]\n" \
    "v_mfma_f32_32x32x16_bf16 v[112:127], a[108:111], v[172:175], v[112:127]\n" \
    "buffer_load_dwordx4 v[168:171], v24, s[44:47], s61 offen offset:1024 sc1\n" \
    "buffer_load_dwordx4 v[172:175], v24, s[44:47], s62 offen offset:1024 sc1\n" \
    "s_waitcnt vmcnt(15)\n" \
    "v_mfma_f32_32x32x16_bf16 v[64:79], a[112:115], v[176:179], v[64:79]\n" \
    "v_mfma_f32_32x32x16_bf16 v[80:95], a[116:119], v[176:179], v[80:95]\n" \
    "s_waitcnt vmcnt(14)\n" \
    "v_mfma_f32_32x32x16_bf16 v[96:111], a[112:115], v[180:183], v[96:111]\n" \
    "v_mfma_f32_32x32x16_bf16 v[112:127], a[116:119], v[180:183], v[112:127]\n" \
    "buffer_load_dwordx4 v[176:179], v24, s[44:47], s61 offen offset:2048 sc1\n" \
    "buffer_load_dwordx4 v[180:183], v24, s[44:47], s62 offen offset:2048 sc1\n" \
    "s_waitcnt vmcnt(15)\n" \
    "v_mfma_f32_32x32x16_bf16 v[64:79], a[120:123], v[184:187], v[64:79]\n" \
    "v_mfma_f32_32x32x16_bf16 v[80:95], a[124:127], v[184:187], v[80:95]\n" \
    "s_waitcnt vmcnt(14)\n" \
    "v_mfma_f32_32x32x16_bf16 v[96:111], a[120:123], v[188:191], v[96:111]\n" \
    "v_mfma_f32_32x32x16_bf16 v[112:127], a[124:127], v[188:191], v[112:127]\n" \
    "buffer_load_dwordx4 v[184:187], v24, s[44:47], s61 offen offset:3072 sc1\n" \
    "buffer_load_dwordx4 v[188:191], v24, s[44:47], s62 offen offset:3072 sc1\n" \
    "s_waitcnt vmcnt(15)\n" \
    "v_mfma_f32_32x32x16_bf16 v[64:79], a[128:131], v[128:131], v[64:79]\n" \
    "v_mfma_f32_32x32x16_bf16 v[80:95], a[132:135], v[128:131], v[80:95]\n" \
    "s_waitcnt vmcnt(14)\n" \
    "v_mfma_f32_32x32x16_bf16 v[96:111], a[128:131], v[132:135], v[96:111]\n" \
    "v_mfma_f32_32x32x16_bf16 v[112:127], a[132:135], v[132:135], v[112:127]\n" \
    "s_add_u32 s61, s61, 4096\n" \
    "s_add_u32 s62, s62, 4096\n" \
    "buffer_load_dwordx4 v[128:131], v24, s[44:47], s61 offen offset:0 sc1\n" \
    "buffer_load_dwordx4 v[132:135], v24, s[44:47], s62 offen offset:0 sc1\n" \
    "s_waitcnt vmcnt(15)\n" \
    "v_mfma_f32_32x32x16_bf16 v[64:79], a[136:139], v[136:139], v[64:79]\n" \
    "v_mfma_f32_32x32x16_bf16 v[80:95], a[140:143], v[136:139], v[80:95]\n" \
    "s_waitcnt vmcnt(14)\n" \
    "v_mfma_f32_32x32x16_bf16 v[96:111], a[136:139], v[140:143], v[96:111]\n" \
    "v_mfma_f32_32x32x16_bf16 v[112:127], a[140:143], v[140:143], v[112:127]\n" \
    "buffer_load_dwordx4 v[136:139], v24, s[44:47], s61 offen offset:1024 sc1\n" \
    "buffer_load_dwordx4 v[140:143], v24, s[44:47], s62 offen offset:1024 sc1\n" \
    "s_waitcnt vmcnt(15)\n" \
    "v_mfma_f32_32x32x16_bf16 v[64:79], a[144:147], v[144:147], v[64:79]\n" \
    "v_mfma_f32_32x32x16_bf16 v[80:95], a[148:151], v[144:147], v[80:95]\n" \
    "s_waitcnt vmcnt(14)\n" \
    "v_mfma_f32_32x32x16_bf16 v[96:111], a[144:147], v[148:151], v[96:111]\n" \
    "v_mfma_f32_32x32x16_bf16 v[112:127], a[148:151], v[148:151], v[112:127]\n" \
    "buffer_load_dwordx4 v[144:147], v24, s[44:47], s61 offen offset:2048 sc1\n" \
    "buffer_load_dwordx4 v[148:151], v24, s[44:47], s62 offen offset:2048 sc1\n" \
    "s_waitcnt vmcnt(15)\n" \
    "v_mfma_f32_32x32x16_bf16 v[64:79], a[152:155], v[152:155], v[64:79]\n" \
    "v_mfma_f32_32x32x16_bf16 v[80:95], a[156:159], v[152:155], v[80:95]\n" \
    "s_waitcnt vmcnt(14)\n" \
    "v_mfma_f32_32x32x16_bf16 v[96:111], a[152:155], v[156:159], v[96:111]\n" \
    "v_mfma_f32_32x32x16_bf16 v[112:127], a[156:159], v[156:159], v[112:127]\n" \
    "buffer_load_dwordx4 v[152:155], v24, s[44:47], s61 offen offset:3072 sc1\n" \
    "buffer_load_dwordx4 v[156:159], v24, s[44:47], s62 offen offset:3072 sc1\n" \
    "s_waitcnt vmcnt(15)\n" \
    "v_mfma_f32_32x32x16_bf16 v[64:79], a[160:163], v[160:163], v[64:79]\n" \
    "ds_read_b64_tr_b16 a[224:225], v18 offset:0\n" \
    "ds_read_b64_tr_b16 a[226:227], v19 offset:0\n" \
    "ds_read_b64_tr_b16 a[228:229], v3 offset:0\n" \
    "v_mfma_f32_32x32x16_bf16 v[80:95], a[164:167], v[160:163], v[80:95]\n" \
    "ds_read_b64_tr_b16 a[230:231], v4 offset:0\n" \
    "ds_read_b64_tr_b16 a[232:233], v18 offset:2048\n" \
    "ds_read_b64_tr_b16 a[234:235], v19 offset:2048\n" \
    "s_waitcnt vmcnt(14)\n" \
    "v_mfma_f32_32x32x16_bf16 v[96:111], a[160:163], v[164:167], v[96:111]\n" \
    "ds_read_b64_tr_b16 a[236:237], v3 offset:2048\n" \
    "ds_read_b64_tr_b16 a[238:239], v4 offset:2048\n" \
    "v_mfma_f32_32x32x16_bf16 v[112:127], a[164:167], v[164:167], v[112:127]\n" \
    "s_add_u32 s61, s61, 4096\n" \
    "s_add_u32 s62, s62, 4096\n" \
    "buffer_load_dwordx4 v[160:163], v24, s[44:47], s61 offen offset:0 sc1\n" \
    "buffer_load_dwordx4 v[164:167], v24, s[44:47], s62 offen offset:0 sc1\n" \
    "s_waitcnt vmcnt(15)\n" \
    "v_mfma_f32_32x32x16_bf16 v[64:79], a[168:171], v[168:171], v[64:79]\n" \
    "v_mfma_f32_32x32x16_bf16 v[80:95], a[172:175], v[168:171], v[80:95]\n" \
    "s_waitcnt vmcnt(14)\n" \
    "v_mfma_f32_32x32x16_bf16 v[96:111], a[168:171], v[172:175], v[96:111]\n" \
    "v_mfma_f32_32x32x16_bf16 v[112:127], a[172:175], v[172:175], v[112:127]\n" \
    "buffer_load_dwordx4 v[168:171], v24, s[44:47], s61 offen offset:1024 sc1\n" \
    "buffer_load_dwordx4 v[172:175], v24, s[44:47], s62 offen offset:1024 sc1\n" \
    "s_waitcnt vmcnt(15)\n" \
    "v_mfma_f32_32x32x16_bf16 v[64:79], a[176:179], v[176:179], v[64:79]\n" \
    "ds_read_b64_tr_b16 a[240:241], v14 offset:0\n" \
    "ds_read_b64_tr_b16 a[242:243], v15 offset:0\n" \
    "ds_read_b64_tr_b16 a[244:245], v10 offset:0\n" \
    "v_mfma_f32_32x32x16_bf16 v[80:95], a[180:183], v[176:179], v[80:95]\n" \
    "ds_read_b64_tr_b16 a[246:247], v11 offset:0\n" \
    "ds_read_b64_tr_b16 a[248:249], v14 offset:2048\n" \
    "ds_read_b64_tr_b16 a[250:251], v15 offset:2048\n" \
    "s_waitcnt vmcnt(14)\n" \
    "v_mfma_f32_32x32x16_bf16 v[96:111], a[176:179], v[180:183], v[96:111]\n" \
    "ds_read_b64_tr_b16 a[252:253], v10 offset:2048\n" \
    "ds_read_b64_tr_b16 a[254:255], v11 offset:2048\n" \
    "v_mfma_f32_32x32x16_bf16 v[112:127], a[180:183], v[180:183], v[112:127]\n" \
    "buffer_load_dwordx4 v[176:179], v24, s[44:47], s61 offen offset:2048 sc1\n" \
    "buffer_load_dwordx4 v[180:183], v24, s[44:47], s62 offen offset:2048 sc1\n" \
    "s_waitcnt vmcnt(15)\n" \
    "v_mfma_f32_32x32x16_bf16 v[64:79], a[184:187], v[184:187], v[64:79]\n" \
    "v_mfma_f32_32x32x16_bf16 v[80:95], a[188:191], v[184:187], v[80:95]\n" \
    "s_waitcnt vmcnt(14)\n" \
    "v_mfma_f32_32x32x16_bf16 v[96:111], a[184:187], v[188:191], v[96:111]\n" \
    "v_mfma_f32_32x32x16_bf16 v[112:127], a[188:191], v[188:191], v[112:127]\n" \
    "buffer_load_dwordx4 v[184:187], v24, s[44:47], s61 offen offset:3072 sc1\n" \
    "buffer_load_dwordx4 v[188:191], v24, s[44:47], s62 offen offset:3072 sc1\n" \
    "s_waitcnt vmcnt(15)\n" \
    "v_mfma_f32_32x32x16_bf16 v[64:79], a[192:195], v[128:131], v[64:79]\n" \
    "ds_read_b64_tr_b16 a[0:1], v14 offset:16384\n" \
    "ds_read_b64_tr_b16 a[2:3], v15 offset:16384\n" \
    "ds_read_b64_tr_b16 a[4:5], v10 offset:16384\n" \
    "v_mfma_f32_32x32x16_bf16 v[80:95], a[196:199], v[128:131], v[80:95]\n" \
    "ds_read_b64_tr_b16 a[6:7], v11 offset:16384\n" \
    "ds_read_b64_tr_b16 a[8:9], v14 offset:18432\n" \
    "ds_read_b64_tr_b16 a[10:11], v15 offset:18432\n" \
    "s_waitcnt vmcnt(14)\n" \
    "v_mfma_f32_32x32x16_bf16 v[96:111], a[192:195], v[132:135], v[96:111]\n" \
    "ds_read_b64_tr_b16 a[12:13], v10 offset:18432\n" \
    "ds_read_b64_tr_b16 a[14:15], v11 offset:18432\n" \
    "v_mfma_f32_32x32x16_bf16 v[112:127], a[196:199], v[132:135], v[112:127]\n" \
    "s_add_u32 s61, s61, 4096\n" \
    "s_add_u32 s62, s62, 4096\n" \
    "buffer_load_dwordx4 v[128:131], v24, s[44:47], s61 offen offset:0 sc1\n" \
    "buffer_load_dwordx4 v[132:135], v24, s[44:47], s62 offen offset:0 sc1\n" \
    "s_waitcnt vmcnt(15)\n" \
    "v_mfma_f32_32x32x16_bf16 v[64:79], a[200:203], v[136:139], v[64:79]\n" \
    "v_mfma_f32_32x32x16_bf16 v[80:95], a[204:207], v[136:139], v[80:95]\n" \
    "s_waitcnt vmcnt(14)\n" \
    "v_mfma_f32_32x32x16_bf16 v[96:111], a[200:203], v[140:143], v[96:111]\n" \
    "v_mfma_f32_32x32x16_bf16 v[112:127], a[204:207], v[140:143], v[112:127]\n" \
    "buffer_load_dwordx4 v[136:139], v24, s[44:47], s61 offen offset:1024 sc1\n" \
    "buffer_load_dwordx4 v[140:143], v24, s[44:47], s62 offen offset:1024 sc1\n" \
    "s_waitcnt vmcnt(15)\n" \
    "v_mfma_f32_32x32x16_bf16 v[64:79], a[208:211], v[144:147], v[64:79]\n" \
    "ds_read_b64_tr_b16 a[16:17], v18 offset:16384\n" \
    "ds_read_b64_tr_b16 a[18:19], v19 offset:16384\n" \
    "ds_read_b64_tr_b16 a[20:21], v3 offset:16384\n" \
    "v_mfma_f32_32x32x16_bf16 v[80:95], a[212:215], v[144:147], v[80:95]\n" \
    "ds_read_b64_tr_b16 a[22:23], v4 offset:16384\n" \
    "ds_read_b64_tr_b16 a[24:25], v18 offset:18432\n" \
    "ds_read_b64_tr_b16 a[26:27], v19 offset:18432\n" \
    "s_waitcnt vmcnt(14)\n" \
    "v_mfma_f32_32x32x16_bf16 v[96:111], a[208:211], v[148:151], v[96:111]\n" \
    "ds_read_b64_tr_b16 a[28:29], v3 offset:18432\n" \
    "ds_read_b64_tr_b16 a[30:31], v4 offset:18432\n" \
    "v_mfma_f32_32x32x16_bf16 v[112:127], a[212:215], v[148:151], v[112:127]\n" \
    "buffer_load_dwordx4 v[144:147], v24, s[44:47], s61 offen offset:2048 sc1\n" \
    "buffer_load_dwordx4 v[148:151], v24, s[44:47], s62 offen offset:2048 sc1\n" \
    "s_waitcnt vmcnt(15)\n" \
    "v_mfma_f32_32x32x16_bf16 v[64:79], a[216:219], v[152:155], v[64:79]\n" \
    "v_mfma_f32_32x32x16_bf16 v[80:95], a[220:223], v[152:155], v[80:95]\n" \
    "s_waitcnt vmcnt(14)\n" \
    "v_mfma_f32_32x32x16_bf16 v[96:111], a[216:219], v[156:159], v[96:111]\n" \
    "v_mfma_f32_32x32x16_bf16 v[112:127], a[220:223], v[156:159], v[112:127]\n" \
    "buffer_load_dwordx4 v[152:155], v24, s[44:47], s61 offen offset:3072 sc1\n" \
    "buffer_load_dwordx4 v[156:159], v24, s[44:47], s62 offen offset:3072 sc1\n" \
    "s_waitcnt vmcnt(15) lgkmcnt(8)\n" \
    "v_mfma_f32_32x32x16_bf16 v[64:79], a[224:227], v[160:163], v[64:79]\n" \
    "ds_read_b64_tr_b16 a[32:33], v18 offset:32768\n" \
    "ds_read_b64_tr_b16 a[34:35], v19 offset:32768\n" \
    "ds_read_b64_tr_b16 a[36:37], v3 offset:32768\n" \
    "v_mfma_f32_32x32x16_bf16 v[80:95], a[228:231], v[160:163], v[80:95]\n" \
    "ds_read_b64_tr_b16 a[38:39], v4 offset:32768\n" \
    "ds_read_b64_tr_b16 a[40:41], v18 offset:34816\n" \
    "ds_read_b64_tr_b16 a[42:43], v19 offset:34816\n" \
    "s_waitcnt vmcnt(14)\n" \
    "v_mfma_f32_32x32x16_bf16 v[96:111], a[224:227], v[164:167], v[96:111]\n" \
    "ds_read_b64_tr_b16 a[44:45], v3 offset:34816\n" \
    "ds_read_b64_tr_b16 a[46:47], v4 offset:34816\n" \
    "v_mfma_f32_32x32x16_bf16 v[112:127], a[228:231], v[164:167], v[112:127]\n" \
    "s_add_u32 s61, s61, 4096\n" \
    "s_add_u32 s62, s62, 4096\n" \
    "buffer_load_dwordx4 v[160:163], v24, s[44:47], s61 offen offset:0 sc1\n" \
    "buffer_load_dwordx4 v[164:167], v24, s[44:47], s62 offen offset:0 sc1\n" \
    "s_waitcnt vmcnt(15)\n" \
    "v_mfma_f32_32x32x16_bf16 v[64:79], a[232:235], v[168:171], v[64:79]\n" \
    "v_mfma_f32_32x32x16_bf16 v[80:95], a[236:239], v[168:171], v[80:95]\n" \
    "s_waitcnt vmcnt(14)\n" \
    "v_mfma_f32_32x32x16_bf16 v[96:111], a[232:235], v[172:175], v[96:111]\n" \
    "v_mfma_f32_32x32x16_bf16 v[112:127], a[236:239], v[172:175], v[112:127]\n" \
    "buffer_load_dwordx4 v[168:171], v24, s[44:47], s61 offen offset:1024 sc1\n" \
    "buffer_load_dwordx4 v[172:175], v24, s[44:47], s62 offen offset:1024 sc1\n" \
    "s_waitcnt vmcnt(15)\n" \
    "v_mfma_f32_32x32x16_bf16 v[64:79], a[240:243], v[176:179], v[64:79]\n" \
    "ds_read_b64_tr_b16 a[48:49], v18 offset:49152\n" \
    "ds_read_b64_tr_b16 a[50:51], v19 offset:49152\n" \
    "ds_read_b64_tr_b16 a[52:53], v3 offset:49152\n" \
    "v_mfma_f32_32x32x16_bf16 v[80:95], a[244:247], v[176:179], v[80:95]\n" \
    "ds_read_b64_tr_b16 a[54:55], v4 offset:49152\n" \
    "ds_read_b64_tr_b16 a[56:57], v18 offset:51200\n" \
    "ds_read_b64_tr_b16 a[58:59], v19 offset:51200\n" \
    "s_waitcnt vmcnt(14)\n" \
    "v_mfma_f32_32x32x16_bf16 v[96:111], a[240:243], v[180:183], v[96:111]\n" \
    "ds_read_b64_tr_b16 a[60:61], v3 offset:51200\n" \
    "ds_read_b64_tr_b16 a[62:63], v4 offset:51200\n" \
    "v_mfma_f32_32x32x16_bf16 v[112:127], a[244:247], v[180:183], v[112:127]\n" \
    "buffer_load_dwordx4 v[176:179], v24, s[44:47], s61 offen offset:2048 sc1\n" \
    "buffer_load_dwordx4 v[180:183], v24, s[44:47], s62 offen offset:2048 sc1\n" \
    "s_waitcnt vmcnt(15)\n" \
    "v_mfma_f32_32x32x16_bf16 v[64:79], a[248:251], v[184:187], v[64:79]\n" \
    "v_mfma_f32_32x32x16_bf16 v[80:95], a[252:255], v[184:187], v[80:95]\n" \
    "s_waitcnt vmcnt(14)\n" \
    "v_mfma_f32_32x32x16_bf16 v[96:111], a[248:251], v[188:191], v[96:111]\n" \
    "v_mfma_f32_32x32x16_bf16 v[112:127], a[252:255], v[188:191], v[112:127]\n" \
    "buffer_load_dwordx4 v[184:187], v24, s[44:47], s61 offen offset:3072 sc1\n" \
    "buffer_load_dwordx4 v[188:191], v24, s[44:47], s62 offen offset:3072 sc1\n" \
    "s_waitcnt vmcnt(15)\n" \
    "v_mfma_f32_32x32x16_bf16 v[64:79], a[0:3], v[128:131], v[64:79]\n" \
    "ds_read_b64_tr_b16 a[64:65], v16 offset:0\n" \
    "ds_read_b64_tr_b16 a[66:67], v17 offset:0\n" \
    "ds_read_b64_tr_b16 a[68:69], v1 offset:0\n" \
    "v_mfma_f32_32x32x16_bf16 v[80:95], a[4:7], v[128:131], v[80:95]\n" \
    "ds_read_b64_tr_b16 a[70:71], v2 offset:0\n" \
    "ds_read_b64_tr_b16 a[72:73], v16 offset:2048\n" \
    "ds_read_b64_tr_b16 a[74:75], v17 offset:2048\n" \
    "s_waitcnt vmcnt(14)\n" \
    "v_mfma_f32_32x32x16_bf16 v[96:111], a[0:3], v[132:135], v[96:111]\n" \
    "ds_read_b64_tr_b16 a[76:77], v1 offset:2048\n" \
    "ds_read_b64_tr_b16 a[78:79], v2 offset:2048\n" \
    "v_mfma_f32_32x32x16_bf16 v[112:127], a[4:7], v[132:135], v[112:127]\n" \
    "s_add_u32 s61, s61, 4096\n" \
    "s_add_u32 s62, s62, 4096\n" \
    "buffer_load_dwordx4 v[128:131], v24, s[44:47], s61 offen offset:0 sc1\n" \
    "buffer_load_dwordx4 v[132:135], v24, s[44:47], s62 offen offset:0 sc1\n" \
    "s_waitcnt vmcnt(15)\n" \
    "v_mfma_f32_32x32x16_bf16 v[64:79], a[8:11], v[136:139], v[64:79]\n" \
    "v_mfma_f32_32x32x16_bf16 v[80:95], a[12:15], v[136:139], v[80:95]\n" \
    "s_waitcnt vmcnt(14)\n" \
    "v_mfma_f32_32x32x16_bf16 v[96:111], a[8:11], v[140:143], v[96:111]\n" \
    "v_mfma_f32_32x32x16_bf16 v[112:127], a[12:15], v[140:143], v[112:127]\n" \
    "buffer_load_dwordx4 v[136:139], v24, s[44:47], s61 offen offset:1024 sc1\n" \
    "buffer_load_dwordx4 v[140:143], v24, s[44:47], s62 offen offset:1024 sc1\n" \
    "s_waitcnt vmcnt(15) lgkmcnt(6)\n" \
    "v_mfma_f32_32x32x16_bf16 v[64:79], a[16:19], v[144:147], v[64:79]\n" \
    "ds_read_b64_tr_b16 a[80:81], v16 offset:16384\n" \
    "ds_read_b64_tr_b16 a[82:83], v17 offset:16384\n" \
    "ds_read_b64_tr_b16 a[84:85], v1 offset:16384\n" \
    "v_mfma_f32_32x32x16_bf16 v[80:95], a[20:23], v[144:147], v[80:95]\n" \
    "ds_read_b64_tr_b16 a[86:87], v2 offset:16384\n" \
    "ds_read_b64_tr_b16 a[88:89], v16 offset:18432\n" \
    "ds_read_b64_tr_b16 a[90:91], v17 offset:18432\n" \
    "s_waitcnt vmcnt(14)\n" \
    "v_mfma_f32_32x32x16_bf16 v[96:111], a[16:19], v[148:151], v[96:111]\n" \
    "ds_read_b64_tr_b16 a[92:93], v1 offset:18432\n" \
    "ds_read_b64_tr_b16 a[94:95], v2 offset:18432\n" \
    "v_mfma_f32_32x32x16_bf16 v[112:127], a[20:23], v[148:151], v[112:127]\n" \
    "buffer_load_dwordx4 v[144:147], v24, s[44:47], s61 offen offset:2048 sc1\n" \
    "buffer_load_dwordx4 v[148:151], v24, s[44:47], s62 offen offset:2048 sc1\n" \
    "s_waitcnt vmcnt(15)\n" \
    "v_mfma_f32_32x32x16_bf16 v[64:79], a[24:27], v[152:155], v[64:79]\n" \
    "v_mfma_f32_32x32x16_bf16 v[80:95], a[28:31], v[152:155], v[80:95]\n" \
    "s_waitcnt vmcnt(14)\n" \
    "v_mfma_f32_32x32x16_bf16 v[96:111], a[24:27], v[156:159], v[96:111]\n" \
    "v_mfma_f32_32x32x16_bf16 v[112:127], a[28:31], v[156:159], v[112:127]\n" \
    "buffer_load_dwordx4 v[152:155], v24, s[44:47], s61 offen offset:3072 sc1\n" \
    "buffer_load_dwordx4 v[156:159], v24, s[44:47], s62 offen offset:3072 sc1\n" \
    "s_waitcnt vmcnt(15)\n" \
    "v_mfma_f32_32x32x16_bf16 v[64:79], a[32:35], v[160:163], v[64:79]\n" \
    "ds_read_b64_tr_b16 a[96:97], v16 offset:32768\n" \
    "ds_read_b64_tr_b16 a[98:99], v17 offset:32768\n" \
    "ds_read_b64_tr_b16 a[100:101], v1 offset:32768\n" \
    "v_mfma_f32_32x32x16_bf16 v[80:95], a[36:39], v[160:163], v[80:95]\n" \
    "ds_read_b64_tr_b16 a[102:103], v2 offset:32768\n" \
    "ds_read_b64_tr_b16 a[104:105], v16 offset:34816\n" \
    "ds_read_b64_tr_b16 a[106:107], v17 offset:34816\n" \
    "s_waitcnt vmcnt(14)\n" \
    "v_mfma_f32_32x32x16_bf16 v[96:111], a[32:35], v[164:167], v[96:111]\n" \
    "ds_read_b64_tr_b16 a[108:109], v1 offset:34816\n" \
    "ds_read_b64_tr_b16 a[110:111], v2 offset:34816\n" \
    "v_mfma_f32_32x32x16_bf16 v[112:127], a[36:39], v[164:167], v[112:127]\n" \
    "s_add_u32 s61, s61, 4096\n" \
    "s_add_u32 s62, s62, 4096\n" \
    "buffer_load_dwordx4 v[160:163], v24, s[44:47], s61 offen offset:0 sc1\n" \
    "buffer_load_dwordx4 v[164:167], v24, s[44:47], s62 offen offset:0 sc1\n" \
    "s_waitcnt vmcnt(15)\n" \
    "v_mfma_f32_32x32x16_bf16 v[64:79], a[40:43], v[168:171], v[64:79]\n" \
    "v_mfma_f32_32x32x16_bf16 v[80:95], a[44:47], v[168:171], v[80:95]\n" \
    "s_waitcnt vmcnt(14)\n" \
    "v_mfma_f32_32x32x16_bf16 v[96:111], a[40:43], v[172:175], v[96:111]\n" \
    "v_mfma_f32_32x32x16_bf16 v[112:127], a[44:47], v[172:175], v[112:127]\n" \
    "buffer_load_dwordx4 v[168:171], v24, s[44:47], s61 offen offset:1024 sc1\n" \
    "buffer_load_dwordx4 v[172:175], v24, s[44:47], s62 offen offset:1024 sc1\n" \
    "s_waitcnt vmcnt(15)\n" \
    "v_mfma_f32_32x32x16_bf16 v[64:79], a[48:51], v[176:179], v[64:79]\n" \
    "ds_read_b64_tr_b16 a[112:113], v16 offset:49152\n" \
    "ds_read_b64_tr_b16 a[114:115], v17 offset:49152\n" \
    "ds_read_b64_tr_b16 a[116:117], v1 offset:49152\n" \
    "v_mfma_f32_32x32x16_bf16 v[80:95], a[52:55], v[176:179], v[80:95]\n" \
    "ds_read_b64_tr_b16 a[118:119], v2 offset:49152\n" \
    "ds_read_b64_tr_b16 a[120:121], v16 offset:51200\n" \
    "ds_read_b64_tr_b16 a[122:123], v17 offset:51200\n" \
    "s_waitcnt vmcnt(14)\n" \
    "v_mfma_f32_32x32x16_bf16 v[96:111], a[48:51], v[180:183], v[96:111]\n" \
    "ds_read_b64_tr_b16 a[124:125], v1 offset:51200\n" \
    "ds_read_b64_tr_b16 a[126:127], v2 offset:51200\n" \
    "v_mfma_f32_32x32x16_bf16 v[112:127], a[52:55], v[180:183], v[112:127]\n" \
    "buffer_load_dwordx4 v[176:179], v24, s[44:47], s61 offen offset:2048 sc1\n" \
    "buffer_load_dwordx4 v[180:183], v24, s[44:47], s62 offen offset:2048 sc1\n" \
    "s_waitcnt vmcnt(15)\n" \
    "v_mfma_f32_32x32x16_bf16 v[64:79], a[56:59], v[184:187], v[64:79]\n" \
    "v_mfma_f32_32x32x16_bf16 v[80:95], a[60:63], v[184:187], v[80:95]\n" \
    "s_waitcnt vmcnt(14)\n" \
    "v_mfma_f32_32x32x16_bf16 v[96:111], a[56:59], v[188:191], v[96:111]\n" \
    "v_mfma_f32_32x32x16_bf16 v[112:127], a[60:63], v[188:191], v[112:127]\n" \
    "buffer_load_dwordx4 v[184:187], v24, s[44:47], s61 offen offset:3072 sc1\n" \
    "buffer_load_dwordx4 v[188:191], v24, s[44:47], s62 offen offset:3072 sc1\n" \
    "s_waitcnt vmcnt(15)\n" \
    "v_mfma_f32_32x32x16_bf16 v[64:79], a[64:67], v[128:131], v[64:79]\n" \
    "s_waitcnt lgkmcnt(7)\n" \
    "v_mfma_f32_32x32x16_bf16 v[80:95], a[68:71], v[128:131], v[80:95]\n" \
    "s_waitcnt vmcnt(14)\n" \
    "v_mfma_f32_32x32x16_bf16 v[96:111], a[64:67], v[132:135], v[96:111]\n" \
    "v_mfma_f32_32x32x16_bf16 v[112:127], a[68:71], v[132:135], v[112:127]\n" \
    "s_waitcnt vmcnt(13)\n" \
    "v_mfma_f32_32x32x16_bf16 v[64:79], a[72:75], v[136:139], v[64:79]\n" \
    "v_mfma_f32_32x32x16_bf16 v[80:95], a[76:79], v[136:139], v[80:95]\n" \
    "s_waitcnt vmcnt(12)\n" \
    "v_mfma_f32_32x32x16_bf16 v[96:111], a[72:75], v[140:143], v[96:111]\n" \
    "v_mfma_f32_32x32x16_bf16 v[112:127], a[76:79], v[140:143], v[112:127]\n" \
    "s_waitcnt vmcnt(11)\n" \
    "v_mfma_f32_32x32x16_bf16 v[64:79], a[80:83], v[144:147], v[64:79]\n" \
    "v_mfma_f32_32x32x16_bf16 v[80:95], a[84:87], v[144:147], v[80:95]\n" \
    "s_waitcnt vmcnt(10)\n" \
    "v_mfma_f32_32x32x16_bf16 v[96:111], a[80:83], v[148:151], v[96:111]\n" \
    "v_mfma_f32_32x32x16_bf16 v[112:127], a[84:87], v[148:151], v[112:127]\n" \
    "s_waitcnt vmcnt(9)\n" \
    "v_mfma_f32_32x32x16_bf16 v[64:79], a[88:91], v[152:155], v[64:79]\n" \
    "v_mfma_f32_32x32x16_bf16 v[80:95], a[92:95], v[152:155], v[80:95]\n" \
    "s_waitcnt vmcnt(8)\n" \
    "v_mfma_f32_32x32x16_bf16 v[96:111], a[88:91], v[156:159], v[96:111]\n" \
    "v_mfma_f32_32x32x16_bf16 v[112:127], a[92:95], v[156:159], v[112:127]\n" \
    "s_waitcnt vmcnt(7)\n" \
    "v_mfma_f32_32x32x16_bf16 v[64:79], a[96:99], v[160:163], v[64:79]\n" \
    "v_mfma_f32_32x32x16_bf16 v[80:95], a[100:103], v[160:163], v[80:95]\n" \
    "s_waitcnt vmcnt(6)\n" \
    "v_mfma_f32_32x32x16_bf16 v[96:111], a[96:99], v[164:167], v[96:111]\n" \
    "v_mfma_f32_32x32x16_bf16 v[112:127], a[100:103], v[164:167], v[112:127]\n" \
    "s_waitcnt vmcnt(5)\n" \
    "v_mfma_f32_32x32x16_bf16 v[64:79], a[104:107], v[168:171], v[64:79]\n" \
    "v_mfma_f32_32x32x16_bf16 v[80:95], a[108:111], v[168:171], v[80:95]\n" \
    "s_waitcnt vmcnt(4)\n" \
    "v_mfma_f32_32x32x16_bf16 v[96:111], a[104:107], v[172:175], v[96:111]\n" \
    "v_mfma_f32_32x32x16_bf16 v[112:127], a[108:111], v[172:175], v[112:127]\n" \
    "s_waitcnt vmcnt(3) lgkmcnt(0)\n" \
    "v_mfma_f32_32x32x16_bf16 v[64:79], a[112:115], v[176:179], v[64:79]\n" \
    "v_mfma_f32_32x32x16_bf16 v[80:95], a[116:119], v[176:179], v[80:95]\n" \
    "s_waitcnt vmcnt(2)\n" \
    "v_mfma_f32_32x32x16_bf16 v[96:111], a[112:115], v[180:183], v[96:111]\n" \
    "v_mfma_f32_32x32x16_bf16 v[112:127], a[116:119], v[180:183], v[112:127]\n" \
    "s_waitcnt vmcnt(1)\n" \
    "v_mfma_f32_32x32x16_bf16 v[64:79], a[120:123], v[184:187], v[64:79]\n" \
    "v_mfma_f32_32x32x16_bf16 v[80:95], a[124:127], v[184:187], v[80:95]\n" \
    "s_waitcnt vmcnt(0)\n" \
    "v_mfma_f32_32x32x16_bf16 v[96:111], a[120:123], v[188:191], v[96:111]\n" \
    "v_mfma_f32_32x32x16_bf16 v[112:127], a[124:127], v[188:191], v[112:127]\n" \
    "s_nop 9\n" \
    "ds_write_b128 v20, v[80:83] offset:16384\n" \
    "ds_write_b128 v20, v[84:87] offset:17408\n" \
    "ds_write_b128 v20, v[88:91] offset:18432\n" \
    "ds_write_b128 v20, v[92:95] offset:19456\n" \
    "ds_write_b128 v20, v[96:99] offset:32768\n" \
    "ds_write_b128 v20, v[100:103] offset:33792\n" \
    "ds_write_b128 v20, v[104:107] offset:34816\n" \
    "ds_write_b128 v20, v[108:111] offset:35840\n" \
    "ds_write_b128 v20, v[112:115] offset:49152\n" \
    "ds_write_b128 v20, v[116:119] offset:50176\n" \
    "ds_write_b128 v20, v[120:123] offset:51200\n" \
    "ds_write_b128 v20, v[124:127] offset:52224\n" \
    "s_waitcnt lgkmcnt(0)\n" \
    "s_barrier\n" \
    "ds_read_b128 v[144:147], v21 offset:16384\n" \
    "ds_read_b128 v[160:163], v22 offset:32768\n" \
    "ds_read_b128 v[176:179], v23 offset:49152\n" \
    "ds_read_b128 v[148:151], v21 offset:17408\n" \
    "ds_read_b128 v[164:167], v22 offset:33792\n" \
    "ds_read_b128 v[180:183], v23 offset:50176\n" \
    "ds_read_b128 v[152:155], v21 offset:18432\n" \
    "ds_read_b128 v[168:171], v22 offset:34816\n" \
    "ds_read_b128 v[184:187], v23 offset:51200\n" \
    "ds_read_b128 v[156:159], v21 offset:19456\n" \
    "ds_read_b128 v[172:175], v22 offset:35840\n" \
    "ds_read_b128 v[188:191], v23 offset:52224\n" \
    "s_waitcnt lgkmcnt(11)\n" \
    "v_add_f32 v64, v64, v144\n" \
    "s_waitcnt lgkmcnt(9)\n" \
    "v_add_f32 v160, v160, v176\n" \
    "v_add_f32 v65, v65, v145\n" \
    "v_add_f32 v161, v161, v177\n" \
    "v_add_f32 v66, v66, v146\n" \
    "v_add_f32 v162, v162, v178\n" \
    "v_add_f32 v67, v67, v147\n" \
    "v_add_f32 v163, v163, v179\n" \
    "v_add_f32 v64, v64, v160\n" \
    "v_add_f32 v65, v65, v161\n" \
    "v_add_f32 v66, v66, v162\n" \
    "v_add_f32 v67, v67, v163\n" \
    "s_waitcnt lgkmcnt(7)\n" \
    "v_add_f32 v68, v68, v148\n" \
    "s_waitcnt lgkmcnt(6)\n" \
    "v_add_f32 v164, v164, v180\n" \
    "v_add_f32 v69, v69, v149\n" \
    "v_add_f32 v165, v165, v181\n" \
    "v_add_f32 v70, v70, v150\n" \
    "v_add_f32 v166, v166, v182\n" \
    "v_add_f32 v71, v71, v151\n" \
    "v_add_f32 v167, v167, v183\n" \
    "v_add_f32 v68, v68, v164\n" \
    "v_add_f32 v69, v69, v165\n" \
    "v_add_f32 v70, v70, v166\n" \
    "v_add_f32 v71, v71, v167\n" \
    "s_waitcnt lgkmcnt(0)\n" \
    "v_add_f32 v72, v72, v152\n" \
    "v_add_f32 v168, v168, v184\n" \
    "v_add_f32 v73, v73, v153\n" \
    "v_add_f32 v169, v169, v185\n" \
    "v_add_f32 v74, v74, v154\n" \
    "v_add_f32 v170, v170, v186\n" \
    "v_add_f32 v75, v75, v155\n" \
    "v_add_f32 v171, v171, v187\n" \
    "v_add_f32 v72, v72, v168\n" \
    "v_add_f32 v73, v73, v169\n" \
    "v_add_f32 v74, v74, v170\n" \
    "v_add_f32 v75, v75, v171\n" \
    "v_add_f32 v76, v76, v156\n" \
    "v_add_f32 v172, v172, v188\n" \
    "v_add_f32 v77, v77, v157\n" \
    "v_add_f32 v173, v173, v189\n" \
    "v_add_f32 v78, v78, v158\n" \
    "v_add_f32 v174, v174, v190\n" \
    "v_add_f32 v79, v79, v159\n" \
    "v_add_f32 v175, v175, v191\n" \
    "v_add_f32 v76, v76, v172\n" \
    "v_add_f32 v77, v77, v173\n" \
    "v_add_f32 v78, v78, v174\n" \
    "v_add_f32 v79, v79, v175\n" \
    "v_cvt_pk_bf16_f32 v40, v64, v65\n" \
    "v_cvt_pk_bf16_f32 v41, v66, v67\n" \
    "v_cvt_pk_bf16_f32 v42, v68, v69\n" \
    "v_cvt_pk_bf16_f32 v43, v70, v71\n" \
    "v_cvt_pk_bf16_f32 v44, v72, v73\n" \
    "v_cvt_pk_bf16_f32 v45, v74, v75\n" \
    "v_cvt_pk_bf16_f32 v46, v76, v77\n" \
    "v_cvt_pk_bf16_f32 v47, v78, v79\n" \
    "v_permlane32_swap_b32 v40, v42\n" \
    "v_permlane32_swap_b32 v41, v43\n" \
    "buffer_store_dwordx4 v[40:43], v28, s[24:27], s57 offen offset:0\n" \
    "v_permlane32_swap_b32 v44, v46\n" \
    "v_permlane32_swap_b32 v45, v47\n" \
    "buffer_store_dwordx4 v[44:47], v28, s[24:27], s57 offen offset:32\n" \
    "s_branch Lmha_end\n" \
    "Lmha_pathS:\n" \
    "s_barrier\n" \
    "ds_read_b128 v[80:83], v14 offset:0\n" \
    "ds_read_b64_tr_b16 v[48:49], v18 offset:0\n" \
    "ds_read_b64_tr_b16 v[50:51], v19 offset:0\n" \
    "ds_read_b64_tr_b16 v[52:53], v18 offset:512\n" \
    "ds_read_b64_tr_b16 v[54:55], v19 offset:512\n" \
    "ds_read_b64_tr_b16 v[56:57], v18 offset:1024\n" \
    "ds_read_b64_tr_b16 v[58:59], v19 offset:1024\n" \
    "ds_read_b64_tr_b16 v[60:61], v18 offset:1536\n" \
    "ds_read_b64_tr_b16 v[62:63], v19 offset:1536\n" \
    "s_waitcnt lgkmcnt(6)\n" \
    "v_mfma_f32_32x32x16_bf16 v[128:143], v[48:51], v[80:83], 0\n" \
    "ds_read_b128 v[88:91], v15 offset:0\n" \
    "ds_read_b64_tr_b16 v[64:65], v18 offset:4096\n" \
    "ds_read_b64_tr_b16 v[66:67], v19 offset:4096\n" \
    "s_waitcnt lgkmcnt(7)\n" \
    "v_mfma_f32_32x32x16_bf16 v[144:159], v[52:55], v[80:83], 0\n" \
    "ds_read_b64_tr_b16 v[68:69], v18 offset:4608\n" \
    "ds_read_b64_tr_b16 v[70:71], v19 offset:4608\n" \
    "ds_read_b64_tr_b16 v[72:73], v18 offset:5120\n" \
    "s_waitcnt lgkmcnt(8)\n" \
    "v_mfma_f32_32x32x16_bf16 v[160:175], v[56:59], v[80:83], 0\n" \
    "ds_read_b64_tr_b16 v[74:75], v19 offset:5120\n" \
    "ds_read_b64_tr_b16 v[76:77], v18 offset:5632\n" \
    "ds_read_b64_tr_b16 v[78:79], v19 offset:5632\n" \
    "s_waitcnt lgkmcnt(9)\n" \
    "v_mfma_f32_32x32x16_bf16 v[176:191], v[60:63], v[80:83], 0\n" \
    "s_waitcnt lgkmcnt(6)\n" \
    "v_mfma_f32_32x32x16_bf16 v[128:143], v[64:67], v[88:91], v[128:143]\n" \
    "ds_read_b128 v[80:83], v14 offset:512\n" \
    "ds_read_b64_tr_b16 v[48:49], v18 offset:8192\n" \
    "ds_read_b64_tr_b16 v[50:51], v19 offset:8192\n" \
    "s_waitcnt lgkmcnt(7)\n" \
    "v_mfma_f32_32x32x16_bf16 v[144:159], v[68:71], v[88:91], v[144:159]\n" \
    "ds_read_b64_tr_b16 v[52:53], v18 offset:8704\n" \
    "ds_read_b64_tr_b16 v[54:55], v19 offset:8704\n" \
    "ds_read_b64_tr_b16 v[56:57], v18 offset:9216\n" \
    "s_waitcnt lgkmcnt(8)\n" \
    "v_mfma_f32_32x32x16_bf16 v[160:175], v[72:75], v[88:91], v[160:175]\n" \
    "ds_read_b64_tr_b16 v[58:59], v19 offset:9216\n" \
    "ds_read_b64_tr_b16 v[60:61], v18 offset:9728\n" \
    "ds_read_b64_tr_b16 v[62:63], v19 offset:9728\n" \
    "s_waitcnt lgkmcnt(9)\n" \
    "v_mfma_f32_32x32x16_bf16 v[176:191], v[76:79], v[88:91], v[176:191]\n" \
    "s_add_u32 s76, s76, s58\n" \
    "s_cmp_eq_u32 s76, 147456\n" \
    "s_cselect_b32 s64, -98304, s58\n" \
    "s_cselect_b32 s76, 0, s76\n" \
    "s_waitcnt lgkmcnt(6)\n" \
    "v_mfma_f32_32x32x16_bf16 v[128:143], v[48:51], v[80:83], v[128:143]\n" \
    "ds_read_b128 v[88:91], v15 offset:512\n" \
    "ds_read_b64_tr_b16 v[64:65], v18 offset:12288\n" \
    "ds_read_b64_tr_b16 v[66:67], v19 offset:12288\n" \
    "s_waitcnt lgkmcnt(7)\n" \
    "v_mfma_f32_32x32x16_bf16 v[144:159], v[52:55], v[80:83], v[144:159]\n" \
    "ds_read_b64_tr_b16 v[68:69], v18 offset:12800\n" \
    "ds_read_b64_tr_b16 v[70:71], v19 offset:12800\n" \
    "ds_read_b64_tr_b16 v[72:73], v18 offset:13312\n" \
    "s_waitcnt lgkmcnt(8)\n" \
    "v_mfma_f32_32x32x16_bf16 v[160:175], v[56:59], v[80:83], v[160:175]\n" \
    "ds_read_b64_tr_b16 v[74:75], v19 offset:13312\n" \
    "ds_read_b64_tr_b16 v[76:77], v18 offset:13824\n" \
    "ds_read_b64_tr_b16 v[78:79], v19 offset:13824\n" \
    "s_waitcnt lgkmcnt(9)\n" \
    "v_mfma_f32_32x32x16_bf16 v[176:191], v[60:63], v[80:83], v[176:191]\n" \
    "v_add_u32 v14, s64, v14\n" \
    "v_add_u32 v15, s64, v15\n" \
    "v_add_u32 v18, s64, v18\n" \
    "v_add_u32 v19, s64, v19\n" \
    "s_mov_b32 s63, 6\n" \
    "s_waitcnt lgkmcnt(0)\n" \
    "s_barrier\n" \
    "s_mov_b32 s63, 2\n" \
    "Lmha_sqp:\n" \
    "v_mfma_f32_32x32x16_bf16 v[128:143], v[64:67], v[88:91], v[128:143]\n" \
    "ds_read_b128 v[80:83], v14 offset:0\n" \
    "ds_read_b64_tr_b16 v[48:49], v18 offset:0\n" \
    "ds_read_b64_tr_b16 v[50:51], v19 offset:0\n" \
    "v_mfma_f32_32x32x16_bf16 v[144:159], v[68:71], v[88:91], v[144:159]\n" \
    "ds_read_b64_tr_b16 v[52:53], v18 offset:512\n" \
    "ds_read_b64_tr_b16 v[54:55], v19 offset:512\n" \
    "ds_read_b64_tr_b16 v[56:57], v18 offset:1024\n" \
    "v_mfma_f32_32x32x16_bf16 v[160:175], v[72:75], v[88:91], v[160:175]\n" \
    "ds_read_b64_tr_b16 v[58:59], v19 offset:1024\n" \
    "ds_read_b64_tr_b16 v[60:61], v18 offset:1536\n" \
    "ds_read_b64_tr_b16 v[62:63], v19 offset:1536\n" \
    "v_mfma_f32_32x32x16_bf16 v[176:191], v[76:79], v[88:91], v[176:191]\n" \
    "s_waitcnt lgkmcnt(6)\n" \
    "v_mfma_f32_32x32x16_bf16 v[128:143], v[48:51], v[80:83], v[128:143]\n" \
    "ds_read_b128 v[88:91], v15 offset:0\n" \
    "ds_read_b64_tr_b16 v[64:65], v18 offset:4096\n" \
    "ds_read_b64_tr_b16 v[66:67], v19 offset:4096\n" \
    "s_waitcnt lgkmcnt(7)\n" \
    "v_mfma_f32_32x32x16_bf16 v[144:159], v[52:55], v[80:83], v[144:159]\n" \
    "ds_read_b64_tr_b16 v[68:69], v18 offset:4608\n" \
    "ds_read_b64_tr_b16 v[70:71], v19 offset:4608\n" \
    "ds_read_b64_tr_b16 v[72:73], v18 offset:5120\n" \
    "s_waitcnt lgkmcnt(8)\n" \
    "v_mfma_f32_32x32x16_bf16 v[160:175], v[56:59], v[80:83], v[160:175]\n" \
    "ds_read_b64_tr_b16 v[74:75], v19 offset:5120\n" \
    "ds_read_b64_tr_b16 v[76:77], v18 offset:5632\n" \
    "ds_read_b64_tr_b16 v[78:79], v19 offset:5632\n" \
    "s_waitcnt lgkmcnt(9)\n" \
    "v_mfma_f32_32x32x16_bf16 v[176:191], v[60:63], v[80:83], v[176:191]\n" \
    "s_waitcnt lgkmcnt(6)\n" \
    "v_mfma_f32_32x32x16_bf16 v[128:143], v[64:67], v[88:91], v[128:143]\n" \
    "ds_read_b128 v[80:83], v14 offset:512\n" \
    "ds_read_b64_tr_b16 v[48:49], v18 offset:8192\n" \
    "ds_read_b64_tr_b16 v[50:51], v19 offset:8192\n" \
    "s_waitcnt lgkmcnt(7)\n" \
    "v_mfma_f32_32x32x16_bf16 v[144:159], v[68:71], v[88:91], v[144:159]\n" \
    "ds_read_b64_tr_b16 v[52:53], v18 offset:8704\n" \
    "ds_read_b64_tr_b16 v[54:55], v19 offset:8704\n" \
    "ds_read_b64_tr_b16 v[56:57], v18 offset:9216\n" \
    "s_waitcnt lgkmcnt(8)\n" \
    "v_mfma_f32_32x32x16_bf16 v[160:175], v[72:75], v[88:91], v[160:175]\n" \
    "ds_read_b64_tr_b16 v[58:59], v19 offset:9216\n" \
    "ds_read_b64_tr_b16 v[60:61], v18 offset:9728\n" \
    "ds_read_b64_tr_b16 v[62:63], v19 offset:9728\n" \
    "s_waitcnt lgkmcnt(9)\n" \
    "v_mfma_f32_32x32x16_bf16 v[176:191], v[76:79], v[88:91], v[176:191]\n" \
    "s_add_u32 s76, s76, s58\n" \
    "s_cmp_eq_u32 s76, 147456\n" \
    "s_cselect_b32 s64, -98304, s58\n" \
    "s_cselect_b32 s76, 0, s76\n" \
    "s_waitcnt lgkmcnt(6)\n" \
    "v_mfma_f32_32x32x16_bf16 v[128:143], v[48:51], v[80:83], v[128:143]\n" \
    "ds_read_b128 v[88:91], v15 offset:512\n" \
    "ds_read_b64_tr_b16 v[64:65], v18 offset:12288\n" \
    "ds_read_b64_tr_b16 v[66:67], v19 offset:12288\n" \
    "s_waitcnt lgkmcnt(7)\n" \
    "v_mfma_f32_32x32x16_bf16 v[144:159], v[52:55], v[80:83], v[144:159]\n" \
    "ds_read_b64_tr_b16 v[68:69], v18 offset:12800\n" \
    "ds_read_b64_tr_b16 v[70:71], v19 offset:12800\n" \
    "ds_read_b64_tr_b16 v[72:73], v18 offset:13312\n" \
    "s_waitcnt lgkmcnt(8)\n" \
    "v_mfma_f32_32x32x16_bf16 v[160:175], v[56:59], v[80:83], v[160:175]\n" \
    "ds_read_b64_tr_b16 v[74:75], v19 offset:13312\n" \
    "ds_read_b64_tr_b16 v[76:77], v18 offset:13824\n" \
    "ds_read_b64_tr_b16 v[78:79], v19 offset:13824\n" \
    "s_waitcnt lgkmcnt(9)\n" \
    "v_mfma_f32_32x32x16_bf16 v[176:191], v[60:63], v[80:83], v[176:191]\n" \
    "v_add_u32 v14, s64, v14\n" \
    "v_add_u32 v15, s64, v15\n" \
    "v_add_u32 v18, s64, v18\n" \
    "v_add_u32 v19, s64, v19\n" \
    "s_sub_u32 s63, s63, 1\n" \
    "s_cmp_lg_u32 s63, 0\n" \
    "s_waitcnt lgkmcnt(0)\n" \
    "s_barrier\n" \
    "s_cbranch_scc1 Lmha_sqp\n" \
    "v_mfma_f32_32x32x16_bf16 v[128:143], v[64:67], v[88:91], v[128:143]\n" \
    "ds_read_b128 v[80:83], v14 offset:0\n" \
    "ds_read_b64_tr_b16 v[48:49], v18 offset:0\n" \
    "ds_read_b64_tr_b16 v[50:51], v19 offset:0\n" \
    "s_mov_b32 m0, s66\n" \
    "s_add_u32 s75, s65, 4096\n" \
    "buffer_load_dwordx4 v25, s[44:47], s65 offen offset:0 nt lds\n" \
    "v_mfma_f32_32x32x16_bf16 v[144:159], v[68:71], v[88:91], v[144:159]\n" \
    "ds_read_b64_tr_b16 v[52:53], v18 offset:512\n" \
    "ds_read_b64_tr_b16 v[54:55], v19 offset:512\n" \
    "ds_read_b64_tr_b16 v[56:57], v18 offset:1024\n" \
    "buffer_load_dwordx4 v26, s[44:47], s65 offen offset:1024 nt lds\n" \
    "v_mfma_f32_32x32x16_bf16 v[160:175], v[72:75], v[88:91], v[160:175]\n" \
    "ds_read_b64_tr_b16 v[58:59], v19 offset:1024\n" \
    "ds_read_b64_tr_b16 v[60:61], v18 offset:1536\n" \
    "ds_read_b64_tr_b16 v[62:63], v19 offset:1536\n" \
    "buffer_load_dwordx4 v27, s[44:47], s65 offen offset:2048 nt lds\n" \
    "buffer_load_dwordx4 v28, s[44:47], s65 offen offset:3072 nt lds\n" \
    "s_add_u32 m0, m0, 4096\n" \
    "v_mfma_f32_32x32x16_bf16 v[176:191], v[76:79], v[88:91], v[176:191]\n" \
    "buffer_load_dwordx4 v25, s[44:47], s75 offen offset:0 nt lds\n" \
    "s_waitcnt lgkmcnt(6)\n" \
    "v_mfma_f32_32x32x16_bf16 v[128:143], v[48:51], v[80:83], v[128:143]\n" \
    "ds_read_b128 v[88:91], v15 offset:0\n" \
    "ds_read_b64_tr_b16 v[64:65], v18 offset:4096\n" \
    "ds_read_b64_tr_b16 v[66:67], v19 offset:4096\n" \
    "buffer_load_dwordx4 v26, s[44:47], s75 offen offset:1024 nt lds\n" \
    "s_waitcnt lgkmcnt(7)\n" \
    "v_mfma_f32_32x32x16_bf16 v[144:159], v[52:55], v[80:83], v[144:159]\n" \
    "ds_read_b64_tr_b16 v[68:69], v18 offset:4608\n" \
    "ds_read_b64_tr_b16 v[70:71], v19 offset:4608\n" \
    "ds_read_b64_tr_b16 v[72:73], v18 offset:5120\n" \
    "buffer_load_dwordx4 v27, s[44:47], s75 offen offset:2048 nt lds\n" \
    "buffer_load_dwordx4 v28, s[44:47], s75 offen offset:3072 nt lds\n" \
    "s_add_u32 m0, m0, 4096\n" \
    "s_waitcnt lgkmcnt(8)\n" \
    "v_mfma_f32_32x32x16_bf16 v[160:175], v[56:59], v[80:83], v[160:175]\n" \
    "ds_read_b64_tr_b16 v[74:75], v19 offset:5120\n" \
    "ds_read_b64_tr_b16 v[76:77], v18 offset:5632\n" \
    "ds_read_b64_tr_b16 v[78:79], v19 offset:5632\n" \
    "buffer_load_dwordx4 v25, s[36:39], s65 offen offset:0 nt lds\n" \
    "s_waitcnt lgkmcnt(9)\n" \
    "v_mfma_f32_32x32x16_bf16 v[176:191], v[60:63], v[80:83], v[176:191]\n" \
    "buffer_load_dwordx4 v26, s[36:39], s65 offen offset:1024 nt lds\n" \
    "buffer_load_dwordx4 v27, s[36:39], s65 offen offset:2048 nt lds\n" \
    "s_waitcnt lgkmcnt(6)\n" \
    "v_mfma_f32_32x32x16_bf16 v[128:143], v[64:67], v[88:91], v[128:143]\n" \
    "ds_read_b128 v[80:83], v14 offset:512\n" \
    "ds_read_b64_tr_b16 v[48:49], v18 offset:8192\n" \
    "ds_read_b64_tr_b16 v[50:51], v19 offset:8192\n" \
    "buffer_load_dwordx4 v28, s[36:39], s65 offen offset:3072 nt lds\n" \
    "s_add_u32 m0, m0, 4096\n" \
    "s_waitcnt lgkmcnt(7)\n" \
    "v_mfma_f32_32x32x16_bf16 v[144:159], v[68:71], v[88:91], v[144:159]\n" \
    "ds_read_b64_tr_b16 v[52:53], v18 offset:8704\n" \
    "ds_read_b64_tr_b16 v[54:55], v19 offset:8704\n" \
    "ds_read_b64_tr_b16 v[56:57], v18 offset:9216\n" \
    "buffer_load_dwordx4 v25, s[36:39], s75 offen offset:0 nt lds\n" \
    "s_waitcnt lgkmcnt(8)\n" \
    "v_mfma_f32_32x32x16_bf16 v[160:175], v[72:75], v[88:91], v[160:175]\n" \
    "ds_read_b64_tr_b16 v[58:59], v19 offset:9216\n" \
    "ds_read_b64_tr_b16 v[60:61], v18 offset:9728\n" \
    "ds_read_b64_tr_b16 v[62:63], v19 offset:9728\n" \
    "buffer_load_dwordx4 v26, s[36:39], s75 offen offset:1024 nt lds\n" \
    "buffer_load_dwordx4 v27, s[36:39], s75 offen offset:2048 nt lds\n" \
    "s_waitcnt lgkmcnt(9)\n" \
    "v_mfma_f32_32x32x16_bf16 v[176:191], v[76:79], v[88:91], v[176:191]\n" \
    "buffer_load_dwordx4 v28, s[36:39], s75 offen offset:3072 nt lds\n" \
    "s_add_u32 m0, m0, 4096\n" \
    "s_add_u32 s76, s76, s58\n" \
    "s_cmp_eq_u32 s76, 147456\n" \
    "s_cselect_b32 s64, -98304, s58\n" \
    "s_cselect_b32 s76, 0, s76\n" \
    "s_waitcnt lgkmcnt(6)\n" \
    "v_mfma_f32_32x32x16_bf16 v[128:143], v[48:51], v[80:83], v[128:143]\n" \
    "ds_read_b128 v[88:91], v15 offset:512\n" \
    "ds_read_b64_tr_b16 v[64:65], v18 offset:12288\n" \
    "ds_read_b64_tr_b16 v[66:67], v19 offset:12288\n" \
    "s_add_u32 s65, s65, 8192\n" \
    "s_and_b32 s65, s65, 131071\n" \
    "s_add_u32 s66, s66, s59\n" \
    "s_and_b32 s66, s66, 131071\n" \
    "s_mul_i32 s55, %1, 18432\n" \
    "v_add_u32 v9, 0, v12\n" \
    "v_add_u32 v9, s55, v9\n" \
    "global_load_dword v13, v9, s[16:17]\n" \
    "v_add_u32 v9, 8192, v12\n" \
    "v_add_u32 v9, s55, v9\n" \
    "global_load_dword v13, v9, s[16:17]\n" \
    "v_add_u32 v9, 16384, v12\n" \
    "v_min_u32 v9, 18304, v9\n" \
    "v_add_u32 v9, s55, v9\n" \
    "global_load_dword v13, v9, s[16:17]\n" \
    "s_waitcnt lgkmcnt(3)\n" \
    "v_mfma_f32_32x32x16_bf16 v[144:159], v[52:55], v[80:83], v[144:159]\n" \
    "ds_read_b64_tr_b16 v[68:69], v18 offset:12800\n" \
    "ds_read_b64_tr_b16 v[70:71], v19 offset:12800\n" \
    "ds_read_b64_tr_b16 v[72:73], v18 offset:13312\n" \
    "v_mfma_f32_32x32x16_bf16 v[160:175], v[56:59], v[80:83], v[160:175]\n" \
    "ds_read_b64_tr_b16 v[74:75], v19 offset:13312\n" \
    "ds_read_b64_tr_b16 v[76:77], v18 offset:13824\n" \
    "ds_read_b64_tr_b16 v[78:79], v19 offset:13824\n" \
    "v_mfma_f32_32x32x16_bf16 v[176:191], v[60:63], v[80:83], v[176:191]\n" \
    "v_add_u32 v14, s64, v14\n" \
    "v_add_u32 v15, s64, v15\n" \
    "v_add_u32 v18, s64, v18\n" \
    "v_add_u32 v19, s64, v19\n" \
    "s_waitcnt lgkmcnt(0)\n" \
    "s_barrier\n" \
    "s_mov_b32 s63, 3\n" \
    "Lmha_sqpb:\n" \
    "v_mfma_f32_32x32x16_bf16 v[128:143], v[64:67], v[88:91], v[128:143]\n" \
    "ds_read_b128 v[80:83], v14 offset:0\n" \
    "ds_read_b64_tr_b16 v[48:49], v18 offset:0\n" \
    "ds_read_b64_tr_b16 v[50:51], v19 offset:0\n" \
    "v_mfma_f32_32x32x16_bf16 v[144:159], v[68:71], v[88:91], v[144:159]\n" \
    "ds_read_b64_tr_b16 v[52:53], v18 offset:512\n" \
    "ds_read_b64_tr_b16 v[54:55], v19 offset:512\n" \
    "ds_read_b64_tr_b16 v[56:57], v18 offset:1024\n" \
    "v_mfma_f32_32x32x16_bf16 v[160:175], v[72:75], v[88:91], v[160:175]\n" \
    "ds_read_b64_tr_b16 v[58:59], v19 offset:1024\n" \
    "ds_read_b64_tr_b16 v[60:61], v18 offset:1536\n" \
    "ds_read_b64_tr_b16 v[62:63], v19 offset:1536\n" \
    "v_mfma_f32_32x32x16_bf16 v[176:191], v[76:79], v[88:91], v[176:191]\n" \
    "s_waitcnt lgkmcnt(6)\n" \
    "v_mfma_f32_32x32x16_bf16 v[128:143], v[48:51], v[80:83], v[128:143]\n" \
    "ds_read_b128 v[88:91], v15 offset:0\n" \
    "ds_read_b64_tr_b16 v[64:65], v18 offset:4096\n" \
    "ds_read_b64_tr_b16 v[66:67], v19 offset:4096\n" \
    "s_waitcnt lgkmcnt(7)\n" \
    "v_mfma_f32_32x32x16_bf16 v[144:159], v[52:55], v[80:83], v[144:159]\n" \
    "ds_read_b64_tr_b16 v[68:69], v18 offset:4608\n" \
    "ds_read_b64_tr_b16 v[70:71], v19 offset:4608\n" \
    "ds_read_b64_tr_b16 v[72:73], v18 offset:5120\n" \
    "s_waitcnt lgkmcnt(8)\n" \
    "v_mfma_f32_32x32x16_bf16 v[160:175], v[56:59], v[80:83], v[160:175]\n" \
    "ds_read_b64_tr_b16 v[74:75], v19 offset:5120\n" \
    "ds_read_b64_tr_b16 v[76:77], v18 offset:5632\n" \
    "ds_read_b64_tr_b16 v[78:79], v19 offset:5632\n" \
    "s_waitcnt lgkmcnt(9)\n" \
    "v_mfma_f32_32x32x16_bf16 v[176:191], v[60:63], v[80:83], v[176:191]\n" \
    "s_waitcnt lgkmcnt(6)\n" \
    "v_mfma_f32_32x32x16_bf16 v[128:143], v[64:67], v[88:91], v[128:143]\n" \
    "ds_read_b128 v[80:83], v14 offset:512\n" \
    "ds_read_b64_tr_b16 v[48:49], v18 offset:8192\n" \
    "ds_read_b64_tr_b16 v[50:51], v19 offset:8192\n" \
    "s_waitcnt lgkmcnt(7)\n" \
    "v_mfma_f32_32x32x16_bf16 v[144:159], v[68:71], v[88:91], v[144:159]\n" \
    "ds_read_b64_tr_b16 v[52:53], v18 offset:8704\n" \
    "ds_read_b64_tr_b16 v[54:55], v19 offset:8704\n" \
    "ds_read_b64_tr_b16 v[56:57], v18 offset:9216\n" \
    "s_waitcnt lgkmcnt(8)\n" \
    "v_mfma_f32_32x32x16_bf16 v[160:175], v[72:75], v[88:91], v[160:175]\n" \
    "ds_read_b64_tr_b16 v[58:59], v19 offset:9216\n" \
    "ds_read_b64_tr_b16 v[60:61], v18 offset:9728\n" \
    "ds_read_b64_tr_b16 v[62:63], v19 offset:9728\n" \
    "s_waitcnt lgkmcnt(9)\n" \
    "v_mfma_f32_32x32x16_bf16 v[176:191], v[76:79], v[88:91], v[176:191]\n" \
    "s_add_u32 s76, s76, s58\n" \
    "s_cmp_eq_u32 s76, 147456\n" \
    "s_cselect_b32 s64, -98304, s58\n" \
    "s_cselect_b32 s76, 0, s76\n" \
    "s_waitcnt lgkmcnt(6)\n" \
    "v_mfma_f32_32x32x16_bf16 v[128:143], v[48:51], v[80:83], v[128:143]\n" \
    "ds_read_b128 v[88:91], v15 offset:512\n" \
    "ds_read_b64_tr_b16 v[64:65], v18 offset:12288\n" \
    "ds_read_b64_tr_b16 v[66:67], v19 offset:12288\n" \
    "s_waitcnt lgkmcnt(7)\n" \
    "v_mfma_f32_32x32x16_bf16 v[144:159], v[52:55], v[80:83], v[144:159]\n" \
    "ds_read_b64_tr_b16 v[68:69], v18 offset:12800\n" \
    "ds_read_b64_tr_b16 v[70:71], v19 offset:12800\n" \
    "ds_read_b64_tr_b16 v[72:73], v18 offset:13312\n" \
    "s_waitcnt lgkmcnt(8)\n" \
    "v_mfma_f32_32x32x16_bf16 v[160:175], v[56:59], v[80:83], v[160:175]\n" \
    "ds_read_b64_tr_b16 v[74:75], v19 offset:13312\n" \
    "ds_read_b64_tr_b16 v[76:77], v18 offset:13824\n" \
    "ds_read_b64_tr_b16 v[78:79], v19 offset:13824\n" \
    "s_waitcnt lgkmcnt(9)\n" \
    "v_mfma_f32_32x32x16_bf16 v[176:191], v[60:63], v[80:83], v[176:191]\n" \
    "v_add_u32 v14, s64, v14\n" \
    "v_add_u32 v15, s64, v15\n" \
    "v_add_u32 v18, s64, v18\n" \
    "v_add_u32 v19, s64, v19\n" \
    "s_sub_u32 s63, s63, 1\n" \
    "s_cmp_lg_u32 s63, 0\n" \
    "s_waitcnt lgkmcnt(0)\n" \
    "s_barrier\n" \
    "s_cbranch_scc1 Lmha_sqpb\n" \
    "v_mfma_f32_32x32x16_bf16 v[128:143], v[64:67], v[88:91], v[128:143]\n" \
    "ds_read_b128 v[80:83], v14 offset:0\n" \
    "ds_read_b64_tr_b16 v[48:49], v18 offset:0\n" \
    "ds_read_b64_tr_b16 v[50:51], v19 offset:0\n" \
    "s_mov_b32 m0, s66\n" \
    "s_add_u32 s75, s65, 4096\n" \
    "buffer_load_dwordx4 v25, s[44:47], s65 offen offset:0 nt lds\n" \
    "v_mfma_f32_32x32x16_bf16 v[144:159], v[68:71], v[88:91], v[144:159]\n" \
    "ds_read_b64_tr_b16 v[52:53], v18 offset:512\n" \
    "ds_read_b64_tr_b16 v[54:55], v19 offset:512\n" \
    "ds_read_b64_tr_b16 v[56:57], v18 offset:1024\n" \
    "buffer_load_dwordx4 v26, s[44:47], s65 offen offset:1024 nt lds\n" \
    "v_mfma_f32_32x32x16_bf16 v[160:175], v[72:75], v[88:91], v[160:175]\n" \
    "ds_read_b64_tr_b16 v[58:59], v19 offset:1024\n" \
    "ds_read_b64_tr_b16 v[60:61], v18 offset:1536\n" \
    "ds_read_b64_tr_b16 v[62:63], v19 offset:1536\n" \
    "buffer_load_dwordx4 v27, s[44:47], s65 offen offset:2048 nt lds\n" \
    "v_mfma_f32_32x32x16_bf16 v[176:191], v[76:79], v[88:91], v[176:191]\n" \
    "buffer_load_dwordx4 v28, s[44:47], s65 offen offset:3072 nt lds\n" \
    "s_add_u32 m0, m0, 4096\n" \
    "s_waitcnt lgkmcnt(6)\n" \
    "v_mfma_f32_32x32x16_bf16 v[128:143], v[48:51], v[80:83], v[128:143]\n" \
    "ds_read_b128 v[88:91], v15 offset:0\n" \
    "ds_read_b64_tr_b16 v[64:65], v18 offset:4096\n" \
    "ds_read_b64_tr_b16 v[66:67], v19 offset:4096\n" \
    "buffer_load_dwordx4 v25, s[44:47], s75 offen offset:0 nt lds\n" \
    "s_waitcnt lgkmcnt(7)\n" \
    "v_mfma_f32_32x32x16_bf16 v[144:159], v[52:55], v[80:83], v[144:159]\n" \
    "ds_read_b64_tr_b16 v[68:69], v18 offset:4608\n" \
    "ds_read_b64_tr_b16 v[70:71], v19 offset:4608\n" \
    "ds_read_b64_tr_b16 v[72:73], v18 offset:5120\n" \
    "buffer_load_dwordx4 v26, s[44:47], s75 offen offset:1024 nt lds\n" \
    "buffer_load_dwordx4 v27, s[44:47], s75 offen offset:2048 nt lds\n" \
    "s_waitcnt lgkmcnt(8)\n" \
    "v_mfma_f32_32x32x16_bf16 v[160:175], v[56:59], v[80:83], v[160:175]\n" \
    "ds_read_b64_tr_b16 v[74:75], v19 offset:5120\n" \
    "ds_read_b64_tr_b16 v[76:77], v18 offset:5632\n" \
    "ds_read_b64_tr_b16 v[78:79], v19 offset:5632\n" \
    "buffer_load_dwordx4 v28, s[44:47], s75 offen offset:3072 nt lds\n" \
    "s_add_u32 m0, m0, 4096\n" \
    "s_waitcnt lgkmcnt(9)\n" \
    "v_mfma_f32_32x32x16_bf16 v[176:191], v[60:63], v[80:83], v[176:191]\n" \
    "buffer_load_dwordx4 v25, s[36:39], s65 offen offset:0 nt lds\n" \
    "s_waitcnt lgkmcnt(6)\n" \
    "v_mfma_f32_32x32x16_bf16 v[128:143], v[64:67], v[88:91], v[128:143]\n" \
    "ds_read_b128 v[80:83], v14 offset:512\n" \
    "ds_read_b64_tr_b16 v[48:49], v18 offset:8192\n" \
    "ds_read_b64_tr_b16 v[50:51], v19 offset:8192\n" \
    "buffer_load_dwordx4 v26, s[36:39], s65 offen offset:1024 nt lds\n" \
    "s_waitcnt lgkmcnt(7)\n" \
    "v_mfma_f32_32x32x16_bf16 v[144:159], v[68:71], v[88:91], v[144:159]\n" \
    "ds_read_b64_tr_b16 v[52:53], v18 offset:8704\n" \
    "ds_read_b64_tr_b16 v[54:55], v19 offset:8704\n" \
    "ds_read_b64_tr_b16 v[56:57], v18 offset:9216\n" \
    "buffer_load_dwordx4 v27, s[36:39], s65 offen offset:2048 nt lds\n" \
    "s_waitcnt lgkmcnt(8)\n" \
    "v_mfma_f32_32x32x16_bf16 v[160:175], v[72:75], v[88:91], v[160:175]\n" \
    "ds_read_b64_tr_b16 v[58:59], v19 offset:9216\n" \
    "ds_read_b64_tr_b16 v[60:61], v18 offset:9728\n" \
    "ds_read_b64_tr_b16 v[62:63], v19 offset:9728\n" \
    "buffer_load_dwordx4 v28, s[36:39], s65 offen offset:3072 nt lds\n" \
    "s_add_u32 m0, m0, 4096\n" \
    "s_nop 0\n" \
    "buffer_load_dwordx4 v25, s[36:39], s75 offen offset:0 nt lds\n" \
    "s_waitcnt lgkmcnt(9)\n" \
    "v_mfma_f32_32x32x16_bf16 v[176:191], v[76:79], v[88:91], v[176:191]\n" \
    "buffer_load_dwordx4 v26, s[36:39], s75 offen offset:1024 nt lds\n" \
    "s_add_u32 s76, s76, s58\n" \
    "s_cmp_eq_u32 s76, 147456\n" \
    "s_cselect_b32 s64, -98304, s58\n" \
    "s_cselect_b32 s76, 0, s76\n" \
    "s_waitcnt lgkmcnt(6)\n" \
    "v_mfma_f32_32x32x16_bf16 v[128:143], v[48:51], v[80:83], v[128:143]\n" \
    "ds_read_b128 v[88:91], v15 offset:512\n" \
    "ds_read_b64_tr_b16 v[64:65], v18 offset:12288\n" \
    "ds_read_b64_tr_b16 v[66:67], v19 offset:12288\n" \
    "buffer_load_dwordx4 v27, s[36:39], s75 offen offset:2048 nt lds\n" \
    "s_waitcnt lgkmcnt(6)\n" \
    "v_mfma_f32_32x32x16_bf16 v[144:159], v[52:55], v[80:83], v[144:159]\n" \
    "ds_read_b64_tr_b16 v[68:69], v18 offset:12800\n" \
    "ds_read_b64_tr_b16 v[70:71], v19 offset:12800\n" \
    "ds_read_b64_tr_b16 v[72:73], v18 offset:13312\n" \
    "buffer_load_dwordx4 v28, s[36:39], s75 offen offset:3072 nt lds\n" \
    "s_add_u32 m0, m0, 4096\n" \
    "s_waitcnt lgkmcnt(6)\n" \
    "v_mfma_f32_32x32x16_bf16 v[160:175], v[56:59], v[80:83], v[160:175]\n" \
    "ds_read_b64_tr_b16 v[74:75], v19 offset:13312\n" \
    "ds_read_b64_tr_b16 v[76:77], v18 offset:13824\n" \
    "ds_read_b64_tr_b16 v[78:79], v19 offset:13824\n" \
    "s_add_u32 s65, s65, 8192\n" \
    "s_and_b32 s65, s65, 131071\n" \
    "s_add_u32 s66, s66, s59\n" \
    "s_and_b32 s66, s66, 131071\n" \
    "v_mfma_f32_32x32x16_bf16 v[176:191], v[60:63], v[80:83], v[176:191]\n" \
    "v_add_u32 v14, s64, v14\n" \
    "v_add_u32 v15, s64, v15\n" \
    "v_add_u32 v18, s64, v18\n" \
    "v_add_u32 v19, s64, v19\n" \
    "s_waitcnt lgkmcnt(0)\n" \
    "s_barrier\n" \
    "v_mfma_f32_32x32x16_bf16 v[128:143], v[64:67], v[88:91], v[128:143]\n" \
    "ds_read_b128 v[80:83], v14 offset:0\n" \
    "ds_read_b64_tr_b16 v[48:49], v18 offset:0\n" \
    "ds_read_b64_tr_b16 v[50:51], v19 offset:0\n" \
    "s_mov_b32 m0, s66\n" \
    "s_add_u32 s75, s65, 4096\n" \
    "v_mfma_f32_32x32x16_bf16 v[144:159], v[68:71], v[88:91], v[144:159]\n" \
    "ds_read_b64_tr_b16 v[52:53], v18 offset:512\n" \
    "ds_read_b64_tr_b16 v[54:55], v19 offset:512\n" \
    "ds_read_b64_tr_b16 v[56:57], v18 offset:1024\n" \
    "buffer_load_dwordx4 v25, s[44:47], s65 offen offset:0 nt lds\n" \
    "v_mfma_f32_32x32x16_bf16 v[160:175], v[72:75], v[88:91], v[160:175]\n" \
    "ds_read_b64_tr_b16 v[58:59], v19 offset:1024\n" \
    "ds_read_b64_tr_b16 v[60:61], v18 offset:1536\n" \
    "ds_read_b64_tr_b16 v[62:63], v19 offset:1536\n" \
    "buffer_load_dwordx4 v26, s[44:47], s65 offen offset:1024 nt lds\n" \
    "v_mfma_f32_32x32x16_bf16 v[176:191], v[76:79], v[88:91], v[176:191]\n" \
    "buffer_load_dwordx4 v27, s[44:47], s65 offen offset:2048 nt lds\n" \
    "s_waitcnt lgkmcnt(6)\n" \
    "v_mfma_f32_32x32x16_bf16 v[128:143], v[48:51], v[80:83], v[128:143]\n" \
    "ds_read_b128 v[88:91], v15 offset:0\n" \
    "ds_read_b64_tr_b16 v[64:65], v18 offset:4096\n" \
    "ds_read_b64_tr_b16 v[66:67], v19 offset:4096\n" \
    "buffer_load_dwordx4 v28, s[44:47], s65 offen offset:3072 nt lds\n" \
    "s_add_u32 m0, m0, 4096\n" \
    "s_waitcnt lgkmcnt(7)\n" \
    "v_mfma_f32_32x32x16_bf16 v[144:159], v[52:55], v[80:83], v[144:159]\n" \
    "ds_read_b64_tr_b16 v[68:69], v18 offset:4608\n" \
    "ds_read_b64_tr_b16 v[70:71], v19 offset:4608\n" \
    "ds_read_b64_tr_b16 v[72:73], v18 offset:5120\n" \
    "buffer_load_dwordx4 v25, s[44:47], s75 offen offset:0 nt lds\n" \
    "s_waitcnt lgkmcnt(8)\n" \
    "v_mfma_f32_32x32x16_bf16 v[160:175], v[56:59], v[80:83], v[160:175]\n" \
    "ds_read_b64_tr_b16 v[74:75], v19 offset:5120\n" \
    "ds_read_b64_tr_b16 v[76:77], v18 offset:5632\n" \
    "ds_read_b64_tr_b16 v[78:79], v19 offset:5632\n" \
    "buffer_load_dwordx4 v26, s[44:47], s75 offen offset:1024 nt lds\n" \
    "s_waitcnt lgkmcnt(9)\n" \
    "v_mfma_f32_32x32x16_bf16 v[176:191], v[60:63], v[80:83], v[176:191]\n" \
    "buffer_load_dwordx4 v27, s[44:47], s75 offen offset:2048 nt lds\n" \
    "s_waitcnt lgkmcnt(6)\n" \
    "v_mfma_f32_32x32x16_bf16 v[128:143], v[64:67], v[88:91], v[128:143]\n" \
    "ds_read_b128 v[80:83], v14 offset:512\n" \
    "ds_read_b64_tr_b16 v[48:49], v18 offset:8192\n" \
    "ds_read_b64_tr_b16 v[50:51], v19 offset:8192\n" \
    "buffer_load_dwordx4 v28, s[44:47], s75 offen offset:3072 nt lds\n" \
    "s_add_u32 m0, m0, 4096\n" \
    "s_waitcnt lgkmcnt(7)\n" \
    "v_mfma_f32_32x32x16_bf16 v[144:159], v[68:71], v[88:91], v[144:159]\n" \
    "ds_read_b64_tr_b16 v[52:53], v18 offset:8704\n" \
    "ds_read_b64_tr_b16 v[54:55], v19 offset:8704\n" \
    "ds_read_b64_tr_b16 v[56:57], v18 offset:9216\n" \
    "buffer_load_dwordx4 v25, s[36:39], s65 offen offset:0 nt lds\n" \
    "s_waitcnt lgkmcnt(8)\n" \
    "v_mfma_f32_32x32x16_bf16 v[160:175], v[72:75], v[88:91], v[160:175]\n" \
    "ds_read_b64_tr_b16 v[58:59], v19 offset:9216\n" \
    "ds_read_b64_tr_b16 v[60:61], v18 offset:9728\n" \
    "ds_read_b64_tr_b16 v[62:63], v19 offset:9728\n" \
    "buffer_load_dwordx4 v26, s[36:39], s65 offen offset:1024 nt lds\n" \
    "s_waitcnt lgkmcnt(9)\n" \
    "v_mfma_f32_32x32x16_bf16 v[176:191], v[76:79], v[88:91], v[176:191]\n" \
    "buffer_load_dwordx4 v27, s[36:39], s65 offen offset:2048 nt lds\n" \
    "s_waitcnt lgkmcnt(6)\n" \
    "v_mfma_f32_32x32x16_bf16 v[128:143], v[48:51], v[80:83], v[128:143]\n" \
    "ds_read_b128 v[88:91], v15 offset:512\n" \
    "ds_read_b64_tr_b16 v[64:65], v18 offset:12288\n" \
    "ds_read_b64_tr_b16 v[66:67], v19 offset:12288\n" \
    "buffer_load_dwordx4 v28, s[36:39], s65 offen offset:3072 nt lds\n" \
    "s_add_u32 m0, m0, 4096\n" \
    "s_waitcnt lgkmcnt(7)\n" \
    "v_mfma_f32_32x32x16_bf16 v[144:159], v[52:55], v[80:83], v[144:159]\n" \
    "ds_read_b64_tr_b16 v[68:69], v18 offset:12800\n" \
    "ds_read_b64_tr_b16 v[70:71], v19 offset:12800\n" \
    "ds_read_b64_tr_b16 v[72:73], v18 offset:13312\n" \
    "buffer_load_dwordx4 v25, s[36:39], s75 offen offset:0 nt lds\n" \
    "s_waitcnt lgkmcnt(8)\n" \
    "v_mfma_f32_32x32x16_bf16 v[160:175], v[56:59], v[80:83], v[160:175]\n" \
    "ds_read_b64_tr_b16 v[74:75], v19 offset:13312\n" \
    "ds_read_b64_tr_b16 v[76:77], v18 offset:13824\n" \
    "ds_read_b64_tr_b16 v[78:79], v19 offset:13824\n" \
    "buffer_load_dwordx4 v26, s[36:39], s75 offen offset:1024 nt lds\n" \
    "s_waitcnt lgkmcnt(9)\n" \
    "v_mfma_f32_32x32x16_bf16 v[176:191], v[60:63], v[80:83], v[176:191]\n" \
    "buffer_load_dwordx4 v27, s[36:39], s75 offen offset:2048 nt lds\n" \
    "s_waitcnt lgkmcnt(6)\n" \
    "v_mfma_f32_32x32x16_bf16 v[128:143], v[64:67], v[88:91], v[128:143]\n" \
    "buffer_load_dwordx4 v28, s[36:39], s75 offen offset:3072 nt lds\n" \
    "s_add_u32 m0, m0, 4096\n" \
    "s_waitcnt lgkmcnt(4)\n" \
    "v_mfma_f32_32x32x16_bf16 v[144:159], v[68:71], v[88:91], v[144:159]\n" \
    "s_add_u32 s65, s65, 8192\n" \
    "s_and_b32 s65, s65, 131071\n" \
    "s_add_u32 s66, s66, s59\n" \
    "s_and_b32 s66, s66, 131071\n" \
    "s_waitcnt lgkmcnt(2)\n" \
    "v_mfma_f32_32x32x16_bf16 v[160:175], v[72:75], v[88:91], v[160:175]\n" \
    "s_waitcnt lgkmcnt(0)\n" \
    "v_mfma_f32_32x32x16_bf16 v[176:191], v[76:79], v[88:91], v[176:191]\n" \
    "s_waitcnt vmcnt(35)\n" \
    "ds_read_b128 a[64:67], v20 offset:0\n" \
    "ds_read_b128 a[68:71], v21 offset:0\n" \
    "ds_read_b128 a[72:75], v20 offset:256\n" \
    "ds_read_b128 a[76:79], v21 offset:256\n" \
    "ds_read_b128 a[80:83], v20 offset:512\n" \
    "ds_read_b128 a[84:87], v21 offset:512\n" \
    "ds_read_b128 a[88:91], v20 offset:768\n" \
    "ds_read_b128 a[92:95], v21 offset:768\n" \
    "v_readfirstlane_b32 s22, v10\n" \
    "v_readfirstlane_b32 s23, v11\n" \
    "v_mov_b64 v[38:39], 0\n" \
    "s_add_u32 s22, s22, 0x7f4a7c15\n" \
    "s_addc_u32 s23, s23, 0x9e3779b9\n" \
    "v_add_u32 v18, 65536, v16\n" \
    "v_add_u32 v19, 65536, v17\n" \
    "v_add_u32 v14, 131072, v16\n" \
    "v_add_u32 v15, 131072, v17\n" \
    "v_xor_b32 v1, 512, v16\n" \
    "v_xor_b32 v2, 512, v17\n" \
    "v_xor_b32 v3, 512, v18\n" \
    "v_xor_b32 v4, 512, v19\n" \
    "v_xor_b32 v10, 512, v14\n" \
    "v_xor_b32 v11, 512, v15\n" \
    "s_mov_b32 m0, s66\n" \
    "s_add_u32 s75, s65, 4096\n" \
    "v_mul_f32 v128, s70, v128\n" \
    "v_mul_f32 v129, s70, v129\n" \
    "v_mul_f32 v130, s70, v130\n" \
    "v_mul_f32 v131, s70, v131\n" \
    "v_mul_f32 v132, s70, v132\n" \
    "v_mul_f32 v133, s70, v133\n" \
    "v_mul_f32 v134, s70, v134\n" \
    "buffer_load_dwordx4 v25, s[44:47], s65 offen offset:0 nt lds\n" \
    "v_mul_f32 v135, s70, v135\n" \
    "v_cvt_pk_bf16_f32 v40, v128, v129\n" \
    "v_cvt_pk_bf16_f32 v41, v130, v131\n" \
    "v_cvt_pk_bf16_f32 v42, v132, v133\n" \
    "v_cvt_pk_bf16_f32 v43, v134, v135\n" \
    "v_accvgpr_write_b32 a0, v40\n" \
    "v_accvgpr_write_b32 a1, v41\n" \
    "buffer_load_dwordx4 v26, s[44:47], s65 offen offset:1024 nt lds\n" \
    "v_accvgpr_write_b32 a2, v42\n" \
    "v_accvgpr_write_b32 a3, v43\n" \
    "v_mul_f32 v136, s70, v136\n" \
    "v_mul_f32 v137, s70, v137\n" \
    "v_mul_f32 v138, s70, v138\n" \
    "v_mul_f32 v139, s70, v139\n" \
    "v_mul_f32 v140, s70, v140\n" \
    "buffer_load_dwordx4 v27, s[44:47], s65 offen offset:2048 nt lds\n" \
    "v_mul_f32 v141, s70, v141\n" \
    "v_mul_f32 v142, s70, v142\n" \
    "v_mul_f32 v143, s70, v143\n" \
    "v_cvt_pk_bf16_f32 v44, v136, v137\n" \
    "v_cvt_pk_bf16_f32 v45, v138, v139\n" \
    "v_cvt_pk_bf16_f32 v46, v140, v141\n" \
    "v_cvt_pk_bf16_f32 v47, v142, v143\n" \
    "buffer_load_dwordx4 v28, s[44:47], s65 offen offset:3072 nt lds\n" \
    "s_add_u32 m0, m0, 4096\n" \
    "v_accvgpr_write_b32 a4, v44\n" \
    "v_accvgpr_write_b32 a5, v45\n" \
    "v_accvgpr_write_b32 a6, v46\n" \
    "v_accvgpr_write_b32 a7, v47\n" \
    "v_mul_f32 v144, s70, v144\n" \
    "v_mul_f32 v145, s70, v145\n" \
    "v_mul_f32 v146, s70, v146\n" \
    "buffer_load_dwordx4 v25, s[44:47], s75 offen offset:0 nt lds\n" \
    "v_mul_f32 v147, s70, v147\n" \
    "v_mul_f32 v148, s70, v148\n" \
    "v_mul_f32 v149, s70, v149\n" \
    "v_mul_f32 v150, s70, v150\n" \
    "v_mul_f32 v151, s70, v151\n" \
    "v_cvt_pk_bf16_f32 v40, v144, v145\n" \
    "v_cvt_pk_bf16_f32 v41, v146, v147\n" \
    "buffer_load_dwordx4 v26, s[44:47], s75 offen offset:1024 nt lds\n" \
    "v_cvt_pk_bf16_f32 v42, v148, v149\n" \
    "v_cvt_pk_bf16_f32 v43, v150, v151\n" \
    "v_accvgpr_write_b32 a8, v40\n" \
    "v_accvgpr_write_b32 a9, v41\n" \
    "v_accvgpr_write_b32 a10, v42\n" \
    "v_accvgpr_write_b32 a11, v43\n" \
    "v_mul_f32 v152, s70, v152\n" \
    "buffer_load_dwordx4 v27, s[44:47], s75 offen offset:2048 nt lds\n" \
    "v_mul_f32 v153, s70, v153\n" \
    "v_mul_f32 v154, s70, v154\n" \
    "v_mul_f32 v155, s70, v155\n" \
    "v_mul_f32 v156, s70, v156\n" \
    "v_mul_f32 v157, s70, v157\n" \
    "v_mul_f32 v158, s70, v158\n" \
    "v_mul_f32 v159, s70, v159\n" \
    "buffer_load_dwordx4 v28, s[44:47], s75 offen offset:3072 nt lds\n" \
    "s_add_u32 m0, m0, 4096\n" \
    "v_cvt_pk_bf16_f32 v44, v152, v153\n" \
    "v_cvt_pk_bf16_f32 v45, v154, v155\n" \
    "v_cvt_pk_bf16_f32 v46, v156, v157\n" \
    "v_cvt_pk_bf16_f32 v47, v158, v159\n" \
    "v_accvgpr_write_b32 a12, v44\n" \
    "v_accvgpr_write_b32 a13, v45\n" \
    "v_accvgpr_write_b32 a14, v46\n" \
    "buffer_load_dwordx4 v25, s[36:39], s65 offen offset:0 nt lds\n" \
    "v_accvgpr_write_b32 a15, v47\n" \
    "v_mul_f32 v160, s70, v160\n" \
    "v_mul_f32 v161, s70, v161\n" \
    "v_mul_f32 v162, s70, v162\n" \
    "v_mul_f32 v163, s70, v163\n" \
    "v_mul_f32 v164, s70, v164\n" \
    "v_mul_f32 v165, s70, v165\n" \
    "buffer_load_dwordx4 v26, s[36:39], s65 offen offset:1024 nt lds\n" \
    "v_mul_f32 v166, s70, v166\n" \
    "v_mul_f32 v167, s70, v167\n" \
    "v_cvt_pk_bf16_f32 v40, v160, v161\n" \
    "v_cvt_pk_bf16_f32 v41, v162, v163\n" \
    "v_cvt_pk_bf16_f32 v42, v164, v165\n" \
    "v_cvt_pk_bf16_f32 v43, v166, v167\n" \
    "v_accvgpr_write_b32 a16, v40\n" \
    "buffer_load_dwordx4 v27, s[36:39], s65 offen offset:2048 nt lds\n" \
    "v_accvgpr_write_b32 a17, v41\n" \
    "v_accvgpr_write_b32 a18, v42\n" \
    "v_accvgpr_write_b32 a19, v43\n" \
    "v_mul_f32 v168, s70, v168\n" \
    "v_mul_f32 v169, s70, v169\n" \
    "v_mul_f32 v170, s70, v170\n" \
    "v_mul_f32 v171, s70, v171\n" \
    "buffer_load_dwordx4 v28, s[36:39], s65 offen offset:3072 nt lds\n" \
    "s_add_u32 m0, m0, 4096\n" \
    "v_mul_f32 v172, s70, v172\n" \
    "v_mul_f32 v173, s70, v173\n" \
    "v_mul_f32 v174, s70, v174\n" \
    "v_mul_f32 v175, s70, v175\n" \
    "v_cvt_pk_bf16_f32 v44, v168, v169\n" \
    "v_cvt_pk_bf16_f32 v45, v170, v171\n" \
    "v_cvt_pk_bf16_f32 v46, v172, v173\n" \
    "buffer_load_dwordx4 v25, s[36:39], s75 offen offset:0 nt lds\n" \
    "v_cvt_pk_bf16_f32 v47, v174, v175\n" \
    "v_accvgpr_write_b32 a20, v44\n" \
    "v_accvgpr_write_b32 a21, v45\n" \
    "v_accvgpr_write_b32 a22, v46\n" \
    "v_accvgpr_write_b32 a23, v47\n" \
    "v_mul_f32 v176, s70, v176\n" \
    "v_mul_f32 v177, s70, v177\n" \
    "buffer_load_dwordx4 v26, s[36:39], s75 offen offset:1024 nt lds\n" \
    "v_mul_f32 v178, s70, v178\n" \
    "v_mul_f32 v179, s70, v179\n" \
    "v_mul_f32 v180, s70, v180\n" \
    "v_mul_f32 v181, s70, v181\n" \
    "v_mul_f32 v182, s70, v182\n" \
    "v_mul_f32 v183, s70, v183\n" \
    "v_cvt_pk_bf16_f32 v40, v176, v177\n" \
    "buffer_load_dwordx4 v27, s[36:39], s75 offen offset:2048 nt lds\n" \
    "v_cvt_pk_bf16_f32 v41, v178, v179\n" \
    "v_cvt_pk_bf16_f32 v42, v180, v181\n" \
    "v_cvt_pk_bf16_f32 v43, v182, v183\n" \
    "v_accvgpr_write_b32 a24, v40\n" \
    "v_accvgpr_write_b32 a25, v41\n" \
    "v_accvgpr_write_b32 a26, v42\n" \
    "v_accvgpr_write_b32 a27, v43\n" \
    "buffer_load_dwordx4 v28, s[36:39], s75 offen offset:3072 nt lds\n" \
    "s_add_u32 m0, m0, 4096\n" \
    "v_mul_f32 v184, s70, v184\n" \
    "v_mul_f32 v185, s70, v185\n" \
    "v_mul_f32 v186, s70, v186\n" \
    "v_mul_f32 v187, s70, v187\n" \
    "v_mul_f32 v188, s70, v188\n" \
    "v_mul_f32 v189, s70, v189\n" \
    "v_mul_f32 v190, s70, v190\n" \
    "s_add_u32 s65, s65, 8192\n" \
    "s_and_b32 s65, s65, 131071\n" \
    "s_add_u32 s66, s66, s59\n" \
    "s_and_b32 s66, s66, 131071\n" \
    "v_mul_f32 v191, s70, v191\n" \
    "v_cvt_pk_bf16_f32 v44, v184, v185\n" \
    "v_cvt_pk_bf16_f32 v45, v186, v187\n" \
    "v_cvt_pk_bf16_f32 v46, v188, v189\n" \
    "v_cvt_pk_bf16_f32 v47, v190, v191\n" \
    "v_accvgpr_write_b32 a28, v44\n" \
    "v_accvgpr_write_b32 a29, v45\n" \
    "v_accvgpr_write_b32 a30, v46\n" \
    "v_accvgpr_write_b32 a31, v47\n" \
    "s_waitcnt vmcnt(16)\n" \
    "s_waitcnt lgkmcnt(0)\n" \
    "s_barrier\n" \
    "v_mfma_f32_32x32x16_bf16 v[96:111], a[64:67], a[0:3], 0\n" \
    "v_add_u32 v0, 65536, v20\n" \
    "v_add_u32 v29, 65536, v21\n" \
    "v_add_u32 v30, 65536, v22\n" \
    "v_add_u32 v31, 65536, v23\n" \
    "s_mov_b32 m0, s66\n" \
    "s_add_u32 s75, s65, 4096\n" \
    "buffer_load_dwordx4 v25, s[44:47], s65 offen offset:0 nt lds\n" \
    "buffer_load_dwordx4 v26, s[44:47], s65 offen offset:1024 nt lds\n" \
    "v_mfma_f32_32x32x16_bf16 v[96:111], a[68:71], a[4:7], v[96:111]\n" \
    "ds_read_b128 a[64:67], v20 offset:16384\n" \
    "buffer_load_dwordx4 v27, s[44:47], s65 offen offset:2048 nt lds\n" \
    "buffer_load_dwordx4 v28, s[44:47], s65 offen offset:3072 nt lds\n" \
    "s_add_u32 m0, m0, 4096\n" \
    "v_mfma_f32_32x32x16_bf16 v[96:111], a[72:75], a[8:11], v[96:111]\n" \
    "ds_read_b128 a[68:71], v21 offset:16384\n" \
    "buffer_load_dwordx4 v25, s[44:47], s75 offen offset:0 nt lds\n" \
    "buffer_load_dwordx4 v26, s[44:47], s75 offen offset:1024 nt lds\n" \
    "v_mfma_f32_32x32x16_bf16 v[96:111], a[76:79], a[12:15], v[96:111]\n" \
    "ds_read_b128 a[72:75], v20 offset:16640\n" \
    "buffer_load_dwordx4 v27, s[44:47], s75 offen offset:2048 nt lds\n" \
    "buffer_load_dwordx4 v28, s[44:47], s75 offen offset:3072 nt lds\n" \
    "s_add_u32 m0, m0, 4096\n" \
    "v_mfma_f32_32x32x16_bf16 v[96:111], a[80:83], a[16:19], v[96:111]\n" \
    "ds_read_b128 a[76:79], v21 offset:16640\n" \
    "buffer_load_dwordx4 v25, s[36:39], s65 offen offset:0 nt lds\n" \
    "buffer_load_dwordx4 v26, s[36:39], s65 offen offset:1024 nt lds\n" \
    "buffer_load_dwordx4 v27, s[36:39], s65 offen offset:2048 nt lds\n" \
    "v_mfma_f32_32x32x16_bf16 v[96:111], a[84:87], a[20:23], v[96:111]\n" \
    "ds_read_b128 a[80:83], v20 offset:16896\n" \
    "buffer_load_dwordx4 v28, s[36:39], s65 offen offset:3072 nt lds\n" \
    "s_add_u32 m0, m0, 4096\n" \
    "s_nop 0\n" \
    "buffer_load_dwordx4 v25, s[36:39], s75 offen offset:0 nt lds\n" \
    "v_mfma_f32_32x32x16_bf16 v[96:111], a[88:91], a[24:27], v[96:111]\n" \
    "ds_read_b128 a[84:87], v21 offset:16896\n" \
    "buffer_load_dwordx4 v26, s[36:39], s75 offen offset:1024 nt lds\n" \
    "buffer_load_dwordx4 v27, s[36:39], s75 offen offset:2048 nt lds\n" \
    "v_mfma_f32_32x32x16_bf16 v[96:111], a[92:95], a[28:31], v[96:111]\n" \
    "ds_read_b128 a[88:91], v20 offset:17152\n" \
    "buffer_load_dwordx4 v28, s[36:39], s75 offen offset:3072 nt lds\n" \
    "s_add_u32 m0, m0, 4096\n" \
    "s_add_u32 s65, s65, 8192\n" \
    "s_and_b32 s65, s65, 131071\n" \
    "s_add_u32 s66, s66, s59\n" \
    "s_and_b32 s66, s66, 131071\n" \
    "ds_read_b128 a[92:95], v21 offset:17152\n" \
    "s_nop 3\n" \
    "v_max3_f32 v46, v96, v97, v98\n" \
    "v_max3_f32 v47, v99, v100, v101\n" \
    "v_max3_f32 v46, v46, v102, v103\n" \
    "v_max3_f32 v47, v47, v104, v105\n" \
    "v_max3_f32 v46, v46, v106, v107\n" \
    "v_max3_f32 v47, v47, v108, v109\n" \
    "v_max3_f32 v46, v46, v110, v111\n" \
    "v_max_f32 v46, v46, v47\n" \
    "v_mov_b32 v47, v46\n" \
    "s_nop 1\n" \
    "v_permlane32_swap_b32 v46, v47\n" \
    "v_max_f32 v46, v46, v47\n" \
    "v_sub_f32 v32, 0, v46\n" \
    "v_sub_f32 v33, 0, v46\n" \
    "v_pk_add_f32 v[96:97], v[96:97], v[32:33] op_sel_hi:[1,0]\n" \
    "v_pk_add_f32 v[98:99], v[98:99], v[32:33] op_sel_hi:[1,0]\n" \
    "v_pk_add_f32 v[100:101], v[100:101], v[32:33] op_sel_hi:[1,0]\n" \
    "v_pk_add_f32 v[102:103], v[102:103], v[32:33] op_sel_hi:[1,0]\n" \
    "v_pk_add_f32 v[104:105], v[104:105], v[32:33] op_sel_hi:[1,0]\n" \
    "v_pk_add_f32 v[106:107], v[106:107], v[32:33] op_sel_hi:[1,0]\n" \
    "v_pk_add_f32 v[108:109], v[108:109], v[32:33] op_sel_hi:[1,0]\n" \
    "v_pk_add_f32 v[110:111], v[110:111], v[32:33] op_sel_hi:[1,0]\n" \
    "v_mov_b64 v[64:65], v[32:33]\n" \
    "v_mov_b64 v[66:67], v[32:33]\n" \
    "v_mov_b64 v[68:69], v[32:33]\n" \
    "v_mov_b64 v[70:71], v[32:33]\n" \
    "v_mov_b64 v[72:73], v[32:33]\n" \
    "v_mov_b64 v[74:75], v[32:33]\n" \
    "v_mov_b64 v[76:77], v[32:33]\n" \
    "v_mov_b64 v[78:79], v[32:33]\n" \
    "v_mov_b64 v[80:81], v[32:33]\n" \
    "v_mov_b64 v[82:83], v[32:33]\n" \
    "v_mov_b64 v[84:85], v[32:33]\n" \
    "v_mov_b64 v[86:87], v[32:33]\n" \
    "v_mov_b64 v[88:89], v[32:33]\n" \
    "v_mov_b64 v[90:91], v[32:33]\n" \
    "v_mov_b64 v[92:93], v[32:33]\n" \
    "v_mov_b64 v[94:95], v[32:33]\n" \
    "s_waitcnt lgkmcnt(0)\n" \
    "s_nop 1\n" \
    "v_mfma_f32_32x32x16_bf16 v[112:127], a[64:67], a[0:3], v[80:95]\n" \
    "ds_read_b64_tr_b16 a[96:97], v22 offset:0\n" \
    "v_exp_f32 v40, v96\n" \
    "v_exp_f32 v41, v97\n" \
    "ds_read_b64_tr_b16 a[98:99], v23 offset:0\n" \
    "v_exp_f32 v42, v98\n" \
    "v_exp_f32 v43, v99\n" \
    "s_mov_b32 m0, s66\n" \
    "s_add_u32 s75, s65, 4096\n" \
    "ds_read_b64_tr_b16 a[100:101], v22 offset:256\n" \
    "v_add_f32 v36, v40, v41\n" \
    "v_cvt_pk_bf16_f32 v48, v40, v41\n" \
    "v_mfma_f32_32x32x16_bf16 v[112:127], a[68:71], a[4:7], v[112:127]\n" \
    "ds_read_b64_tr_b16 a[102:103], v23 offset:256\n" \
    "v_exp_f32 v44, v100\n" \
    "v_exp_f32 v45, v101\n" \
    "buffer_load_dwordx4 v25, s[44:47], s65 offen offset:0 nt lds\n" \
    "ds_read_b64_tr_b16 a[104:105], v22 offset:512\n" \
    "v_add_f32 v36, v36, v42\n" \
    "v_add_f32 v36, v36, v43\n" \
    "v_mfma_f32_32x32x16_bf16 v[112:127], a[72:75], a[8:11], v[112:127]\n" \
    "ds_read_b64_tr_b16 a[106:107], v23 offset:512\n" \
    "v_cvt_pk_bf16_f32 v49, v42, v43\n" \
    "v_exp_f32 v46, v102\n" \
    "buffer_load_dwordx4 v26, s[44:47], s65 offen offset:1024 nt lds\n" \
    "ds_read_b64_tr_b16 a[108:109], v22 offset:768\n" \
    "v_exp_f32 v47, v103\n" \
    "v_add_f32 v36, v36, v44\n" \
    "ds_read_b64_tr_b16 a[110:111], v23 offset:768\n" \
    "v_add_f32 v36, v36, v45\n" \
    "v_cvt_pk_bf16_f32 v50, v44, v45\n" \
    "buffer_load_dwordx4 v27, s[44:47], s65 offen offset:2048 nt lds\n" \
    "v_mfma_f32_32x32x16_bf16 v[112:127], a[76:79], a[12:15], v[112:127]\n" \
    "ds_read_b64_tr_b16 a[112:113], v22 offset:4096\n" \
    "v_exp_f32 v40, v104\n" \
    "v_exp_f32 v41, v105\n" \
    "ds_read_b64_tr_b16 a[114:115], v23 offset:4096\n" \
    "v_add_f32 v36, v36, v46\n" \
    "v_add_f32 v36, v36, v47\n" \
    "buffer_load_dwordx4 v28, s[44:47], s65 offen offset:3072 nt lds\n" \
    "s_add_u32 m0, m0, 4096\n" \
    "v_mfma_f32_32x32x16_bf16 v[112:127], a[80:83], a[16:19], v[112:127]\n" \
    "ds_read_b64_tr_b16 a[116:117], v22 offset:4352\n" \
    "v_cvt_pk_bf16_f32 v51, v46, v47\n" \
    "v_exp_f32 v42, v106\n" \
    "ds_read_b64_tr_b16 a[118:119], v23 offset:4352\n" \
    "v_exp_f32 v43, v107\n" \
    "v_add_f32 v36, v36, v40\n" \
    "buffer_load_dwordx4 v25, s[44:47], s75 offen offset:0 nt lds\n" \
    "ds_read_b64_tr_b16 a[120:121], v22 offset:4608\n" \
    "v_add_f32 v36, v36, v41\n" \
    "v_cvt_pk_bf16_f32 v52, v40, v41\n" \
    "v_mfma_f32_32x32x16_bf16 v[112:127], a[84:87], a[20:23], v[112:127]\n" \
    "ds_read_b64_tr_b16 a[122:123], v23 offset:4608\n" \
    "v_exp_f32 v44, v108\n" \
    "v_exp_f32 v45, v109\n" \
    "buffer_load_dwordx4 v26, s[44:47], s75 offen offset:1024 nt lds\n" \
    "ds_read_b64_tr_b16 a[124:125], v22 offset:4864\n" \
    "v_add_f32 v36, v36, v42\n" \
    "v_add_f32 v36, v36, v43\n" \
    "v_mfma_f32_32x32x16_bf16 v[112:127], a[88:91], a[24:27], v[112:127]\n" \
    "ds_read_b64_tr_b16 a[126:127], v23 offset:4864\n" \
    "v_cvt_pk_bf16_f32 v53, v42, v43\n" \
    "v_exp_f32 v46, v110\n" \
    "buffer_load_dwordx4 v27, s[44:47], s75 offen offset:2048 nt lds\n" \
    "v_exp_f32 v47, v111\n" \
    "v_add_f32 v36, v36, v44\n" \
    "v_add_f32 v36, v36, v45\n" \
    "v_cvt_pk_bf16_f32 v54, v44, v45\n" \
    "buffer_load_dwordx4 v28, s[44:47], s75 offen offset:3072 nt lds\n" \
    "s_add_u32 m0, m0, 4096\n" \
    "v_mfma_f32_32x32x16_bf16 v[112:127], a[92:95], a[28:31], v[112:127]\n" \
    "v_add_f32 v36, v36, v46\n" \
    "v_add_f32 v36, v36, v47\n" \
    "v_cvt_pk_bf16_f32 v55, v46, v47\n" \
    "buffer_load_dwordx4 v25, s[36:39], s65 offen offset:0 nt lds\n" \
    "v_cmp_lt_f32 vcc, s69, v36\n" \
    "s_cbranch_vccnz Lmha_stub_si0b\n" \
    "Lmha_back_si0b:\n" \
    "s_waitcnt lgkmcnt(1)\n" \
    "v_mfma_f32_32x32x16_bf16 v[128:143], a[96:99], v[48:51], 0\n" \
    "v_add_f32 v38, v38, v36\n" \
    "ds_read_b128 a[64:67], v20 offset:32768\n" \
    "buffer_load_dwordx4 v26, s[36:39], s65 offen offset:1024 nt lds\n" \
    "v_mfma_f32_32x32x16_bf16 v[144:159], a[100:103], v[48:51], 0\n" \
    "ds_read_b128 a[68:71], v21 offset:32768\n" \
    "buffer_load_dwordx4 v27, s[36:39], s65 offen offset:2048 nt lds\n" \
    "v_mfma_f32_32x32x16_bf16 v[160:175], a[104:107], v[48:51], 0\n" \
    "ds_read_b128 a[72:75], v20 offset:33024\n" \
    "buffer_load_dwordx4 v28, s[36:39], s65 offen offset:3072 nt lds\n" \
    "s_add_u32 m0, m0, 4096\n" \
    "v_mfma_f32_32x32x16_bf16 v[176:191], a[108:111], v[48:51], 0\n" \
    "ds_read_b128 a[76:79], v21 offset:33024\n" \
    "buffer_load_dwordx4 v25, s[36:39], s75 offen offset:0 nt lds\n" \
    "v_mfma_f32_32x32x16_bf16 v[128:143], a[112:115], v[52:55], v[128:143]\n" \
    "ds_read_b128 a[80:83], v20 offset:33280\n" \
    "buffer_load_dwordx4 v26, s[36:39], s75 offen offset:1024 nt lds\n" \
    "v_mfma_f32_32x32x16_bf16 v[144:159], a[116:119], v[52:55], v[144:159]\n" \
    "ds_read_b128 a[84:87], v21 offset:33280\n" \
    "buffer_load_dwordx4 v27, s[36:39], s75 offen offset:2048 nt lds\n" \
    "v_mfma_f32_32x32x16_bf16 v[160:175], a[120:123], v[52:55], v[160:175]\n" \
    "ds_read_b128 a[88:91], v20 offset:33536\n" \
    "buffer_load_dwordx4 v28, s[36:39], s75 offen offset:3072 nt lds\n" \
    "s_add_u32 m0, m0, 4096\n" \
    "s_waitcnt lgkmcnt(6)\n" \
    "v_mfma_f32_32x32x16_bf16 v[176:191], a[124:127], v[52:55], v[176:191]\n" \
    "ds_read_b128 a[92:95], v21 offset:33536\n" \
    "s_add_u32 s65, s65, 8192\n" \
    "s_and_b32 s65, s65, 131071\n" \
    "s_add_u32 s66, s66, s59\n" \
    "s_and_b32 s66, s66, 131071\n" \
    "v_mfma_f32_32x32x16_bf16 v[96:111], a[64:67], a[0:3], v[64:79]\n" \
    "ds_read_b64_tr_b16 a[96:97], v22 offset:16384\n" \
    "v_exp_f32 v40, v112\n" \
    "v_exp_f32 v41, v113\n" \
    "ds_read_b64_tr_b16 a[98:99], v23 offset:16384\n" \
    "v_exp_f32 v42, v114\n" \
    "v_exp_f32 v43, v115\n" \
    "s_mov_b32 m0, 131072\n" \
    "s_mov_b32 s75, 0\n" \
    "buffer_load_dwordx4 v5, s[28:31], s75 offen offset:0 lds\n" \
    "ds_read_b64_tr_b16 a[100:101], v22 offset:16640\n" \
    "v_add_f32 v37, v40, v41\n" \
    "v_cvt_pk_bf16_f32 v56, v40, v41\n" \
    "buffer_load_dwordx4 v6, s[28:31], s75 offen offset:1024 lds\n" \
    "s_waitcnt lgkmcnt(4)\n" \
    "v_mfma_f32_32x32x16_bf16 v[96:111], a[68:71], a[4:7], v[96:111]\n" \
    "ds_read_b64_tr_b16 a[102:103], v23 offset:16640\n" \
    "v_exp_f32 v44, v116\n" \
    "v_exp_f32 v45, v117\n" \
    "buffer_load_dwordx4 v7, s[28:31], s75 offen offset:2048 lds\n" \
    "ds_read_b64_tr_b16 a[104:105], v22 offset:16896\n" \
    "v_add_f32 v37, v37, v42\n" \
    "v_add_f32 v37, v37, v43\n" \
    "buffer_load_dwordx4 v8, s[28:31], s75 offen offset:3072 lds\n" \
    "v_mfma_f32_32x32x16_bf16 v[96:111], a[72:75], a[8:11], v[96:111]\n" \
    "ds_read_b64_tr_b16 a[106:107], v23 offset:16896\n" \
    "v_cvt_pk_bf16_f32 v57, v42, v43\n" \
    "v_exp_f32 v46, v118\n" \
    "s_mov_b32 m0, 135168\n" \
    "s_mov_b32 s75, 1179648\n" \
    "buffer_load_dwordx4 v5, s[28:31], s75 offen offset:0 lds\n" \
    "ds_read_b64_tr_b16 a[108:109], v22 offset:17152\n" \
    "v_exp_f32 v47, v119\n" \
    "v_add_f32 v37, v37, v44\n" \
    "buffer_load_dwordx4 v6, s[28:31], s75 offen offset:1024 lds\n" \
    "ds_read_b64_tr_b16 a[110:111], v23 offset:17152\n" \
    "v_add_f32 v37, v37, v45\n" \
    "v_cvt_pk_bf16_f32 v58, v44, v45\n" \
    "buffer_load_dwordx4 v7, s[28:31], s75 offen offset:2048 lds\n" \
    "v_mfma_f32_32x32x16_bf16 v[96:111], a[76:79], a[12:15], v[96:111]\n" \
    "ds_read_b64_tr_b16 a[112:113], v22 offset:20480\n" \
    "v_exp_f32 v40, v120\n" \
    "v_exp_f32 v41, v121\n" \
    "buffer_load_dwordx4 v8, s[28:31], s75 offen offset:3072 lds\n" \
    "ds_read_b64_tr_b16 a[114:115], v23 offset:20480\n" \
    "v_add_f32 v37, v37, v46\n" \
    "v_add_f32 v37, v37, v47\n" \
    "s_mov_b32 m0, 139264\n" \
    "s_mov_b32 s75, 2359296\n" \
    "buffer_load_dwordx4 v5, s[28:31], s75 offen offset:0 lds\n" \
    "v_mfma_f32_32x32x16_bf16 v[96:111], a[80:83], a[16:19], v[96:111]\n" \
    "ds_read_b64_tr_b16 a[116:117], v22 offset:20736\n" \
    "v_cvt_pk_bf16_f32 v59, v46, v47\n" \
    "v_exp_f32 v42, v122\n" \
    "ds_read_b64_tr_b16 a[118:119], v23 offset:20736\n" \
    "v_exp_f32 v43, v123\n" \
    "v_add_f32 v37, v37, v40\n" \
    "buffer_load_dwordx4 v6, s[28:31], s75 offen offset:1024 lds\n" \
    "ds_read_b64_tr_b16 a[120:121], v22 offset:20992\n" \
    "v_add_f32 v37, v37, v41\n" \
    "v_cvt_pk_bf16_f32 v60, v40, v41\n" \
    "buffer_load_dwordx4 v7, s[28:31], s75 offen offset:2048 lds\n" \
    "v_mfma_f32_32x32x16_bf16 v[96:111], a[84:87], a[20:23], v[96:111]\n" \
    "ds_read_b64_tr_b16 a[122:123], v23 offset:20992\n" \
    "v_exp_f32 v44, v124\n" \
    "v_exp_f32 v45, v125\n" \
    "buffer_load_dwordx4 v8, s[28:31], s75 offen offset:3072 lds\n" \
    "ds_read_b64_tr_b16 a[124:125], v22 offset:21248\n" \
    "v_add_f32 v37, v37, v42\n" \
    "v_add_f32 v37, v37, v43\n" \
    "s_mov_b32 m0, 143360\n" \
    "s_mov_b32 s75, 3538944\n" \
    "buffer_load_dwordx4 v5, s[28:31], s75 offen offset:0 lds\n" \
    "v_mfma_f32_32x32x16_bf16 v[96:111], a[88:91], a[24:27], v[96:111]\n" \
    "ds_read_b64_tr_b16 a[126:127], v23 offset:21248\n" \
    "v_cvt_pk_bf16_f32 v61, v42, v43\n" \
    "v_exp_f32 v46, v126\n" \
    "buffer_load_dwordx4 v6, s[28:31], s75 offen offset:1024 lds\n" \
    "v_exp_f32 v47, v127\n" \
    "v_add_f32 v37, v37, v44\n" \
    "buffer_load_dwordx4 v7, s[28:31], s75 offen offset:2048 lds\n" \
    "v_add_f32 v37, v37, v45\n" \
    "v_cvt_pk_bf16_f32 v62, v44, v45\n" \
    "buffer_load_dwordx4 v8, s[28:31], s75 offen offset:3072 lds\n" \
    "s_waitcnt lgkmcnt(2)\n" \
    "v_mfma_f32_32x32x16_bf16 v[96:111], a[92:95], a[28:31], v[96:111]\n" \
    "v_add_f32 v37, v37, v46\n" \
    "v_add_f32 v37, v37, v47\n" \
    "s_mov_b32 m0, s66\n" \
    "s_add_u32 s75, s65, 4096\n" \
    "v_cvt_pk_bf16_f32 v63, v46, v47\n" \
    "buffer_load_dwordx4 v25, s[44:47], s65 offen offset:0 nt lds\n" \
    "v_cmp_lt_f32 vcc, s69, v37\n" \
    "s_cbranch_vccnz Lmha_stub_si1b\n" \
    "Lmha_back_si1b:\n" \
    "v_mfma_f32_32x32x16_bf16 v[192:207], a[96:99], v[56:59], 0\n" \
    "v_add_f32 v39, v39, v37\n" \
    "s_waitcnt vmcnt(49)\n" \
    "ds_read_b128 a[64:67], v20 offset:49152\n" \
    "buffer_load_dwordx4 v26, s[44:47], s65 offen offset:1024 nt lds\n" \
    "buffer_load_dwordx4 v27, s[44:47], s65 offen offset:2048 nt lds\n" \
    "v_mfma_f32_32x32x16_bf16 v[208:223], a[100:103], v[56:59], 0\n" \
    "ds_read_b128 a[68:71], v21 offset:49152\n" \
    "buffer_load_dwordx4 v28, s[44:47], s65 offen offset:3072 nt lds\n" \
    "s_add_u32 m0, m0, 4096\n" \
    "s_nop 0\n" \
    "buffer_load_dwordx4 v25, s[44:47], s75 offen offset:0 nt lds\n" \
    "v_mfma_f32_32x32x16_bf16 v[224:239], a[104:107], v[56:59], 0\n" \
    "ds_read_b128 a[72:75], v20 offset:49408\n" \
    "buffer_load_dwordx4 v26, s[44:47], s75 offen offset:1024 nt lds\n" \
    "buffer_load_dwordx4 v27, s[44:47], s75 offen offset:2048 nt lds\n" \
    "v_mfma_f32_32x32x16_bf16 v[240:255], a[108:111], v[56:59], 0\n" \
    "ds_read_b128 a[76:79], v21 offset:49408\n" \
    "buffer_load_dwordx4 v28, s[44:47], s75 offen offset:3072 nt lds\n" \
    "s_add_u32 m0, m0, 4096\n" \
    "s_nop 0\n" \
    "buffer_load_dwordx4 v25, s[36:39], s65 offen offset:0 nt lds\n" \
    "v_mfma_f32_32x32x16_bf16 v[192:207], a[112:115], v[60:63], v[192:207]\n" \
    "ds_read_b128 a[80:83], v20 offset:49664\n" \
    "buffer_load_dwordx4 v26, s[36:39], s65 offen offset:1024 nt lds\n" \
    "buffer_load_dwordx4 v27, s[36:39], s65 offen offset:2048 nt lds\n" \
    "v_mfma_f32_32x32x16_bf16 v[208:223], a[116:119], v[60:63], v[208:223]\n" \
    "ds_read_b128 a[84:87], v21 offset:49664\n" \
    "buffer_load_dwordx4 v28, s[36:39], s65 offen offset:3072 nt lds\n" \
    "s_add_u32 m0, m0, 4096\n" \
    "s_nop 0\n" \
    "buffer_load_dwordx4 v25, s[36:39], s75 offen offset:0 nt lds\n" \
    "v_mfma_f32_32x32x16_bf16 v[224:239], a[120:123], v[60:63], v[224:239]\n" \
    "ds_read_b128 a[88:91], v20 offset:49920\n" \
    "buffer_load_dwordx4 v26, s[36:39], s75 offen offset:1024 nt lds\n" \
    "buffer_load_dwordx4 v27, s[36:39], s75 offen offset:2048 nt lds\n" \
    "s_waitcnt lgkmcnt(4)\n" \
    "v_mfma_f32_32x32x16_bf16 v[240:255], a[124:127], v[60:63], v[240:255]\n" \
    "ds_read_b128 a[92:95], v21 offset:49920\n" \
    "buffer_load_dwordx4 v28, s[36:39], s75 offen offset:3072 nt lds\n" \
    "s_add_u32 m0, m0, 4096\n" \
    "s_add_u32 s65, s65, 8192\n" \
    "s_and_b32 s65, s65, 131071\n" \
    "s_add_u32 s66, s66, s59\n" \
    "s_and_b32 s66, s66, 131071\n" \
    "s_barrier\n" \
    "v_mfma_f32_32x32x16_bf16 v[112:127], a[64:67], a[0:3], v[80:95]\n" \
    "ds_read_b64_tr_b16 a[96:97], v22 offset:32768\n" \
    "v_exp_f32 v40, v96\n" \
    "v_exp_f32 v41, v97\n" \
    "ds_read_b64_tr_b16 a[98:99], v23 offset:32768\n" \
    "v_exp_f32 v42, v98\n" \
    "v_exp_f32 v43, v99\n" \
    "s_mov_b32 m0, 147456\n" \
    "s_mov_b32 s75, 36864\n" \
    "buffer_load_dwordx4 v5, s[28:31], s75 offen offset:0 lds\n" \
    "ds_read_b64_tr_b16 a[100:101], v22 offset:33024\n" \
    "v_add_f32 v36, v40, v41\n" \
    "v_cvt_pk_bf16_f32 v48, v40, v41\n" \
    "buffer_load_dwordx4 v6, s[28:31], s75 offen offset:1024 lds\n" \
    "v_mfma_f32_32x32x16_bf16 v[112:127], a[68:71], a[4:7], v[112:127]\n" \
    "ds_read_b64_tr_b16 a[102:103], v23 offset:33024\n" \
    "v_exp_f32 v44, v100\n" \
    "v_exp_f32 v45, v101\n" \
    "buffer_load_dwordx4 v7, s[28:31], s75 offen offset:2048 lds\n" \
    "ds_read_b64_tr_b16 a[104:105], v22 offset:33280\n" \
    "v_add_f32 v36, v36, v42\n" \
    "v_add_f32 v36, v36, v43\n" \
    "buffer_load_dwordx4 v8, s[28:31], s75 offen offset:3072 lds\n" \
    "v_mfma_f32_32x32x16_bf16 v[112:127], a[72:75], a[8:11], v[112:127]\n" \
    "ds_read_b64_tr_b16 a[106:107], v23 offset:33280\n" \
    "v_cvt_pk_bf16_f32 v49, v42, v43\n" \
    "v_exp_f32 v46, v102\n" \
    "s_mov_b32 m0, 151552\n" \
    "s_mov_b32 s75, 1216512\n" \
    "buffer_load_dwordx4 v5, s[28:31], s75 offen offset:0 lds\n" \
    "ds_read_b64_tr_b16 a[108:109], v22 offset:33536\n" \
    "v_exp_f32 v47, v103\n" \
    "v_add_f32 v36, v36, v44\n" \
    "buffer_load_dwordx4 v6, s[28:31], s75 offen offset:1024 lds\n" \
    "ds_read_b64_tr_b16 a[110:111], v23 offset:33536\n" \
    "v_add_f32 v36, v36, v45\n" \
    "v_cvt_pk_bf16_f32 v50, v44, v45\n" \
    "buffer_load_dwordx4 v7, s[28:31], s75 offen offset:2048 lds\n" \
    "s_waitcnt lgkmcnt(4)\n" \
    "v_mfma_f32_32x32x16_bf16 v[112:127], a[76:79], a[12:15], v[112:127]\n" \
    "ds_read_b64_tr_b16 a[112:113], v22 offset:36864\n" \
    "v_exp_f32 v40, v104\n" \
    "v_exp_f32 v41, v105\n" \
    "buffer_load_dwordx4 v8, s[28:31], s75 offen offset:3072 lds\n" \
    "ds_read_b64_tr_b16 a[114:115], v23 offset:36864\n" \
    "v_add_f32 v36, v36, v46\n" \
    "v_add_f32 v36, v36, v47\n" \
    "s_mov_b32 m0, 155648\n" \
    "s_mov_b32 s75, 2396160\n" \
    "buffer_load_dwordx4 v5, s[28:31], s75 offen offset:0 lds\n" \
    "v_mfma_f32_32x32x16_bf16 v[112:127], a[80:83], a[16:19], v[112:127]\n" \
    "ds_read_b64_tr_b16 a[116:117], v22 offset:37120\n" \
    "v_cvt_pk_bf16_f32 v51, v46, v47\n" \
    "v_exp_f32 v42, v106\n" \
    "ds_read_b64_tr_b16 a[118:119], v23 offset:37120\n" \
    "v_exp_f32 v43, v107\n" \
    "v_add_f32 v36, v36, v40\n" \
    "buffer_load_dwordx4 v6, s[28:31], s75 offen offset:1024 lds\n" \
    "ds_read_b64_tr_b16 a[120:121], v22 offset:37376\n" \
    "v_add_f32 v36, v36, v41\n" \
    "v_cvt_pk_bf16_f32 v52, v40, v41\n" \
    "buffer_load_dwordx4 v7, s[28:31], s75 offen offset:2048 lds\n" \
    "v_mfma_f32_32x32x16_bf16 v[112:127], a[84:87], a[20:23], v[112:127]\n" \
    "ds_read_b64_tr_b16 a[122:123], v23 offset:37376\n" \
    "v_exp_f32 v44, v108\n" \
    "v_exp_f32 v45, v109\n" \
    "buffer_load_dwordx4 v8, s[28:31], s75 offen offset:3072 lds\n" \
    "ds_read_b64_tr_b16 a[124:125], v22 offset:37632\n" \
    "v_add_f32 v36, v36, v42\n" \
    "v_add_f32 v36, v36, v43\n" \
    "s_mov_b32 m0, 159744\n" \
    "s_mov_b32 s75, 3575808\n" \
    "buffer_load_dwordx4 v5, s[28:31], s75 offen offset:0 lds\n" \
    "v_mfma_f32_32x32x16_bf16 v[112:127], a[88:91], a[24:27], v[112:127]\n" \
    "ds_read_b64_tr_b16 a[126:127], v23 offset:37632\n" \
    "v_cvt_pk_bf16_f32 v53, v42, v43\n" \
    "v_exp_f32 v46, v110\n" \
    "buffer_load_dwordx4 v6, s[28:31], s75 offen offset:1024 lds\n" \
    "v_exp_f32 v47, v111\n" \
    "v_add_f32 v36, v36, v44\n" \
    "buffer_load_dwordx4 v7, s[28:31], s75 offen offset:2048 lds\n" \
    "v_add_f32 v36, v36, v45\n" \
    "v_cvt_pk_bf16_f32 v54, v44, v45\n" \
    "buffer_load_dwordx4 v8, s[28:31], s75 offen offset:3072 lds\n" \
    "v_mfma_f32_32x32x16_bf16 v[112:127], a[92:95], a[28:31], v[112:127]\n" \
    "v_add_f32 v36, v36, v46\n" \
    "v_add_f32 v36, v36, v47\n" \
    "s_mov_b32 m0, s66\n" \
    "s_add_u32 s75, s65, 4096\n" \
    "v_cvt_pk_bf16_f32 v55, v46, v47\n" \
    "buffer_load_dwordx4 v25, s[44:47], s65 offen offset:0 nt lds\n" \
    "v_cmp_lt_f32 vcc, s69, v36\n" \
    "s_cbranch_vccnz Lmha_stub_si2b\n" \
    "Lmha_back_si2b:\n" \
    "v_mfma_f32_32x32x16_bf16 v[128:143], a[96:99], v[48:51], v[128:143]\n" \
    "v_add_f32 v38, v38, v36\n" \
    "s_waitcnt vmcnt(63)\n" \
    "ds_read_b128 a[64:67], v0 offset:0\n" \
    "buffer_load_dwordx4 v26, s[44:47], s65 offen offset:1024 nt lds\n" \
    "buffer_load_dwordx4 v27, s[44:47], s65 offen offset:2048 nt lds\n" \
    "v_mfma_f32_32x32x16_bf16 v[144:159], a[100:103], v[48:51], v[144:159]\n" \
    "ds_read_b128 a[68:71], v29 offset:0\n" \
    "buffer_load_dwordx4 v28, s[44:47], s65 offen offset:3072 nt lds\n" \
    "s_add_u32 m0, m0, 4096\n" \
    "s_nop 0\n" \
    "buffer_load_dwordx4 v25, s[44:47], s75 offen offset:0 nt lds\n" \
    "s_waitcnt lgkmcnt(2)\n" \
    "v_mfma_f32_32x32x16_bf16 v[160:175], a[104:107], v[48:51], v[160:175]\n" \
    "ds_read_b128 a[72:75], v0 offset:256\n" \
    "buffer_load_dwordx4 v26, s[44:47], s75 offen offset:1024 nt lds\n" \
    "buffer_load_dwordx4 v27, s[44:47], s75 offen offset:2048 nt lds\n" \
    "v_mfma_f32_32x32x16_bf16 v[176:191], a[108:111], v[48:51], v[176:191]\n" \
    "ds_read_b128 a[76:79], v29 offset:256\n" \
    "buffer_load_dwordx4 v28, s[44:47], s75 offen offset:3072 nt lds\n" \
    "s_add_u32 m0, m0, 4096\n" \
    "s_nop 0\n" \
    "buffer_load_dwordx4 v25, s[36:39], s65 offen offset:0 nt lds\n" \
    "v_mfma_f32_32x32x16_bf16 v[128:143], a[112:115], v[52:55], v[128:143]\n" \
    "ds_read_b128 a[80:83], v0 offset:512\n" \
    "buffer_load_dwordx4 v26, s[36:39], s65 offen offset:1024 nt lds\n" \
    "buffer_load_dwordx4 v27, s[36:39], s65 offen offset:2048 nt lds\n" \
    "v_mfma_f32_32x32x16_bf16 v[144:159], a[116:119], v[52:55], v[144:159]\n" \
    "ds_read_b128 a[84:87], v29 offset:512\n" \
    "buffer_load_dwordx4 v28, s[36:39], s65 offen offset:3072 nt lds\n" \
    "s_add_u32 m0, m0, 4096\n" \
    "s_nop 0\n" \
    "buffer_load_dwordx4 v25, s[36:39], s75 offen offset:0 nt lds\n" \
    "v_mfma_f32_32x32x16_bf16 v[160:175], a[120:123], v[52:55], v[160:175]\n" \
    "ds_read_b128 a[88:91], v0 offset:768\n" \
    "buffer_load_dwordx4 v26, s[36:39], s75 offen offset:1024 nt lds\n" \
    "buffer_load_dwordx4 v27, s[36:39], s75 offen offset:2048 nt lds\n" \
    "v_mfma_f32_32x32x16_bf16 v[176:191], a[124:127], v[52:55], v[176:191]\n" \
    "ds_read_b128 a[92:95], v29 offset:768\n" \
    "buffer_load_dwordx4 v28, s[36:39], s75 offen offset:3072 nt lds\n" \
    "s_add_u32 m0, m0, 4096\n" \
    "s_add_u32 s65, s65, 8192\n" \
    "s_and_b32 s65, s65, 131071\n" \
    "s_add_u32 s66, s66, s59\n" \
    "s_and_b32 s66, s66, 131071\n" \
    "s_waitcnt vmcnt(48)\n" \
    "s_barrier\n" \
    "s_waitcnt lgkmcnt(3)\n" \
    "v_mfma_f32_32x32x16_bf16 v[96:111], a[64:67], a[0:3], v[64:79]\n" \
    "ds_read_b64_tr_b16 a[96:97], v22 offset:49152\n" \
    "v_exp_f32 v40, v112\n" \
    "v_exp_f32 v41, v113\n" \
    "ds_read_b64_tr_b16 a[98:99], v23 offset:49152\n" \
    "v_exp_f32 v42, v114\n" \
    "v_exp_f32 v43, v115\n" \
    "s_mov_b32 m0, s66\n" \
    "s_add_u32 s75, s65, 4096\n" \
    "ds_read_b64_tr_b16 a[100:101], v22 offset:49408\n" \
    "v_add_f32 v37, v40, v41\n" \
    "v_cvt_pk_bf16_f32 v56, v40, v41\n" \
    "v_mfma_f32_32x32x16_bf16 v[96:111], a[68:71], a[4:7], v[96:111]\n" \
    "ds_read_b64_tr_b16 a[102:103], v23 offset:49408\n" \
    "v_exp_f32 v44, v116\n" \
    "v_exp_f32 v45, v117\n" \
    "buffer_load_dwordx4 v25, s[44:47], s65 offen offset:0 nt lds\n" \
    "ds_read_b64_tr_b16 a[104:105], v22 offset:49664\n" \
    "v_add_f32 v37, v37, v42\n" \
    "v_add_f32 v37, v37, v43\n" \
    "v_mfma_f32_32x32x16_bf16 v[96:111], a[72:75], a[8:11], v[96:111]\n" \
    "ds_read_b64_tr_b16 a[106:107], v23 offset:49664\n" \
    "v_cvt_pk_bf16_f32 v57, v42, v43\n" \
    "v_exp_f32 v46, v118\n" \
    "buffer_load_dwordx4 v26, s[44:47], s65 offen offset:1024 nt lds\n" \
    "ds_read_b64_tr_b16 a[108:109], v22 offset:49920\n" \
    "v_exp_f32 v47, v119\n" \
    "v_add_f32 v37, v37, v44\n" \
    "ds_read_b64_tr_b16 a[110:111], v23 offset:49920\n" \
    "v_add_f32 v37, v37, v45\n" \
    "v_cvt_pk_bf16_f32 v58, v44, v45\n" \
    "buffer_load_dwordx4 v27, s[44:47], s65 offen offset:2048 nt lds\n" \
    "v_mfma_f32_32x32x16_bf16 v[96:111], a[76:79], a[12:15], v[96:111]\n" \
    "ds_read_b64_tr_b16 a[112:113], v22 offset:53248\n" \
    "v_exp_f32 v40, v120\n" \
    "v_exp_f32 v41, v121\n" \
    "ds_read_b64_tr_b16 a[114:115], v23 offset:53248\n" \
    "v_add_f32 v37, v37, v46\n" \
    "v_add_f32 v37, v37, v47\n" \
    "buffer_load_dwordx4 v28, s[44:47], s65 offen offset:3072 nt lds\n" \
    "s_add_u32 m0, m0, 4096\n" \
    "v_mfma_f32_32x32x16_bf16 v[96:111], a[80:83], a[16:19], v[96:111]\n" \
    "ds_read_b64_tr_b16 a[116:117], v22 offset:53504\n" \
    "v_cvt_pk_bf16_f32 v59, v46, v47\n" \
    "v_exp_f32 v42, v122\n" \
    "ds_read_b64_tr_b16 a[118:119], v23 offset:53504\n" \
    "v_exp_f32 v43, v123\n" \
    "v_add_f32 v37, v37, v40\n" \
    "buffer_load_dwordx4 v25, s[44:47], s75 offen offset:0 nt lds\n" \
    "ds_read_b64_tr_b16 a[120:121], v22 offset:53760\n" \
    "v_add_f32 v37, v37, v41\n" \
    "v_cvt_pk_bf16_f32 v60, v40, v41\n" \
    "s_waitcnt lgkmcnt(5)\n" \
    "v_mfma_f32_32x32x16_bf16 v[96:111], a[84:87], a[20:23], v[96:111]\n" \
    "ds_read_b64_tr_b16 a[122:123], v23 offset:53760\n" \
    "v_exp_f32 v44, v124\n" \
    "v_exp_f32 v45, v125\n" \
    "buffer_load_dwordx4 v26, s[44:47], s75 offen offset:1024 nt lds\n" \
    "ds_read_b64_tr_b16 a[124:125], v22 offset:54016\n" \
    "v_add_f32 v37, v37, v42\n" \
    "v_add_f32 v37, v37, v43\n" \
    "v_mfma_f32_32x32x16_bf16 v[96:111], a[88:91], a[24:27], v[96:111]\n" \
    "ds_read_b64_tr_b16 a[126:127], v23 offset:54016\n" \
    "v_cvt_pk_bf16_f32 v61, v42, v43\n" \
    "v_exp_f32 v46, v126\n" \
    "buffer_load_dwordx4 v27, s[44:47], s75 offen offset:2048 nt lds\n" \
    "v_exp_f32 v47, v127\n" \
    "v_add_f32 v37, v37, v44\n" \
    "v_add_f32 v37, v37, v45\n" \
    "v_cvt_pk_bf16_f32 v62, v44, v45\n" \
    "buffer_load_dwordx4 v28, s[44:47], s75 offen offset:3072 nt lds\n" \
    "s_add_u32 m0, m0, 4096\n" \
    "v_mfma_f32_32x32x16_bf16 v[96:111], a[92:95], a[28:31], v[96:111]\n" \
    "v_add_f32 v37, v37, v46\n" \
    "v_add_f32 v37, v37, v47\n" \
    "v_cvt_pk_bf16_f32 v63, v46, v47\n" \
    "buffer_load_dwordx4 v25, s[36:39], s65 offen offset:0 nt lds\n" \
    "v_cmp_lt_f32 vcc, s69, v37\n" \
    "s_cbranch_vccnz Lmha_stub_si3b\n" \
    "Lmha_back_si3b:\n" \
    "v_mfma_f32_32x32x16_bf16 v[192:207], a[96:99], v[56:59], v[192:207]\n" \
    "v_add_f32 v39, v39, v37\n" \
    "ds_read_b128 a[64:67], v0 offset:16384\n" \
    "ds_read_b128 a[68:71], v29 offset:16384\n" \
    "buffer_load_dwordx4 v26, s[36:39], s65 offen offset:1024 nt lds\n" \
    "v_mfma_f32_32x32x16_bf16 v[208:223], a[100:103], v[56:59], v[208:223]\n" \
    "ds_read_b128 a[72:75], v0 offset:16640\n" \
    "ds_read_b128 a[76:79], v29 offset:16640\n" \
    "buffer_load_dwordx4 v27, s[36:39], s65 offen offset:2048 nt lds\n" \
    "v_mfma_f32_32x32x16_bf16 v[224:239], a[104:107], v[56:59], v[224:239]\n" \
    "ds_read_b128 a[80:83], v0 offset:16896\n" \
    "ds_read_b128 a[84:87], v29 offset:16896\n" \
    "buffer_load_dwordx4 v28, s[36:39], s65 offen offset:3072 nt lds\n" \
    "s_add_u32 m0, m0, 4096\n" \
    "v_mfma_f32_32x32x16_bf16 v[240:255], a[108:111], v[56:59], v[240:255]\n" \
    "ds_read_b128 a[88:91], v0 offset:17152\n" \
    "ds_read_b128 a[92:95], v29 offset:17152\n" \
    "buffer_load_dwordx4 v25, s[36:39], s75 offen offset:0 nt lds\n" \
    "s_waitcnt lgkmcnt(8)\n" \
    "v_mfma_f32_32x32x16_bf16 v[192:207], a[112:115], v[60:63], v[192:207]\n" \
    "ds_read_b64_tr_b16 a[128:129], v14 offset:0\n" \
    "ds_read_b64_tr_b16 a[130:131], v15 offset:0\n" \
    "buffer_load_dwordx4 v26, s[36:39], s75 offen offset:1024 nt lds\n" \
    "v_mfma_f32_32x32x16_bf16 v[208:223], a[116:119], v[60:63], v[208:223]\n" \
    "ds_read_b64_tr_b16 a[132:133], v10 offset:0\n" \
    "ds_read_b64_tr_b16 a[134:135], v11 offset:0\n" \
    "buffer_load_dwordx4 v27, s[36:39], s75 offen offset:2048 nt lds\n" \
    "v_mfma_f32_32x32x16_bf16 v[224:239], a[120:123], v[60:63], v[224:239]\n" \
    "ds_read_b64_tr_b16 a[136:137], v14 offset:2048\n" \
    "ds_read_b64_tr_b16 a[138:139], v15 offset:2048\n" \
    "buffer_load_dwordx4 v28, s[36:39], s75 offen offset:3072 nt lds\n" \
    "s_add_u32 m0, m0, 4096\n" \
    "v_mfma_f32_32x32x16_bf16 v[240:255], a[124:127], v[60:63], v[240:255]\n" \
    "ds_read_b64_tr_b16 a[140:141], v10 offset:2048\n" \
    "ds_read_b64_tr_b16 a[142:143], v11 offset:2048\n" \
    "s_add_u32 s65, s65, 8192\n" \
    "s_and_b32 s65, s65, 131071\n" \
    "s_add_u32 s66, s66, s59\n" \
    "s_and_b32 s66, s66, 131071\n" \
    "s_waitcnt vmcnt(32)\n" \
    "s_barrier\n" \
    "s_waitcnt lgkmcnt(6)\n" \
    "v_mfma_f32_32x32x16_bf16 v[112:127], a[64:67], a[0:3], v[80:95]\n" \
    "ds_read_b64_tr_b16 a[96:97], v30 offset:0\n" \
    "v_exp_f32 v40, v96\n" \
    "v_exp_f32 v41, v97\n" \
    "ds_read_b64_tr_b16 a[98:99], v31 offset:0\n" \
    "v_exp_f32 v42, v98\n" \
    "v_exp_f32 v43, v99\n" \
    "s_mov_b32 m0, 131072\n" \
    "s_mov_b32 s75, 73728\n" \
    "buffer_load_dwordx4 v5, s[28:31], s75 offen offset:0 lds\n" \
    "ds_read_b64_tr_b16 a[100:101], v30 offset:256\n" \
    "v_add_f32 v36, v40, v41\n" \
    "v_cvt_pk_bf16_f32 v48, v40, v41\n" \
    "buffer_load_dwordx4 v6, s[28:31], s75 offen offset:1024 lds\n" \
    "v_mfma_f32_32x32x16_bf16 v[112:127], a[68:71], a[4:7], v[112:127]\n" \
    "ds_read_b64_tr_b16 a[102:103], v31 offset:256\n" \
    "v_exp_f32 v44, v100\n" \
    "v_exp_f32 v45, v101\n" \
    "buffer_load_dwordx4 v7, s[28:31], s75 offen offset:2048 lds\n" \
    "ds_read_b64_tr_b16 a[104:105], v30 offset:512\n" \
    "v_add_f32 v36, v36, v42\n" \
    "v_add_f32 v36, v36, v43\n" \
    "buffer_load_dwordx4 v8, s[28:31], s75 offen offset:3072 lds\n" \
    "v_mfma_f32_32x32x16_bf16 v[112:127], a[72:75], a[8:11], v[112:127]\n" \
    "ds_read_b64_tr_b16 a[106:107], v31 offset:512\n" \
    "v_cvt_pk_bf16_f32 v49, v42, v43\n" \
    "v_exp_f32 v46, v102\n" \
    "s_mov_b32 m0, 135168\n" \
    "s_mov_b32 s75, 1253376\n" \
    "buffer_load_dwordx4 v5, s[28:31], s75 offen offset:0 lds\n" \
    "ds_read_b64_tr_b16 a[108:109], v30 offset:768\n" \
    "v_exp_f32 v47, v103\n" \
    "v_add_f32 v36, v36, v44\n" \
    "buffer_load_dwordx4 v6, s[28:31], s75 offen offset:1024 lds\n" \
    "ds_read_b64_tr_b16 a[110:111], v31 offset:768\n" \
    "v_add_f32 v36, v36, v45\n" \
    "v_cvt_pk_bf16_f32 v50, v44, v45\n" \
    "buffer_load_dwordx4 v7, s[28:31], s75 offen offset:2048 lds\n" \
    "v_mfma_f32_32x32x16_bf16 v[112:127], a[76:79], a[12:15], v[112:127]\n" \
    "ds_read_b64_tr_b16 a[112:113], v30 offset:4096\n" \
    "v_exp_f32 v40, v104\n" \
    "v_exp_f32 v41, v105\n" \
    "buffer_load_dwordx4 v8, s[28:31], s75 offen offset:3072 lds\n" \
    "ds_read_b64_tr_b16 a[114:115], v31 offset:4096\n" \
    "v_add_f32 v36, v36, v46\n" \
    "v_add_f32 v36, v36, v47\n" \
    "s_mov_b32 m0, 139264\n" \
    "s_mov_b32 s75, 2433024\n" \
    "buffer_load_dwordx4 v5, s[28:31], s75 offen offset:0 lds\n" \
    "v_mfma_f32_32x32x16_bf16 v[112:127], a[80:83], a[16:19], v[112:127]\n" \
    "ds_read_b64_tr_b16 a[116:117], v30 offset:4352\n" \
    "v_cvt_pk_bf16_f32 v51, v46, v47\n" \
    "v_exp_f32 v42, v106\n" \
    "ds_read_b64_tr_b16 a[118:119], v31 offset:4352\n" \
    "v_exp_f32 v43, v107\n" \
    "v_add_f32 v36, v36, v40\n" \
    "buffer_load_dwordx4 v6, s[28:31], s75 offen offset:1024 lds\n" \
    "ds_read_b64_tr_b16 a[120:121], v30 offset:4608\n" \
    "v_add_f32 v36, v36, v41\n" \
    "v_cvt_pk_bf16_f32 v52, v40, v41\n" \
    "buffer_load_dwordx4 v7, s[28:31], s75 offen offset:2048 lds\n" \
    "v_mfma_f32_32x32x16_bf16 v[112:127], a[84:87], a[20:23], v[112:127]\n" \
    "ds_read_b64_tr_b16 a[122:123], v31 offset:4608\n" \
    "v_exp_f32 v44, v108\n" \
    "v_exp_f32 v45, v109\n" \
    "buffer_load_dwordx4 v8, s[28:31], s75 offen offset:3072 lds\n" \
    "ds_read_b64_tr_b16 a[124:125], v30 offset:4864\n" \
    "v_add_f32 v36, v36, v42\n" \
    "v_add_f32 v36, v36, v43\n" \
    "s_mov_b32 m0, 143360\n" \
    "s_mov_b32 s75, 3612672\n" \
    "buffer_load_dwordx4 v5, s[28:31], s75 offen offset:0 lds\n" \
    "v_mfma_f32_32x32x16_bf16 v[112:127], a[88:91], a[24:27], v[112:127]\n" \
    "ds_read_b64_tr_b16 a[126:127], v31 offset:4864\n" \
    "v_cvt_pk_bf16_f32 v53, v42, v43\n" \
    "v_exp_f32 v46, v110\n" \
    "buffer_load_dwordx4 v6, s[28:31], s75 offen offset:1024 lds\n" \
    "v_exp_f32 v47, v111\n" \
    "v_add_f32 v36, v36, v44\n" \
    "buffer_load_dwordx4 v7, s[28:31], s75 offen offset:2048 lds\n" \
    "v_add_f32 v36, v36, v45\n" \
    "v_cvt_pk_bf16_f32 v54, v44, v45\n" \
    "buffer_load_dwordx4 v8, s[28:31], s75 offen offset:3072 lds\n" \
    "v_mfma_f32_32x32x16_bf16 v[112:127], a[92:95], a[28:31], v[112:127]\n" \
    "v_add_f32 v36, v36, v46\n" \
    "v_add_f32 v36, v36, v47\n" \
    "s_mov_b32 m0, s66\n" \
    "s_add_u32 s75, s65, 4096\n" \
    "v_cvt_pk_bf16_f32 v55, v46, v47\n" \
    "buffer_load_dwordx4 v25, s[44:47], s65 offen offset:0 nt lds\n" \
    "v_cmp_lt_f32 vcc, s69, v36\n" \
    "s_cbranch_vccnz Lmha_stub_si4b\n" \
    "Lmha_back_si4b:\n" \
    "s_waitcnt lgkmcnt(0)\n" \
    "v_mfma_f32_32x32x16_bf16 v[128:143], a[96:99], v[48:51], v[128:143]\n" \
    "v_add_f32 v38, v38, v36\n" \
    "ds_read_b128 a[64:67], v0 offset:32768\n" \
    "ds_read_b128 a[68:71], v29 offset:32768\n" \
    "buffer_load_dwordx4 v26, s[44:47], s65 offen offset:1024 nt lds\n" \
    "buffer_load_dwordx4 v27, s[44:47], s65 offen offset:2048 nt lds\n" \
    "v_mfma_f32_32x32x16_bf16 v[144:159], a[100:103], v[48:51], v[144:159]\n" \
    "ds_read_b128 a[72:75], v0 offset:33024\n" \
    "ds_read_b128 a[76:79], v29 offset:33024\n" \
    "buffer_load_dwordx4 v28, s[44:47], s65 offen offset:3072 nt lds\n" \
    "s_add_u32 m0, m0, 4096\n" \
    "s_nop 0\n" \
    "buffer_load_dwordx4 v25, s[44:47], s75 offen offset:0 nt lds\n" \
    "v_mfma_f32_32x32x16_bf16 v[160:175], a[104:107], v[48:51], v[160:175]\n" \
    "ds_read_b128 a[80:83], v0 offset:33280\n" \
    "ds_read_b128 a[84:87], v29 offset:33280\n" \
    "buffer_load_dwordx4 v26, s[44:47], s75 offen offset:1024 nt lds\n" \
    "buffer_load_dwordx4 v27, s[44:47], s75 offen offset:2048 nt lds\n" \
    "v_mfma_f32_32x32x16_bf16 v[176:191], a[108:111], v[48:51], v[176:191]\n" \
    "ds_read_b128 a[88:91], v0 offset:33536\n" \
    "ds_read_b128 a[92:95], v29 offset:33536\n" \
    "buffer_load_dwordx4 v28, s[44:47], s75 offen offset:3072 nt lds\n" \
    "s_add_u32 m0, m0, 4096\n" \
    "s_nop 0\n" \
    "buffer_load_dwordx4 v25, s[36:39], s65 offen offset:0 nt lds\n" \
    "v_mfma_f32_32x32x16_bf16 v[128:143], a[112:115], v[52:55], v[128:143]\n" \
    "ds_read_b64_tr_b16 a[144:145], v14 offset:16384\n" \
    "ds_read_b64_tr_b16 a[146:147], v15 offset:16384\n" \
    "buffer_load_dwordx4 v26, s[36:39], s65 offen offset:1024 nt lds\n" \
    "buffer_load_dwordx4 v27, s[36:39], s65 offen offset:2048 nt lds\n" \
    "v_mfma_f32_32x32x16_bf16 v[144:159], a[116:119], v[52:55], v[144:159]\n" \
    "ds_read_b64_tr_b16 a[148:149], v10 offset:16384\n" \
    "ds_read_b64_tr_b16 a[150:151], v11 offset:16384\n" \
    "buffer_load_dwordx4 v28, s[36:39], s65 offen offset:3072 nt lds\n" \
    "s_add_u32 m0, m0, 4096\n" \
    "s_nop 0\n" \
    "buffer_load_dwordx4 v25, s[36:39], s75 offen offset:0 nt lds\n" \
    "v_mfma_f32_32x32x16_bf16 v[160:175], a[120:123], v[52:55], v[160:175]\n" \
    "ds_read_b64_tr_b16 a[152:153], v14 offset:18432\n" \
    "ds_read_b64_tr_b16 a[154:155], v15 offset:18432\n" \
    "buffer_load_dwordx4 v26, s[36:39], s75 offen offset:1024 nt lds\n" \
    "buffer_load_dwordx4 v27, s[36:39], s75 offen offset:2048 nt lds\n" \
    "v_mfma_f32_32x32x16_bf16 v[176:191], a[124:127], v[52:55], v[176:191]\n" \
    "ds_read_b64_tr_b16 a[156:157], v10 offset:18432\n" \
    "ds_read_b64_tr_b16 a[158:159], v11 offset:18432\n" \
    "buffer_load_dwordx4 v28, s[36:39], s75 offen offset:3072 nt lds\n" \
    "s_add_u32 m0, m0, 4096\n" \
    "s_add_u32 s65, s65, 8192\n" \
    "s_and_b32 s65, s65, 131071\n" \
    "s_add_u32 s66, s66, s59\n" \
    "s_and_b32 s66, s66, 131071\n" \
    "s_barrier\n" \
    "s_waitcnt lgkmcnt(4)\n" \
    "v_mfma_f32_32x32x16_bf16 v[96:111], a[64:67], a[0:3], v[64:79]\n" \
    "ds_read_b64_tr_b16 a[96:97], v30 offset:16384\n" \
    "v_exp_f32 v40, v112\n" \
    "v_exp_f32 v41, v113\n" \
    "ds_read_b64_tr_b16 a[98:99], v31 offset:16384\n" \
    "v_exp_f32 v42, v114\n" \
    "v_exp_f32 v43, v115\n" \
    "s_mov_b32 m0, 147456\n" \
    "s_mov_b32 s75, 110592\n" \
    "buffer_load_dwordx4 v5, s[28:31], s75 offen offset:0 lds\n" \
    "ds_read_b64_tr_b16 a[100:101], v30 offset:16640\n" \
    "v_add_f32 v37, v40, v41\n" \
    "v_cvt_pk_bf16_f32 v56, v40, v41\n" \
    "buffer_load_dwordx4 v6, s[28:31], s75 offen offset:1024 lds\n" \
    "v_mfma_f32_32x32x16_bf16 v[96:111], a[68:71], a[4:7], v[96:111]\n" \
    "ds_read_b64_tr_b16 a[102:103], v31 offset:16640\n" \
    "v_exp_f32 v44, v116\n" \
    "v_exp_f32 v45, v117\n" \
    "buffer_load_dwordx4 v7, s[28:31], s75 offen offset:2048 lds\n" \
    "ds_read_b64_tr_b16 a[104:105], v30 offset:16896\n" \
    "v_add_f32 v37, v37, v42\n" \
    "v_add_f32 v37, v37, v43\n" \
    "buffer_load_dwordx4 v8, s[28:31], s75 offen offset:3072 lds\n" \
    "v_mfma_f32_32x32x16_bf16 v[96:111], a[72:75], a[8:11], v[96:111]\n" \
    "ds_read_b64_tr_b16 a[106:107], v31 offset:16896\n" \
    "v_cvt_pk_bf16_f32 v57, v42, v43\n" \
    "v_exp_f32 v46, v118\n" \
    "s_mov_b32 m0, 151552\n" \
    "s_mov_b32 s75, 1290240\n" \
    "buffer_load_dwordx4 v5, s[28:31], s75 offen offset:0 lds\n" \
    "ds_read_b64_tr_b16 a[108:109], v30 offset:17152\n" \
    "v_exp_f32 v47, v119\n" \
    "v_add_f32 v37, v37, v44\n" \
    "buffer_load_dwordx4 v6, s[28:31], s75 offen offset:1024 lds\n" \
    "ds_read_b64_tr_b16 a[110:111], v31 offset:17152\n" \
    "v_add_f32 v37, v37, v45\n" \
    "v_cvt_pk_bf16_f32 v58, v44, v45\n" \
    "buffer_load_dwordx4 v7, s[28:31], s75 offen offset:2048 lds\n" \
    "v_mfma_f32_32x32x16_bf16 v[96:111], a[76:79], a[12:15], v[96:111]\n" \
    "ds_read_b64_tr_b16 a[112:113], v30 offset:20480\n" \
    "v_exp_f32 v40, v120\n" \
    "v_exp_f32 v41, v121\n" \
    "buffer_load_dwordx4 v8, s[28:31], s75 offen offset:3072 lds\n" \
    "ds_read_b64_tr_b16 a[114:115], v31 offset:20480\n" \
    "v_add_f32 v37, v37, v46\n" \
    "v_add_f32 v37, v37, v47\n" \
    "s_mov_b32 m0, 155648\n" \
    "s_mov_b32 s75, 2469888\n" \
    "buffer_load_dwordx4 v5, s[28:31], s75 offen offset:0 lds\n" \
    "v_mfma_f32_32x32x16_bf16 v[96:111], a[80:83], a[16:19], v[96:111]\n" \
    "ds_read_b64_tr_b16 a[116:117], v30 offset:20736\n" \
    "v_cvt_pk_bf16_f32 v59, v46, v47\n" \
    "v_exp_f32 v42, v122\n" \
    "ds_read_b64_tr_b16 a[118:119], v31 offset:20736\n" \
    "v_exp_f32 v43, v123\n" \
    "v_add_f32 v37, v37, v40\n" \
    "buffer_load_dwordx4 v6, s[28:31], s75 offen offset:1024 lds\n" \
    "ds_read_b64_tr_b16 a[120:121], v30 offset:20992\n" \
    "v_add_f32 v37, v37, v41\n" \
    "v_cvt_pk_bf16_f32 v60, v40, v41\n" \
    "buffer_load_dwordx4 v7, s[28:31], s75 offen offset:2048 lds\n" \
    "v_mfma_f32_32x32x16_bf16 v[96:111], a[84:87], a[20:23], v[96:111]\n" \
    "ds_read_b64_tr_b16 a[122:123], v31 offset:20992\n" \
    "v_exp_f32 v44, v124\n" \
    "v_exp_f32 v45, v125\n" \
    "buffer_load_dwordx4 v8, s[28:31], s75 offen offset:3072 lds\n" \
    "ds_read_b64_tr_b16 a[124:125], v30 offset:21248\n" \
    "v_add_f32 v37, v37, v42\n" \
    "v_add_f32 v37, v37, v43\n" \
    "s_mov_b32 m0, 159744\n" \
    "s_mov_b32 s75, 3649536\n" \
    "buffer_load_dwordx4 v5, s[28:31], s75 offen offset:0 lds\n" \
    "v_mfma_f32_32x32x16_bf16 v[96:111], a[88:91], a[24:27], v[96:111]\n" \
    "ds_read_b64_tr_b16 a[126:127], v31 offset:21248\n" \
    "v_cvt_pk_bf16_f32 v61, v42, v43\n" \
    "v_exp_f32 v46, v126\n" \
    "buffer_load_dwordx4 v6, s[28:31], s75 offen offset:1024 lds\n" \
    "v_exp_f32 v47, v127\n" \
    "v_add_f32 v37, v37, v44\n" \
    "buffer_load_dwordx4 v7, s[28:31], s75 offen offset:2048 lds\n" \
    "v_add_f32 v37, v37, v45\n" \
    "v_cvt_pk_bf16_f32 v62, v44, v45\n" \
    "buffer_load_dwordx4 v8, s[28:31], s75 offen offset:3072 lds\n" \
    "v_mfma_f32_32x32x16_bf16 v[96:111], a[92:95], a[28:31], v[96:111]\n" \
    "v_add_f32 v37, v37, v46\n" \
    "v_add_f32 v37, v37, v47\n" \
    "s_mov_b32 m0, s66\n" \
    "s_add_u32 s75, s65, 4096\n" \
    "v_cvt_pk_bf16_f32 v63, v46, v47\n" \
    "buffer_load_dwordx4 v25, s[44:47], s65 offen offset:0 nt lds\n" \
    "v_cmp_lt_f32 vcc, s69, v37\n" \
    "s_cbranch_vccnz Lmha_stub_si5b\n" \
    "Lmha_back_si5b:\n" \
    "s_waitcnt lgkmcnt(0)\n" \
    "v_mfma_f32_32x32x16_bf16 v[192:207], a[96:99], v[56:59], v[192:207]\n" \
    "v_add_f32 v39, v39, v37\n" \
    "s_waitcnt vmcnt(63)\n" \
    "ds_read_b128 a[64:67], v0 offset:49152\n" \
    "buffer_load_dwordx4 v26, s[44:47], s65 offen offset:1024 nt lds\n" \
    "buffer_load_dwordx4 v27, s[44:47], s65 offen offset:2048 nt lds\n" \
    "v_mfma_f32_32x32x16_bf16 v[208:223], a[100:103], v[56:59], v[208:223]\n" \
    "ds_read_b128 a[68:71], v29 offset:49152\n" \
    "buffer_load_dwordx4 v28, s[44:47], s65 offen offset:3072 nt lds\n" \
    "s_add_u32 m0, m0, 4096\n" \
    "s_nop 0\n" \
    "buffer_load_dwordx4 v25, s[44:47], s75 offen offset:0 nt lds\n" \
    "v_mfma_f32_32x32x16_bf16 v[224:239], a[104:107], v[56:59], v[224:239]\n" \
    "ds_read_b128 a[72:75], v0 offset:49408\n" \
    "buffer_load_dwordx4 v26, s[44:47], s75 offen offset:1024 nt lds\n" \
    "buffer_load_dwordx4 v27, s[44:47], s75 offen offset:2048 nt lds\n" \
    "v_mfma_f32_32x32x16_bf16 v[240:255], a[108:111], v[56:59], v[240:255]\n" \
    "ds_read_b128 a[76:79], v29 offset:49408\n" \
    "buffer_load_dwordx4 v28, s[44:47], s75 offen offset:3072 nt lds\n" \
    "s_add_u32 m0, m0, 4096\n" \
    "s_nop 0\n" \
    "buffer_load_dwordx4 v25, s[36:39], s65 offen offset:0 nt lds\n" \
    "v_mfma_f32_32x32x16_bf16 v[192:207], a[112:115], v[60:63], v[192:207]\n" \
    "ds_read_b128 a[80:83], v0 offset:49664\n" \
    "buffer_load_dwordx4 v26, s[36:39], s65 offen offset:1024 nt lds\n" \
    "buffer_load_dwordx4 v27, s[36:39], s65 offen offset:2048 nt lds\n" \
    "v_mfma_f32_32x32x16_bf16 v[208:223], a[116:119], v[60:63], v[208:223]\n" \
    "ds_read_b128 a[84:87], v29 offset:49664\n" \
    "buffer_load_dwordx4 v28, s[36:39], s65 offen offset:3072 nt lds\n" \
    "s_add_u32 m0, m0, 4096\n" \
    "s_nop 0\n" \
    "buffer_load_dwordx4 v25, s[36:39], s75 offen offset:0 nt lds\n" \
    "v_mfma_f32_32x32x16_bf16 v[224:239], a[120:123], v[60:63], v[224:239]\n" \
    "ds_read_b128 a[88:91], v0 offset:49920\n" \
    "buffer_load_dwordx4 v26, s[36:39], s75 offen offset:1024 nt lds\n" \
    "buffer_load_dwordx4 v27, s[36:39], s75 offen offset:2048 nt lds\n" \
    "v_mfma_f32_32x32x16_bf16 v[240:255], a[124:127], v[60:63], v[240:255]\n" \
    "ds_read_b128 a[92:95], v29 offset:49920\n" \
    "buffer_load_dwordx4 v28, s[36:39], s75 offen offset:3072 nt lds\n" \
    "s_add_u32 m0, m0, 4096\n" \
    "s_add_u32 s65, s65, 8192\n" \
    "s_and_b32 s65, s65, 131071\n" \
    "s_add_u32 s66, s66, s59\n" \
    "s_and_b32 s66, s66, 131071\n" \
    "s_waitcnt vmcnt(48)\n" \
    "s_barrier\n" \
    "s_waitcnt lgkmcnt(3)\n" \
    "v_mfma_f32_32x32x16_bf16 v[112:127], a[64:67], a[0:3], v[80:95]\n" \
    "ds_read_b64_tr_b16 a[96:97], v30 offset:32768\n" \
    "v_exp_f32 v40, v96\n" \
    "v_exp_f32 v41, v97\n" \
    "ds_read_b64_tr_b16 a[98:99], v31 offset:32768\n" \
    "v_exp_f32 v42, v98\n" \
    "v_exp_f32 v43, v99\n" \
    "s_mov_b32 m0, s66\n" \
    "s_add_u32 s75, s65, 4096\n" \
    "ds_read_b64_tr_b16 a[100:101], v30 offset:33024\n" \
    "v_add_f32 v36, v40, v41\n" \
    "v_cvt_pk_bf16_f32 v48, v40, v41\n" \
    "v_mfma_f32_32x32x16_bf16 v[112:127], a[68:71], a[4:7], v[112:127]\n" \
    "ds_read_b64_tr_b16 a[102:103], v31 offset:33024\n" \
    "v_exp_f32 v44, v100\n" \
    "v_exp_f32 v45, v101\n" \
    "buffer_load_dwordx4 v25, s[44:47], s65 offen offset:0 nt lds\n" \
    "ds_read_b64_tr_b16 a[104:105], v30 offset:33280\n" \
    "v_add_f32 v36, v36, v42\n" \
    "v_add_f32 v36, v36, v43\n" \
    "v_mfma_f32_32x32x16_bf16 v[112:127], a[72:75], a[8:11], v[112:127]\n" \
    "ds_read_b64_tr_b16 a[106:107], v31 offset:33280\n" \
    "v_cvt_pk_bf16_f32 v49, v42, v43\n" \
    "v_exp_f32 v46, v102\n" \
    "buffer_load_dwordx4 v26, s[44:47], s65 offen offset:1024 nt lds\n" \
    "ds_read_b64_tr_b16 a[108:109], v30 offset:33536\n" \
    "v_exp_f32 v47, v103\n" \
    "v_add_f32 v36, v36, v44\n" \
    "ds_read_b64_tr_b16 a[110:111], v31 offset:33536\n" \
    "v_add_f32 v36, v36, v45\n" \
    "v_cvt_pk_bf16_f32 v50, v44, v45\n" \
    "buffer_load_dwordx4 v27, s[44:47], s65 offen offset:2048 nt lds\n" \
    "v_mfma_f32_32x32x16_bf16 v[112:127], a[76:79], a[12:15], v[112:127]\n" \
    "ds_read_b64_tr_b16 a[112:113], v30 offset:36864\n" \
    "v_exp_f32 v40, v104\n" \
    "v_exp_f32 v41, v105\n" \
    "ds_read_b64_tr_b16 a[114:115], v31 offset:36864\n" \
    "v_add_f32 v36, v36, v46\n" \
    "v_add_f32 v36, v36, v47\n" \
    "buffer_load_dwordx4 v28, s[44:47], s65 offen offset:3072 nt lds\n" \
    "s_add_u32 m0, m0, 4096\n" \
    "v_mfma_f32_32x32x16_bf16 v[112:127], a[80:83], a[16:19], v[112:127]\n" \
    "ds_read_b64_tr_b16 a[116:117], v30 offset:37120\n" \
    "v_cvt_pk_bf16_f32 v51, v46, v47\n" \
    "v_exp_f32 v42, v106\n" \
    "ds_read_b64_tr_b16 a[118:119], v31 offset:37120\n" \
    "v_exp_f32 v43, v107\n" \
    "v_add_f32 v36, v36, v40\n" \
    "buffer_load_dwordx4 v25, s[44:47], s75 offen offset:0 nt lds\n" \
    "ds_read_b64_tr_b16 a[120:121], v30 offset:37376\n" \
    "v_add_f32 v36, v36, v41\n" \
    "v_cvt_pk_bf16_f32 v52, v40, v41\n" \
    "s_waitcnt lgkmcnt(5)\n" \
    "v_mfma_f32_32x32x16_bf16 v[112:127], a[84:87], a[20:23], v[112:127]\n" \
    "ds_read_b64_tr_b16 a[122:123], v31 offset:37376\n" \
    "v_exp_f32 v44, v108\n" \
    "v_exp_f32 v45, v109\n" \
    "buffer_load_dwordx4 v26, s[44:47], s75 offen offset:1024 nt lds\n" \
    "ds_read_b64_tr_b16 a[124:125], v30 offset:37632\n" \
    "v_add_f32 v36, v36, v42\n" \
    "v_add_f32 v36, v36, v43\n" \
    "v_mfma_f32_32x32x16_bf16 v[112:127], a[88:91], a[24:27], v[112:127]\n" \
    "ds_read_b64_tr_b16 a[126:127], v31 offset:37632\n" \
    "v_cvt_pk_bf16_f32 v53, v42, v43\n" \
    "v_exp_f32 v46, v110\n" \
    "buffer_load_dwordx4 v27, s[44:47], s75 offen offset:2048 nt lds\n" \
    "v_exp_f32 v47, v111\n" \
    "v_add_f32 v36, v36, v44\n" \
    "v_add_f32 v36, v36, v45\n" \
    "v_cvt_pk_bf16_f32 v54, v44, v45\n" \
    "buffer_load_dwordx4 v28, s[44:47], s75 offen offset:3072 nt lds\n" \
    "s_add_u32 m0, m0, 4096\n" \
    "v_mfma_f32_32x32x16_bf16 v[112:127], a[92:95], a[28:31], v[112:127]\n" \
    "v_add_f32 v36, v36, v46\n" \
    "v_add_f32 v36, v36, v47\n" \
    "v_cvt_pk_bf16_f32 v55, v46, v47\n" \
    "buffer_load_dwordx4 v25, s[36:39], s65 offen offset:0 nt lds\n" \
    "v_cmp_lt_f32 vcc, s69, v36\n" \
    "s_cbranch_vccnz Lmha_stub_si6b\n" \
    "Lmha_back_si6b:\n" \
    "v_mfma_f32_32x32x16_bf16 v[128:143], a[96:99], v[48:51], v[128:143]\n" \
    "v_add_f32 v38, v38, v36\n" \
    "ds_read_b128 a[64:67], v20 offset:0\n" \
    "ds_read_b128 a[68:71], v21 offset:0\n" \
    "buffer_load_dwordx4 v26, s[36:39], s65 offen offset:1024 nt lds\n" \
    "v_mfma_f32_32x32x16_bf16 v[144:159], a[100:103], v[48:51], v[144:159]\n" \
    "ds_read_b128 a[72:75], v20 offset:256\n" \
    "ds_read_b128 a[76:79], v21 offset:256\n" \
    "buffer_load_dwordx4 v27, s[36:39], s65 offen offset:2048 nt lds\n" \
    "v_mfma_f32_32x32x16_bf16 v[160:175], a[104:107], v[48:51], v[160:175]\n" \
    "ds_read_b128 a[80:83], v20 offset:512\n" \
    "ds_read_b128 a[84:87], v21 offset:512\n" \
    "buffer_load_dwordx4 v28, s[36:39], s65 offen offset:3072 nt lds\n" \
    "s_add_u32 m0, m0, 4096\n" \
    "v_mfma_f32_32x32x16_bf16 v[176:191], a[108:111], v[48:51], v[176:191]\n" \
    "ds_read_b128 a[88:91], v20 offset:768\n" \
    "ds_read_b128 a[92:95], v21 offset:768\n" \
    "buffer_load_dwordx4 v25, s[36:39], s75 offen offset:0 nt lds\n" \
    "s_waitcnt lgkmcnt(8)\n" \
    "v_mfma_f32_32x32x16_bf16 v[128:143], a[112:115], v[52:55], v[128:143]\n" \
    "ds_read_b64_tr_b16 a[160:161], v14 offset:0\n" \
    "ds_read_b64_tr_b16 a[162:163], v15 offset:0\n" \
    "buffer_load_dwordx4 v26, s[36:39], s75 offen offset:1024 nt lds\n" \
    "v_mfma_f32_32x32x16_bf16 v[144:159], a[116:119], v[52:55], v[144:159]\n" \
    "ds_read_b64_tr_b16 a[164:165], v10 offset:0\n" \
    "ds_read_b64_tr_b16 a[166:167], v11 offset:0\n" \
    "buffer_load_dwordx4 v27, s[36:39], s75 offen offset:2048 nt lds\n" \
    "v_mfma_f32_32x32x16_bf16 v[160:175], a[120:123], v[52:55], v[160:175]\n" \
    "ds_read_b64_tr_b16 a[168:169], v14 offset:2048\n" \
    "ds_read_b64_tr_b16 a[170:171], v15 offset:2048\n" \
    "buffer_load_dwordx4 v28, s[36:39], s75 offen offset:3072 nt lds\n" \
    "s_add_u32 m0, m0, 4096\n" \
    "v_mfma_f32_32x32x16_bf16 v[176:191], a[124:127], v[52:55], v[176:191]\n" \
    "ds_read_b64_tr_b16 a[172:173], v10 offset:2048\n" \
    "ds_read_b64_tr_b16 a[174:175], v11 offset:2048\n" \
    "s_add_u32 s65, s65, 8192\n" \
    "s_and_b32 s65, s65, 131071\n" \
    "s_add_u32 s66, s66, s59\n" \
    "s_and_b32 s66, s66, 131071\n" \
    "s_waitcnt vmcnt(32)\n" \
    "s_barrier\n" \
    "s_waitcnt lgkmcnt(6)\n" \
    "v_mfma_f32_32x32x16_bf16 v[96:111], a[64:67], a[0:3], v[64:79]\n" \
    "ds_read_b64_tr_b16 a[96:97], v30 offset:49152\n" \
    "v_exp_f32 v40, v112\n" \
    "v_exp_f32 v41, v113\n" \
    "ds_read_b64_tr_b16 a[98:99], v31 offset:49152\n" \
    "v_exp_f32 v42, v114\n" \
    "v_exp_f32 v43, v115\n" \
    "s_mov_b32 m0, 131072\n" \
    "s_mov_b32 s75, 147456\n" \
    "buffer_load_dwordx4 v5, s[28:31], s75 offen offset:0 lds\n" \
    "ds_read_b64_tr_b16 a[100:101], v30 offset:49408\n" \
    "v_add_f32 v37, v40, v41\n" \
    "v_cvt_pk_bf16_f32 v56, v40, v41\n" \
    "buffer_load_dwordx4 v6, s[28:31], s75 offen offset:1024 lds\n" \
    "v_mfma_f32_32x32x16_bf16 v[96:111], a[68:71], a[4:7], v[96:111]\n" \
    "ds_read_b64_tr_b16 a[102:103], v31 offset:49408\n" \
    "v_exp_f32 v44, v116\n" \
    "v_exp_f32 v45, v117\n" \
    "buffer_load_dwordx4 v7, s[28:31], s75 offen offset:2048 lds\n" \
    "ds_read_b64_tr_b16 a[104:105], v30 offset:49664\n" \
    "v_add_f32 v37, v37, v42\n" \
    "v_add_f32 v37, v37, v43\n" \
    "buffer_load_dwordx4 v8, s[28:31], s75 offen offset:3072 lds\n" \
    "v_mfma_f32_32x32x16_bf16 v[96:111], a[72:75], a[8:11], v[96:111]\n" \
    "ds_read_b64_tr_b16 a[106:107], v31 offset:49664\n" \
    "v_cvt_pk_bf16_f32 v57, v42, v43\n" \
    "v_exp_f32 v46, v118\n" \
    "s_mov_b32 m0, 135168\n" \
    "s_mov_b32 s75, 1327104\n" \
    "buffer_load_dwordx4 v5, s[28:31], s75 offen offset:0 lds\n" \
    "ds_read_b64_tr_b16 a[108:109], v30 offset:49920\n" \
    "v_exp_f32 v47, v119\n" \
    "v_add_f32 v37, v37, v44\n" \
    "buffer_load_dwordx4 v6, s[28:31], s75 offen offset:1024 lds\n" \
    "ds_read_b64_tr_b16 a[110:111], v31 offset:49920\n" \
    "v_add_f32 v37, v37, v45\n" \
    "v_cvt_pk_bf16_f32 v58, v44, v45\n" \
    "buffer_load_dwordx4 v7, s[28:31], s75 offen offset:2048 lds\n" \
    "v_mfma_f32_32x32x16_bf16 v[96:111], a[76:79], a[12:15], v[96:111]\n" \
    "ds_read_b64_tr_b16 a[112:113], v30 offset:53248\n" \
    "v_exp_f32 v40, v120\n" \
    "v_exp_f32 v41, v121\n" \
    "buffer_load_dwordx4 v8, s[28:31], s75 offen offset:3072 lds\n" \
    "ds_read_b64_tr_b16 a[114:115], v31 offset:53248\n" \
    "v_add_f32 v37, v37, v46\n" \
    "v_add_f32 v37, v37, v47\n" \
    "s_mov_b32 m0, 139264\n" \
    "s_mov_b32 s75, 2506752\n" \
    "buffer_load_dwordx4 v5, s[28:31], s75 offen offset:0 lds\n" \
    "v_mfma_f32_32x32x16_bf16 v[96:111], a[80:83], a[16:19], v[96:111]\n" \
    "ds_read_b64_tr_b16 a[116:117], v30 offset:53504\n" \
    "v_cvt_pk_bf16_f32 v59, v46, v47\n" \
    "v_exp_f32 v42, v122\n" \
    "ds_read_b64_tr_b16 a[118:119], v31 offset:53504\n" \
    "v_exp_f32 v43, v123\n" \
    "v_add_f32 v37, v37, v40\n" \
    "buffer_load_dwordx4 v6, s[28:31], s75 offen offset:1024 lds\n" \
    "ds_read_b64_tr_b16 a[120:121], v30 offset:53760\n" \
    "v_add_f32 v37, v37, v41\n" \
    "v_cvt_pk_bf16_f32 v60, v40, v41\n" \
    "buffer_load_dwordx4 v7, s[28:31], s75 offen offset:2048 lds\n" \
    "v_mfma_f32_32x32x16_bf16 v[96:111], a[84:87], a[20:23], v[96:111]\n" \
    "ds_read_b64_tr_b16 a[122:123], v31 offset:53760\n" \
    "v_exp_f32 v44, v124\n" \
    "v_exp_f32 v45, v125\n" \
    "buffer_load_dwordx4 v8, s[28:31], s75 offen offset:3072 lds\n" \
    "ds_read_b64_tr_b16 a[124:125], v30 offset:54016\n" \
    "v_add_f32 v37, v37, v42\n" \
    "v_add_f32 v37, v37, v43\n" \
    "s_mov_b32 m0, 143360\n" \
    "s_mov_b32 s75, 3686400\n" \
    "buffer_load_dwordx4 v5, s[28:31], s75 offen offset:0 lds\n" \
    "v_mfma_f32_32x32x16_bf16 v[96:111], a[88:91], a[24:27], v[96:111]\n" \
    "ds_read_b64_tr_b16 a[126:127], v31 offset:54016\n" \
    "v_cvt_pk_bf16_f32 v61, v42, v43\n" \
    "v_exp_f32 v46, v126\n" \
    "buffer_load_dwordx4 v6, s[28:31], s75 offen offset:1024 lds\n" \
    "v_exp_f32 v47, v127\n" \
    "v_add_f32 v37, v37, v44\n" \
    "buffer_load_dwordx4 v7, s[28:31], s75 offen offset:2048 lds\n" \
    "v_add_f32 v37, v37, v45\n" \
    "v_cvt_pk_bf16_f32 v62, v44, v45\n" \
    "buffer_load_dwordx4 v8, s[28:31], s75 offen offset:3072 lds\n" \
    "v_mfma_f32_32x32x16_bf16 v[96:111], a[92:95], a[28:31], v[96:111]\n" \
    "v_add_f32 v37, v37, v46\n" \
    "v_add_f32 v37, v37, v47\n" \
    "s_mov_b32 m0, s66\n" \
    "s_add_u32 s75, s65, 4096\n" \
    "v_cvt_pk_bf16_f32 v63, v46, v47\n" \
    "buffer_load_dwordx4 v25, s[44:47], s65 offen offset:0 nt lds\n" \
    "v_cmp_lt_f32 vcc, s69, v37\n" \
    "s_cbranch_vccnz Lmha_stub_si7b\n" \
    "Lmha_back_si7b:\n" \
    "s_waitcnt lgkmcnt(0)\n" \
    "v_mfma_f32_32x32x16_bf16 v[192:207], a[96:99], v[56:59], v[192:207]\n" \
    "v_add_f32 v39, v39, v37\n" \
    "ds_read_b128 a[64:67], v20 offset:16384\n" \
    "ds_read_b128 a[68:71], v21 offset:16384\n" \
    "buffer_load_dwordx4 v26, s[44:47], s65 offen offset:1024 nt lds\n" \
    "buffer_load_dwordx4 v27, s[44:47], s65 offen offset:2048 nt lds\n" \
    "v_mfma_f32_32x32x16_bf16 v[208:223], a[100:103], v[56:59], v[208:223]\n" \
    "ds_read_b128 a[72:75], v20 offset:16640\n" \
    "ds_read_b128 a[76:79], v21 offset:16640\n" \
    "buffer_load_dwordx4 v28, s[44:47], s65 offen offset:3072 nt lds\n" \
    "s_add_u32 m0, m0, 4096\n" \
    "s_nop 0\n" \
    "buffer_load_dwordx4 v25, s[44:47], s75 offen offset:0 nt lds\n" \
    "v_mfma_f32_32x32x16_bf16 v[224:239], a[104:107], v[56:59], v[224:239]\n" \
    "ds_read_b128 a[80:83], v20 offset:16896\n" \
    "ds_read_b128 a[84:87], v21 offset:16896\n" \
    "buffer_load_dwordx4 v26, s[44:47], s75 offen offset:1024 nt lds\n" \
    "buffer_load_dwordx4 v27, s[44:47], s75 offen offset:2048 nt lds\n" \
    "v_mfma_f32_32x32x16_bf16 v[240:255], a[108:111], v[56:59], v[240:255]\n" \
    "ds_read_b128 a[88:91], v20 offset:17152\n" \
    "ds_read_b128 a[92:95], v21 offset:17152\n" \
    "buffer_load_dwordx4 v28, s[44:47], s75 offen offset:3072 nt lds\n" \
    "s_add_u32 m0, m0, 4096\n" \
    "s_nop 0\n" \
    "buffer_load_dwordx4 v25, s[36:39], s65 offen offset:0 nt lds\n" \
    "v_mfma_f32_32x32x16_bf16 v[192:207], a[112:115], v[60:63], v[192:207]\n" \
    "ds_read_b64_tr_b16 a[176:177], v14 offset:16384\n" \
    "ds_read_b64_tr_b16 a[178:179], v15 offset:16384\n" \
    "buffer_load_dwordx4 v26, s[36:39], s65 offen offset:1024 nt lds\n" \
    "buffer_load_dwordx4 v27, s[36:39], s65 offen offset:2048 nt lds\n" \
    "v_mfma_f32_32x32x16_bf16 v[208:223], a[116:119], v[60:63], v[208:223]\n" \
    "ds_read_b64_tr_b16 a[180:181], v10 offset:16384\n" \
    "ds_read_b64_tr_b16 a[182:183], v11 offset:16384\n" \
    "buffer_load_dwordx4 v28, s[36:39], s65 offen offset:3072 nt lds\n" \
    "s_add_u32 m0, m0, 4096\n" \
    "s_nop 0\n" \
    "buffer_load_dwordx4 v25, s[36:39], s75 offen offset:0 nt lds\n" \
    "v_mfma_f32_32x32x16_bf16 v[224:239], a[120:123], v[60:63], v[224:239]\n" \
    "ds_read_b64_tr_b16 a[184:185], v14 offset:18432\n" \
    "ds_read_b64_tr_b16 a[186:187], v15 offset:18432\n" \
    "buffer_load_dwordx4 v26, s[36:39], s75 offen offset:1024 nt lds\n" \
    "buffer_load_dwordx4 v27, s[36:39], s75 offen offset:2048 nt lds\n" \
    "v_mfma_f32_32x32x16_bf16 v[240:255], a[124:127], v[60:63], v[240:255]\n" \
    "ds_read_b64_tr_b16 a[188:189], v10 offset:18432\n" \
    "ds_read_b64_tr_b16 a[190:191], v11 offset:18432\n" \
    "buffer_load_dwordx4 v28, s[36:39], s75 offen offset:3072 nt lds\n" \
    "s_add_u32 m0, m0, 4096\n" \
    "s_add_u32 s65, s65, 8192\n" \
    "s_and_b32 s65, s65, 131071\n" \
    "s_add_u32 s66, s66, s59\n" \
    "s_and_b32 s66, s66, 131071\n" \
    "s_barrier\n" \
    "s_waitcnt lgkmcnt(4)\n" \
    "v_mfma_f32_32x32x16_bf16 v[112:127], a[64:67], a[0:3], v[80:95]\n" \
    "ds_read_b64_tr_b16 a[96:97], v22 offset:0\n" \
    "v_exp_f32 v40, v96\n" \
    "v_exp_f32 v41, v97\n" \
    "ds_read_b64_tr_b16 a[98:99], v23 offset:0\n" \
    "v_exp_f32 v42, v98\n" \
    "v_exp_f32 v43, v99\n" \
    "s_mov_b32 m0, 147456\n" \
    "s_mov_b32 s75, 184320\n" \
    "buffer_load_dwordx4 v5, s[28:31], s75 offen offset:0 lds\n" \
    "ds_read_b64_tr_b16 a[100:101], v22 offset:256\n" \
    "v_add_f32 v36, v40, v41\n" \
    "v_cvt_pk_bf16_f32 v48, v40, v41\n" \
    "buffer_load_dwordx4 v6, s[28:31], s75 offen offset:1024 lds\n" \
    "v_mfma_f32_32x32x16_bf16 v[112:127], a[68:71], a[4:7], v[112:127]\n" \
    "ds_read_b64_tr_b16 a[102:103], v23 offset:256\n" \
    "v_exp_f32 v44, v100\n" \
    "v_exp_f32 v45, v101\n" \
    "buffer_load_dwordx4 v7, s[28:31], s75 offen offset:2048 lds\n" \
    "ds_read_b64_tr_b16 a[104:105], v22 offset:512\n" \
    "v_add_f32 v36, v36, v42\n" \
    "v_add_f32 v36, v36, v43\n" \
    "buffer_load_dwordx4 v8, s[28:31], s75 offen offset:3072 lds\n" \
    "v_mfma_f32_32x32x16_bf16 v[112:127], a[72:75], a[8:11], v[112:127]\n" \
    "ds_read_b64_tr_b16 a[106:107], v23 offset:512\n" \
    "v_cvt_pk_bf16_f32 v49, v42, v43\n" \
    "v_exp_f32 v46, v102\n" \
    "s_mov_b32 m0, 151552\n" \
    "s_mov_b32 s75, 1363968\n" \
    "buffer_load_dwordx4 v5, s[28:31], s75 offen offset:0 lds\n" \
    "ds_read_b64_tr_b16 a[108:109], v22 offset:768\n" \
    "v_exp_f32 v47, v103\n" \
    "v_add_f32 v36, v36, v44\n" \
    "buffer_load_dwordx4 v6, s[28:31], s75 offen offset:1024 lds\n" \
    "ds_read_b64_tr_b16 a[110:111], v23 offset:768\n" \
    "v_add_f32 v36, v36, v45\n" \
    "v_cvt_pk_bf16_f32 v50, v44, v45\n" \
    "buffer_load_dwordx4 v7, s[28:31], s75 offen offset:2048 lds\n" \
    "v_mfma_f32_32x32x16_bf16 v[112:127], a[76:79], a[12:15], v[112:127]\n" \
    "ds_read_b64_tr_b16 a[112:113], v22 offset:4096\n" \
    "v_exp_f32 v40, v104\n" \
    "v_exp_f32 v41, v105\n" \
    "buffer_load_dwordx4 v8, s[28:31], s75 offen offset:3072 lds\n" \
    "ds_read_b64_tr_b16 a[114:115], v23 offset:4096\n" \
    "v_add_f32 v36, v36, v46\n" \
    "v_add_f32 v36, v36, v47\n" \
    "s_mov_b32 m0, 155648\n" \
    "s_mov_b32 s75, 2543616\n" \
    "buffer_load_dwordx4 v5, s[28:31], s75 offen offset:0 lds\n" \
    "v_mfma_f32_32x32x16_bf16 v[112:127], a[80:83], a[16:19], v[112:127]\n" \
    "ds_read_b64_tr_b16 a[116:117], v22 offset:4352\n" \
    "v_cvt_pk_bf16_f32 v51, v46, v47\n" \
    "v_exp_f32 v42, v106\n" \
    "ds_read_b64_tr_b16 a[118:119], v23 offset:4352\n" \
    "v_exp_f32 v43, v107\n" \
    "v_add_f32 v36, v36, v40\n" \
    "buffer_load_dwordx4 v6, s[28:31], s75 offen offset:1024 lds\n" \
    "ds_read_b64_tr_b16 a[120:121], v22 offset:4608\n" \
    "v_add_f32 v36, v36, v41\n" \
    "v_cvt_pk_bf16_f32 v52, v40, v41\n" \
    "buffer_load_dwordx4 v7, s[28:31], s75 offen offset:2048 lds\n" \
    "v_mfma_f32_32x32x16_bf16 v[112:127], a[84:87], a[20:23], v[112:127]\n" \
    "ds_read_b64_tr_b16 a[122:123], v23 offset:4608\n" \
    "v_exp_f32 v44, v108\n" \
    "v_exp_f32 v45, v109\n" \
    "buffer_load_dwordx4 v8, s[28:31], s75 offen offset:3072 lds\n" \
    "ds_read_b64_tr_b16 a[124:125], v22 offset:4864\n" \
    "v_add_f32 v36, v36, v42\n" \
    "v_add_f32 v36, v36, v43\n" \
    "s_mov_b32 m0, 159744\n" \
    "s_mov_b32 s75, 3723264\n" \
    "buffer_load_dwordx4 v5, s[28:31], s75 offen offset:0 lds\n" \
    "v_mfma_f32_32x32x16_bf16 v[112:127], a[88:91], a[24:27], v[112:127]\n" \
    "ds_read_b64_tr_b16 a[126:127], v23 offset:4864\n" \
    "v_cvt_pk_bf16_f32 v53, v42, v43\n" \
    "v_exp_f32 v46, v110\n" \
    "buffer_load_dwordx4 v6, s[28:31], s75 offen offset:1024 lds\n" \
    "v_exp_f32 v47, v111\n" \
    "v_add_f32 v36, v36, v44\n" \
    "buffer_load_dwordx4 v7, s[28:31], s75 offen offset:2048 lds\n" \
    "v_add_f32 v36, v36, v45\n" \
    "v_cvt_pk_bf16_f32 v54, v44, v45\n" \
    "buffer_load_dwordx4 v8, s[28:31], s75 offen offset:3072 lds\n" \
    "v_mfma_f32_32x32x16_bf16 v[112:127], a[92:95], a[28:31], v[112:127]\n" \
    "v_add_f32 v36, v36, v46\n" \
    "v_add_f32 v36, v36, v47\n" \
    "s_mov_b32 m0, s66\n" \
    "s_add_u32 s75, s65, 4096\n" \
    "v_cvt_pk_bf16_f32 v55, v46, v47\n" \
    "buffer_load_dwordx4 v25, s[44:47], s65 offen offset:0 nt lds\n" \
    "v_cmp_lt_f32 vcc, s69, v36\n" \
    "s_cbranch_vccnz Lmha_stub_si8b\n" \
    "Lmha_back_si8b:\n" \
    "s_waitcnt lgkmcnt(0)\n" \
    "v_mfma_f32_32x32x16_bf16 v[128:143], a[96:99], v[48:51], v[128:143]\n" \
    "v_add_f32 v38, v38, v36\n" \
    "s_waitcnt vmcnt(63)\n" \
    "ds_read_b128 a[64:67], v20 offset:32768\n" \
    "buffer_load_dwordx4 v26, s[44:47], s65 offen offset:1024 nt lds\n" \
    "buffer_load_dwordx4 v27, s[44:47], s65 offen offset:2048 nt lds\n" \
    "v_mfma_f32_32x32x16_bf16 v[144:159], a[100:103], v[48:51], v[144:159]\n" \
    "ds_read_b128 a[68:71], v21 offset:32768\n" \
    "buffer_load_dwordx4 v28, s[44:47], s65 offen offset:3072 nt lds\n" \
    "s_add_u32 m0, m0, 4096\n" \
    "s_nop 0\n" \
    "buffer_load_dwordx4 v25, s[44:47], s75 offen offset:0 nt lds\n" \
    "v_mfma_f32_32x32x16_bf16 v[160:175], a[104:107], v[48:51], v[160:175]\n" \
    "ds_read_b128 a[72:75], v20 offset:33024\n" \
    "buffer_load_dwordx4 v26, s[44:47], s75 offen offset:1024 nt lds\n" \
    "buffer_load_dwordx4 v27, s[44:47], s75 offen offset:2048 nt lds\n" \
    "v_mfma_f32_32x32x16_bf16 v[176:191], a[108:111], v[48:51], v[176:191]\n" \
    "ds_read_b128 a[76:79], v21 offset:33024\n" \
    "buffer_load_dwordx4 v28, s[44:47], s75 offen offset:3072 nt lds\n" \
    "s_add_u32 m0, m0, 4096\n" \
    "s_nop 0\n" \
    "buffer_load_dwordx4 v25, s[36:39], s65 offen offset:0 nt lds\n" \
    "v_mfma_f32_32x32x16_bf16 v[128:143], a[112:115], v[52:55], v[128:143]\n" \
    "ds_read_b128 a[80:83], v20 offset:33280\n" \
    "buffer_load_dwordx4 v26, s[36:39], s65 offen offset:1024 nt lds\n" \
    "buffer_load_dwordx4 v27, s[36:39], s65 offen offset:2048 nt lds\n" \
    "v_mfma_f32_32x32x16_bf16 v[144:159], a[116:119], v[52:55], v[144:159]\n" \
    "ds_read_b128 a[84:87], v21 offset:33280\n" \
    "buffer_load_dwordx4 v28, s[36:39], s65 offen offset:3072 nt lds\n" \
    "s_add_u32 m0, m0, 4096\n" \
    "s_nop 0\n" \
    "buffer_load_dwordx4 v25, s[36:39], s75 offen offset:0 nt lds\n" \
    "v_mfma_f32_32x32x16_bf16 v[160:175], a[120:123], v[52:55], v[160:175]\n" \
    "ds_read_b128 a[88:91], v20 offset:33536\n" \
    "buffer_load_dwordx4 v26, s[36:39], s75 offen offset:1024 nt lds\n" \
    "buffer_load_dwordx4 v27, s[36:39], s75 offen offset:2048 nt lds\n" \
    "v_mfma_f32_32x32x16_bf16 v[176:191], a[124:127], v[52:55], v[176:191]\n" \
    "ds_read_b128 a[92:95], v21 offset:33536\n" \
    "buffer_load_dwordx4 v28, s[36:39], s75 offen offset:3072 nt lds\n" \
    "s_add_u32 m0, m0, 4096\n" \
    "s_add_u32 s65, s65, 8192\n" \
    "s_and_b32 s65, s65, 131071\n" \
    "s_add_u32 s66, s66, s59\n" \
    "s_and_b32 s66, s66, 131071\n" \
    "s_waitcnt vmcnt(48)\n" \
    "s_barrier\n" \
    "s_waitcnt lgkmcnt(3)\n" \
    "v_mfma_f32_32x32x16_bf16 v[96:111], a[64:67], a[0:3], v[64:79]\n" \
    "ds_read_b64_tr_b16 a[96:97], v22 offset:16384\n" \
    "v_exp_f32 v40, v112\n" \
    "v_exp_f32 v41, v113\n" \
    "ds_read_b64_tr_b16 a[98:99], v23 offset:16384\n" \
    "v_exp_f32 v42, v114\n" \
    "v_exp_f32 v43, v115\n" \
    "s_mov_b32 m0, s66\n" \
    "s_add_u32 s75, s65, 4096\n" \
    "ds_read_b64_tr_b16 a[100:101], v22 offset:16640\n" \
    "v_add_f32 v37, v40, v41\n" \
    "v_cvt_pk_bf16_f32 v56, v40, v41\n" \
    "v_mfma_f32_32x32x16_bf16 v[96:111], a[68:71], a[4:7], v[96:111]\n" \
    "ds_read_b64_tr_b16 a[102:103], v23 offset:16640\n" \
    "v_exp_f32 v44, v116\n" \
    "v_exp_f32 v45, v117\n" \
    "buffer_load_dwordx4 v25, s[44:47], s65 offen offset:0 nt lds\n" \
    "ds_read_b64_tr_b16 a[104:105], v22 offset:16896\n" \
    "v_add_f32 v37, v37, v42\n" \
    "v_add_f32 v37, v37, v43\n" \
    "v_mfma_f32_32x32x16_bf16 v[96:111], a[72:75], a[8:11], v[96:111]\n" \
    "ds_read_b64_tr_b16 a[106:107], v23 offset:16896\n" \
    "v_cvt_pk_bf16_f32 v57, v42, v43\n" \
    "v_exp_f32 v46, v118\n" \
    "buffer_load_dwordx4 v26, s[44:47], s65 offen offset:1024 nt lds\n" \
    "ds_read_b64_tr_b16 a[108:109], v22 offset:17152\n" \
    "v_exp_f32 v47, v119\n" \
    "v_add_f32 v37, v37, v44\n" \
    "ds_read_b64_tr_b16 a[110:111], v23 offset:17152\n" \
    "v_add_f32 v37, v37, v45\n" \
    "v_cvt_pk_bf16_f32 v58, v44, v45\n" \
    "buffer_load_dwordx4 v27, s[44:47], s65 offen offset:2048 nt lds\n" \
    "v_mfma_f32_32x32x16_bf16 v[96:111], a[76:79], a[12:15], v[96:111]\n" \
    "ds_read_b64_tr_b16 a[112:113], v22 offset:20480\n" \
    "v_exp_f32 v40, v120\n" \
    "v_exp_f32 v41, v121\n" \
    "ds_read_b64_tr_b16 a[114:115], v23 offset:20480\n" \
    "v_add_f32 v37, v37, v46\n" \
    "v_add_f32 v37, v37, v47\n" \
    "buffer_load_dwordx4 v28, s[44:47], s65 offen offset:3072 nt lds\n" \
    "s_add_u32 m0, m0, 4096\n" \
    "v_mfma_f32_32x32x16_bf16 v[96:111], a[80:83], a[16:19], v[96:111]\n" \
    "ds_read_b64_tr_b16 a[116:117], v22 offset:20736\n" \
    "v_cvt_pk_bf16_f32 v59, v46, v47\n" \
    "v_exp_f32 v42, v122\n" \
    "ds_read_b64_tr_b16 a[118:119], v23 offset:20736\n" \
    "v_exp_f32 v43, v123\n" \
    "v_add_f32 v37, v37, v40\n" \
    "buffer_load_dwordx4 v25, s[44:47], s75 offen offset:0 nt lds\n" \
    "ds_read_b64_tr_b16 a[120:121], v22 offset:20992\n" \
    "v_add_f32 v37, v37, v41\n" \
    "v_cvt_pk_bf16_f32 v60, v40, v41\n" \
    "s_waitcnt lgkmcnt(5)\n" \
    "v_mfma_f32_32x32x16_bf16 v[96:111], a[84:87], a[20:23], v[96:111]\n" \
    "ds_read_b64_tr_b16 a[122:123], v23 offset:20992\n" \
    "v_exp_f32 v44, v124\n" \
    "v_exp_f32 v45, v125\n" \
    "buffer_load_dwordx4 v26, s[44:47], s75 offen offset:1024 nt lds\n" \
    "ds_read_b64_tr_b16 a[124:125], v22 offset:21248\n" \
    "v_add_f32 v37, v37, v42\n" \
    "v_add_f32 v37, v37, v43\n" \
    "v_mfma_f32_32x32x16_bf16 v[96:111], a[88:91], a[24:27], v[96:111]\n" \
    "ds_read_b64_tr_b16 a[126:127], v23 offset:21248\n" \
    "v_cvt_pk_bf16_f32 v61, v42, v43\n" \
    "v_exp_f32 v46, v126\n" \
    "buffer_load_dwordx4 v27, s[44:47], s75 offen offset:2048 nt lds\n" \
    "v_exp_f32 v47, v127\n" \
    "v_add_f32 v37, v37, v44\n" \
    "v_add_f32 v37, v37, v45\n" \
    "v_cvt_pk_bf16_f32 v62, v44, v45\n" \
    "buffer_load_dwordx4 v28, s[44:47], s75 offen offset:3072 nt lds\n" \
    "s_add_u32 m0, m0, 4096\n" \
    "v_mfma_f32_32x32x16_bf16 v[96:111], a[92:95], a[28:31], v[96:111]\n" \
    "v_add_f32 v37, v37, v46\n" \
    "v_add_f32 v37, v37, v47\n" \
    "v_cvt_pk_bf16_f32 v63, v46, v47\n" \
    "buffer_load_dwordx4 v25, s[36:39], s65 offen offset:0 nt lds\n" \
    "v_cmp_lt_f32 vcc, s69, v37\n" \
    "s_cbranch_vccnz Lmha_stub_si9b\n" \
    "Lmha_back_si9b:\n" \
    "v_mfma_f32_32x32x16_bf16 v[192:207], a[96:99], v[56:59], v[192:207]\n" \
    "v_add_f32 v39, v39, v37\n" \
    "ds_read_b128 a[64:67], v20 offset:49152\n" \
    "ds_read_b128 a[68:71], v21 offset:49152\n" \
    "buffer_load_dwordx4 v26, s[36:39], s65 offen offset:1024 nt lds\n" \
    "v_mfma_f32_32x32x16_bf16 v[208:223], a[100:103], v[56:59], v[208:223]\n" \
    "ds_read_b128 a[72:75], v20 offset:49408\n" \
    "ds_read_b128 a[76:79], v21 offset:49408\n" \
    "buffer_load_dwordx4 v27, s[36:39], s65 offen offset:2048 nt lds\n" \
    "v_mfma_f32_32x32x16_bf16 v[224:239], a[104:107], v[56:59], v[224:239]\n" \
    "ds_read_b128 a[80:83], v20 offset:49664\n" \
    "ds_read_b128 a[84:87], v21 offset:49664\n" \
    "buffer_load_dwordx4 v28, s[36:39], s65 offen offset:3072 nt lds\n" \
    "s_add_u32 m0, m0, 4096\n" \
    "v_mfma_f32_32x32x16_bf16 v[240:255], a[108:111], v[56:59], v[240:255]\n" \
    "ds_read_b128 a[88:91], v20 offset:49920\n" \
    "ds_read_b128 a[92:95], v21 offset:49920\n" \
    "buffer_load_dwordx4 v25, s[36:39], s75 offen offset:0 nt lds\n" \
    "s_waitcnt lgkmcnt(8)\n" \
    "v_mfma_f32_32x32x16_bf16 v[192:207], a[112:115], v[60:63], v[192:207]\n" \
    "ds_read_b64_tr_b16 a[192:193], v14 offset:0\n" \
    "ds_read_b64_tr_b16 a[194:195], v15 offset:0\n" \
    "buffer_load_dwordx4 v26, s[36:39], s75 offen offset:1024 nt lds\n" \
    "v_mfma_f32_32x32x16_bf16 v[208:223], a[116:119], v[60:63], v[208:223]\n" \
    "ds_read_b64_tr_b16 a[196:197], v10 offset:0\n" \
    "ds_read_b64_tr_b16 a[198:199], v11 offset:0\n" \
    "buffer_load_dwordx4 v27, s[36:39], s75 offen offset:2048 nt lds\n" \
    "v_mfma_f32_32x32x16_bf16 v[224:239], a[120:123], v[60:63], v[224:239]\n" \
    "ds_read_b64_tr_b16 a[200:201], v14 offset:2048\n" \
    "ds_read_b64_tr_b16 a[202:203], v15 offset:2048\n" \
    "buffer_load_dwordx4 v28, s[36:39], s75 offen offset:3072 nt lds\n" \
    "s_add_u32 m0, m0, 4096\n" \
    "v_mfma_f32_32x32x16_bf16 v[240:255], a[124:127], v[60:63], v[240:255]\n" \
    "ds_read_b64_tr_b16 a[204:205], v10 offset:2048\n" \
    "ds_read_b64_tr_b16 a[206:207], v11 offset:2048\n" \
    "s_add_u32 s65, s65, 8192\n" \
    "s_and_b32 s65, s65, 131071\n" \
    "s_add_u32 s66, s66, s59\n" \
    "s_and_b32 s66, s66, 131071\n" \
    "s_waitcnt vmcnt(32)\n" \
    "s_barrier\n" \
    "s_waitcnt lgkmcnt(6)\n" \
    "v_mfma_f32_32x32x16_bf16 v[112:127], a[64:67], a[0:3], v[80:95]\n" \
    "ds_read_b64_tr_b16 a[96:97], v22 offset:32768\n" \
    "v_exp_f32 v40, v96\n" \
    "v_exp_f32 v41, v97\n" \
    "s_mov_b32 m0, 0\n" \
    "s_mov_b32 s75, 294912\n" \
    "buffer_load_dwordx4 v5, s[28:31], s75 offen offset:0 lds\n" \
    "ds_read_b64_tr_b16 a[98:99], v23 offset:32768\n" \
    "v_exp_f32 v42, v98\n" \
    "v_exp_f32 v43, v99\n" \
    "buffer_load_dwordx4 v6, s[28:31], s75 offen offset:1024 lds\n" \
    "ds_read_b64_tr_b16 a[100:101], v22 offset:33024\n" \
    "v_add_f32 v36, v40, v41\n" \
    "v_cvt_pk_bf16_f32 v48, v40, v41\n" \
    "buffer_load_dwordx4 v7, s[28:31], s75 offen offset:2048 lds\n" \
    "buffer_load_dwordx4 v8, s[28:31], s75 offen offset:3072 lds\n" \
    "v_mfma_f32_32x32x16_bf16 v[112:127], a[68:71], a[4:7], v[112:127]\n" \
    "ds_read_b64_tr_b16 a[102:103], v23 offset:33024\n" \
    "v_exp_f32 v44, v100\n" \
    "v_exp_f32 v45, v101\n" \
    "s_mov_b32 m0, 4096\n" \
    "s_mov_b32 s75, 1474560\n" \
    "buffer_load_dwordx4 v5, s[28:31], s75 offen offset:0 lds\n" \
    "ds_read_b64_tr_b16 a[104:105], v22 offset:33280\n" \
    "v_add_f32 v36, v36, v42\n" \
    "v_add_f32 v36, v36, v43\n" \
    "buffer_load_dwordx4 v6, s[28:31], s75 offen offset:1024 lds\n" \
    "v_mfma_f32_32x32x16_bf16 v[112:127], a[72:75], a[8:11], v[112:127]\n" \
    "ds_read_b64_tr_b16 a[106:107], v23 offset:33280\n" \
    "v_cvt_pk_bf16_f32 v49, v42, v43\n" \
    "v_exp_f32 v46, v102\n" \
    "buffer_load_dwordx4 v7, s[28:31], s75 offen offset:2048 lds\n" \
    "buffer_load_dwordx4 v8, s[28:31], s75 offen offset:3072 lds\n" \
    "ds_read_b64_tr_b16 a[108:109], v22 offset:33536\n" \
    "v_exp_f32 v47, v103\n" \
    "v_add_f32 v36, v36, v44\n" \
    "s_mov_b32 m0, 8192\n" \
    "s_mov_b32 s75, 2654208\n" \
    "buffer_load_dwordx4 v5, s[28:31], s75 offen offset:0 lds\n" \
    "ds_read_b64_tr_b16 a[110:111], v23 offset:33536\n" \
    "v_add_f32 v36, v36, v45\n" \
    "v_cvt_pk_bf16_f32 v50, v44, v45\n" \
    "buffer_load_dwordx4 v6, s[28:31], s75 offen offset:1024 lds\n" \
    "v_mfma_f32_32x32x16_bf16 v[112:127], a[76:79], a[12:15], v[112:127]\n" \
    "ds_read_b64_tr_b16 a[112:113], v22 offset:36864\n" \
    "v_exp_f32 v40, v104\n" \
    "v_exp_f32 v41, v105\n" \
    "buffer_load_dwordx4 v7, s[28:31], s75 offen offset:2048 lds\n" \
    "buffer_load_dwordx4 v8, s[28:31], s75 offen offset:3072 lds\n" \
    "ds_read_b64_tr_b16 a[114:115], v23 offset:36864\n" \
    "v_add_f32 v36, v36, v46\n" \
    "v_add_f32 v36, v36, v47\n" \
    "s_mov_b32 m0, 12288\n" \
    "s_mov_b32 s75, 3833856\n" \
    "buffer_load_dwordx4 v5, s[28:31], s75 offen offset:0 lds\n" \
    "v_mfma_f32_32x32x16_bf16 v[112:127], a[80:83], a[16:19], v[112:127]\n" \
    "ds_read_b64_tr_b16 a[116:117], v22 offset:37120\n" \
    "v_cvt_pk_bf16_f32 v51, v46, v47\n" \
    "v_exp_f32 v42, v106\n" \
    "buffer_load_dwordx4 v6, s[28:31], s75 offen offset:1024 lds\n" \
    "ds_read_b64_tr_b16 a[118:119], v23 offset:37120\n" \
    "v_exp_f32 v43, v107\n" \
    "v_add_f32 v36, v36, v40\n" \
    "buffer_load_dwordx4 v7, s[28:31], s75 offen offset:2048 lds\n" \
    "buffer_load_dwordx4 v8, s[28:31], s75 offen offset:3072 lds\n" \
    "ds_read_b64_tr_b16 a[120:121], v22 offset:37376\n" \
    "v_add_f32 v36, v36, v41\n" \
    "v_cvt_pk_bf16_f32 v52, v40, v41\n" \
    "s_mov_b32 m0, 131072\n" \
    "s_mov_b32 s75, 221184\n" \
    "buffer_load_dwordx4 v5, s[28:31], s75 offen offset:0 lds\n" \
    "v_mfma_f32_32x32x16_bf16 v[112:127], a[84:87], a[20:23], v[112:127]\n" \
    "ds_read_b64_tr_b16 a[122:123], v23 offset:37376\n" \
    "v_exp_f32 v44, v108\n" \
    "v_exp_f32 v45, v109\n" \
    "buffer_load_dwordx4 v6, s[28:31], s75 offen offset:1024 lds\n" \
    "ds_read_b64_tr_b16 a[124:125], v22 offset:37632\n" \
    "v_add_f32 v36, v36, v42\n" \
    "v_add_f32 v36, v36, v43\n" \
    "buffer_load_dwordx4 v7, s[28:31], s75 offen offset:2048 lds\n" \
    "buffer_load_dwordx4 v8, s[28:31], s75 offen offset:3072 lds\n" \
    "v_mfma_f32_32x32x16_bf16 v[112:127], a[88:91], a[24:27], v[112:127]\n" \
    "ds_read_b64_tr_b16 a[126:127], v23 offset:37632\n" \
    "v_cvt_pk_bf16_f32 v53, v42, v43\n" \
    "v_exp_f32 v46, v110\n" \
    "s_mov_b32 m0, 135168\n" \
    "s_mov_b32 s75, 1400832\n" \
    "buffer_load_dwordx4 v5, s[28:31], s75 offen offset:0 lds\n" \
    "v_exp_f32 v47, v111\n" \
    "v_add_f32 v36, v36, v44\n" \
    "buffer_load_dwordx4 v6, s[28:31], s75 offen offset:1024 lds\n" \
    "v_add_f32 v36, v36, v45\n" \
    "v_cvt_pk_bf16_f32 v54, v44, v45\n" \
    "buffer_load_dwordx4 v7, s[28:31], s75 offen offset:2048 lds\n" \
    "buffer_load_dwordx4 v8, s[28:31], s75 offen offset:3072 lds\n" \
    "v_mfma_f32_32x32x16_bf16 v[112:127], a[92:95], a[28:31], v[112:127]\n" \
    "v_add_f32 v36, v36, v46\n" \
    "v_add_f32 v36, v36, v47\n" \
    "s_mov_b32 m0, 139264\n" \
    "s_mov_b32 s75, 2580480\n" \
    "buffer_load_dwordx4 v5, s[28:31], s75 offen offset:0 lds\n" \
    "v_cvt_pk_bf16_f32 v55, v46, v47\n" \
    "buffer_load_dwordx4 v6, s[28:31], s75 offen offset:1024 lds\n" \
    "buffer_load_dwordx4 v7, s[28:31], s75 offen offset:2048 lds\n" \
    "v_cmp_lt_f32 vcc, s69, v36\n" \
    "s_cbranch_vccnz Lmha_stub_si10b\n" \
    "Lmha_back_si10b:\n" \
    "s_waitcnt lgkmcnt(0)\n" \
    "v_mfma_f32_32x32x16_bf16 v[128:143], a[96:99], v[48:51], v[128:143]\n" \
    "v_add_f32 v38, v38, v36\n" \
    "ds_read_b128 a[64:67], v0 offset:0\n" \
    "ds_read_b128 a[68:71], v29 offset:0\n" \
    "buffer_load_dwordx4 v8, s[28:31], s75 offen offset:3072 lds\n" \
    "s_mov_b32 m0, 143360\n" \
    "s_mov_b32 s75, 3760128\n" \
    "buffer_load_dwordx4 v5, s[28:31], s75 offen offset:0 lds\n" \
    "v_mfma_f32_32x32x16_bf16 v[144:159], a[100:103], v[48:51], v[144:159]\n" \
    "ds_read_b128 a[72:75], v0 offset:256\n" \
    "ds_read_b128 a[76:79], v29 offset:256\n" \
    "buffer_load_dwordx4 v6, s[28:31], s75 offen offset:1024 lds\n" \
    "buffer_load_dwordx4 v7, s[28:31], s75 offen offset:2048 lds\n" \
    "buffer_load_dwordx4 v8, s[28:31], s75 offen offset:3072 lds\n" \
    "v_mfma_f32_32x32x16_bf16 v[160:175], a[104:107], v[48:51], v[160:175]\n" \
    "ds_read_b128 a[80:83], v0 offset:512\n" \
    "ds_read_b128 a[84:87], v29 offset:512\n" \
    "s_mov_b32 m0, s66\n" \
    "s_add_u32 s75, s65, 4096\n" \
    "buffer_load_dwordx4 v25, s[44:47], s65 offen offset:0 nt lds\n" \
    "buffer_load_dwordx4 v26, s[44:47], s65 offen offset:1024 nt lds\n" \
    "v_mfma_f32_32x32x16_bf16 v[176:191], a[108:111], v[48:51], v[176:191]\n" \
    "ds_read_b128 a[88:91], v0 offset:768\n" \
    "ds_read_b128 a[92:95], v29 offset:768\n" \
    "buffer_load_dwordx4 v27, s[44:47], s65 offen offset:2048 nt lds\n" \
    "buffer_load_dwordx4 v28, s[44:47], s65 offen offset:3072 nt lds\n" \
    "s_add_u32 m0, m0, 4096\n" \
    "s_nop 0\n" \
    "buffer_load_dwordx4 v25, s[44:47], s75 offen offset:0 nt lds\n" \
    "v_mfma_f32_32x32x16_bf16 v[128:143], a[112:115], v[52:55], v[128:143]\n" \
    "ds_read_b64_tr_b16 a[208:209], v14 offset:16384\n" \
    "ds_read_b64_tr_b16 a[210:211], v15 offset:16384\n" \
    "buffer_load_dwordx4 v26, s[44:47], s75 offen offset:1024 nt lds\n" \
    "buffer_load_dwordx4 v27, s[44:47], s75 offen offset:2048 nt lds\n" \
    "buffer_load_dwordx4 v28, s[44:47], s75 offen offset:3072 nt lds\n" \
    "s_add_u32 m0, m0, 4096\n" \
    "v_mfma_f32_32x32x16_bf16 v[144:159], a[116:119], v[52:55], v[144:159]\n" \
    "ds_read_b64_tr_b16 a[212:213], v10 offset:16384\n" \
    "ds_read_b64_tr_b16 a[214:215], v11 offset:16384\n" \
    "buffer_load_dwordx4 v25, s[36:39], s65 offen offset:0 nt lds\n" \
    "buffer_load_dwordx4 v26, s[36:39], s65 offen offset:1024 nt lds\n" \
    "buffer_load_dwordx4 v27, s[36:39], s65 offen offset:2048 nt lds\n" \
    "v_mfma_f32_32x32x16_bf16 v[160:175], a[120:123], v[52:55], v[160:175]\n" \
    "ds_read_b64_tr_b16 a[216:217], v14 offset:18432\n" \
    "ds_read_b64_tr_b16 a[218:219], v15 offset:18432\n" \
    "buffer_load_dwordx4 v28, s[36:39], s65 offen offset:3072 nt lds\n" \
    "s_add_u32 m0, m0, 4096\n" \
    "s_nop 0\n" \
    "buffer_load_dwordx4 v25, s[36:39], s75 offen offset:0 nt lds\n" \
    "buffer_load_dwordx4 v26, s[36:39], s75 offen offset:1024 nt lds\n" \
    "v_mfma_f32_32x32x16_bf16 v[176:191], a[124:127], v[52:55], v[176:191]\n" \
    "ds_read_b64_tr_b16 a[220:221], v10 offset:18432\n" \
    "ds_read_b64_tr_b16 a[222:223], v11 offset:18432\n" \
    "buffer_load_dwordx4 v27, s[36:39], s75 offen offset:2048 nt lds\n" \
    "buffer_load_dwordx4 v28, s[36:39], s75 offen offset:3072 nt lds\n" \
    "s_add_u32 m0, m0, 4096\n" \
    "s_add_u32 s65, s65, 8192\n" \
    "s_and_b32 s65, s65, 131071\n" \
    "s_add_u32 s66, s66, s59\n" \
    "s_and_b32 s66, s66, 131071\n" \
    "s_barrier\n" \
    "s_waitcnt lgkmcnt(4)\n" \
    "v_mfma_f32_32x32x16_bf16 v[96:111], a[64:67], a[0:3], v[64:79]\n" \
    "ds_read_b64_tr_b16 a[96:97], v22 offset:49152\n" \
    "v_exp_f32 v40, v112\n" \
    "v_exp_f32 v41, v113\n" \
    "ds_read_b64_tr_b16 a[98:99], v23 offset:49152\n" \
    "v_exp_f32 v42, v114\n" \
    "v_exp_f32 v43, v115\n" \
    "s_mov_b32 m0, 16384\n" \
    "s_mov_b32 s75, 331776\n" \
    "buffer_load_dwordx4 v5, s[28:31], s75 offen offset:0 lds\n" \
    "ds_read_b64_tr_b16 a[100:101], v22 offset:49408\n" \
    "v_add_f32 v37, v40, v41\n" \
    "v_cvt_pk_bf16_f32 v56, v40, v41\n" \
    "buffer_load_dwordx4 v6, s[28:31], s75 offen offset:1024 lds\n" \
    "v_mfma_f32_32x32x16_bf16 v[96:111], a[68:71], a[4:7], v[96:111]\n" \
    "ds_read_b64_tr_b16 a[102:103], v23 offset:49408\n" \
    "v_exp_f32 v44, v116\n" \
    "v_exp_f32 v45, v117\n" \
    "buffer_load_dwordx4 v7, s[28:31], s75 offen offset:2048 lds\n" \
    "ds_read_b64_tr_b16 a[104:105], v22 offset:49664\n" \
    "v_add_f32 v37, v37, v42\n" \
    "v_add_f32 v37, v37, v43\n" \
    "buffer_load_dwordx4 v8, s[28:31], s75 offen offset:3072 lds\n" \
    "v_mfma_f32_32x32x16_bf16 v[96:111], a[72:75], a[8:11], v[96:111]\n" \
    "ds_read_b64_tr_b16 a[106:107], v23 offset:49664\n" \
    "v_cvt_pk_bf16_f32 v57, v42, v43\n" \
    "v_exp_f32 v46, v118\n" \
    "s_mov_b32 m0, 20480\n" \
    "s_mov_b32 s75, 1511424\n" \
    "buffer_load_dwordx4 v5, s[28:31], s75 offen offset:0 lds\n" \
    "ds_read_b64_tr_b16 a[108:109], v22 offset:49920\n" \
    "v_exp_f32 v47, v119\n" \
    "v_add_f32 v37, v37, v44\n" \
    "buffer_load_dwordx4 v6, s[28:31], s75 offen offset:1024 lds\n" \
    "ds_read_b64_tr_b16 a[110:111], v23 offset:49920\n" \
    "v_add_f32 v37, v37, v45\n" \
    "v_cvt_pk_bf16_f32 v58, v44, v45\n" \
    "buffer_load_dwordx4 v7, s[28:31], s75 offen offset:2048 lds\n" \
    "v_mfma_f32_32x32x16_bf16 v[96:111], a[76:79], a[12:15], v[96:111]\n" \
    "ds_read_b64_tr_b16 a[112:113], v22 offset:53248\n" \
    "v_exp_f32 v40, v120\n" \
    "v_exp_f32 v41, v121\n" \
    "buffer_load_dwordx4 v8, s[28:31], s75 offen offset:3072 lds\n" \
    "ds_read_b64_tr_b16 a[114:115], v23 offset:53248\n" \
    "v_add_f32 v37, v37, v46\n" \
    "v_add_f32 v37, v37, v47\n" \
    "s_mov_b32 m0, 24576\n" \
    "s_mov_b32 s75, 2691072\n" \
    "buffer_load_dwordx4 v5, s[28:31], s75 offen offset:0 lds\n" \
    "v_mfma_f32_32x32x16_bf16 v[96:111], a[80:83], a[16:19], v[96:111]\n" \
    "ds_read_b64_tr_b16 a[116:117], v22 offset:53504\n" \
    "v_cvt_pk_bf16_f32 v59, v46, v47\n" \
    "v_exp_f32 v42, v122\n" \
    "buffer_load_dwordx4 v6, s[28:31], s75 offen offset:1024 lds\n" \
    "ds_read_b64_tr_b16 a[118:119], v23 offset:53504\n" \
    "v_exp_f32 v43, v123\n" \
    "v_add_f32 v37, v37, v40\n" \
    "buffer_load_dwordx4 v7, s[28:31], s75 offen offset:2048 lds\n" \
    "ds_read_b64_tr_b16 a[120:121], v22 offset:53760\n" \
    "v_add_f32 v37, v37, v41\n" \
    "v_cvt_pk_bf16_f32 v60, v40, v41\n" \
    "buffer_load_dwordx4 v8, s[28:31], s75 offen offset:3072 lds\n" \
    "v_mfma_f32_32x32x16_bf16 v[96:111], a[84:87], a[20:23], v[96:111]\n" \
    "ds_read_b64_tr_b16 a[122:123], v23 offset:53760\n" \
    "v_exp_f32 v44, v124\n" \
    "v_exp_f32 v45, v125\n" \
    "s_mov_b32 m0, 28672\n" \
    "s_mov_b32 s75, 3870720\n" \
    "buffer_load_dwordx4 v5, s[28:31], s75 offen offset:0 lds\n" \
    "ds_read_b64_tr_b16 a[124:125], v22 offset:54016\n" \
    "v_add_f32 v37, v37, v42\n" \
    "v_add_f32 v37, v37, v43\n" \
    "buffer_load_dwordx4 v6, s[28:31], s75 offen offset:1024 lds\n" \
    "v_mfma_f32_32x32x16_bf16 v[96:111], a[88:91], a[24:27], v[96:111]\n" \
    "ds_read_b64_tr_b16 a[126:127], v23 offset:54016\n" \
    "v_cvt_pk_bf16_f32 v61, v42, v43\n" \
    "v_exp_f32 v46, v126\n" \
    "buffer_load_dwordx4 v7, s[28:31], s75 offen offset:2048 lds\n" \
    "v_exp_f32 v47, v127\n" \
    "v_add_f32 v37, v37, v44\n" \
    "buffer_load_dwordx4 v8, s[28:31], s75 offen offset:3072 lds\n" \
    "v_add_f32 v37, v37, v45\n" \
    "v_cvt_pk_bf16_f32 v62, v44, v45\n" \
    "s_mov_b32 m0, 147456\n" \
    "s_mov_b32 s75, 258048\n" \
    "buffer_load_dwordx4 v5, s[28:31], s75 offen offset:0 lds\n" \
    "v_mfma_f32_32x32x16_bf16 v[96:111], a[92:95], a[28:31], v[96:111]\n" \
    "v_add_f32 v37, v37, v46\n" \
    "v_add_f32 v37, v37, v47\n" \
    "buffer_load_dwordx4 v6, s[28:31], s75 offen offset:1024 lds\n" \
    "v_cvt_pk_bf16_f32 v63, v46, v47\n" \
    "buffer_load_dwordx4 v7, s[28:31], s75 offen offset:2048 lds\n" \
    "v_cmp_lt_f32 vcc, s69, v37\n" \
    "s_cbranch_vccnz Lmha_stub_si11b\n" \
    "Lmha_back_si11b:\n" \
    "s_waitcnt lgkmcnt(0)\n" \
    "v_mfma_f32_32x32x16_bf16 v[192:207], a[96:99], v[56:59], v[192:207]\n" \
    "v_add_f32 v39, v39, v37\n" \
    "s_waitcnt vmcnt(63)\n" \
    "ds_read_b128 a[64:67], v0 offset:16384\n" \
    "buffer_load_dwordx4 v8, s[28:31], s75 offen offset:3072 lds\n" \
    "s_mov_b32 m0, 151552\n" \
    "s_mov_b32 s75, 1437696\n" \
    "buffer_load_dwordx4 v5, s[28:31], s75 offen offset:0 lds\n" \
    "v_mfma_f32_32x32x16_bf16 v[208:223], a[100:103], v[56:59], v[208:223]\n" \
    "ds_read_b128 a[68:71], v29 offset:16384\n" \
    "buffer_load_dwordx4 v6, s[28:31], s75 offen offset:1024 lds\n" \
    "buffer_load_dwordx4 v7, s[28:31], s75 offen offset:2048 lds\n" \
    "v_mfma_f32_32x32x16_bf16 v[224:239], a[104:107], v[56:59], v[224:239]\n" \
    "ds_read_b128 a[72:75], v0 offset:16640\n" \
    "buffer_load_dwordx4 v8, s[28:31], s75 offen offset:3072 lds\n" \
    "s_mov_b32 m0, 155648\n" \
    "s_mov_b32 s75, 2617344\n" \
    "buffer_load_dwordx4 v5, s[28:31], s75 offen offset:0 lds\n" \
    "v_mfma_f32_32x32x16_bf16 v[240:255], a[108:111], v[56:59], v[240:255]\n" \
    "ds_read_b128 a[76:79], v29 offset:16640\n" \
    "buffer_load_dwordx4 v6, s[28:31], s75 offen offset:1024 lds\n" \
    "buffer_load_dwordx4 v7, s[28:31], s75 offen offset:2048 lds\n" \
    "v_mfma_f32_32x32x16_bf16 v[192:207], a[112:115], v[60:63], v[192:207]\n" \
    "ds_read_b128 a[80:83], v0 offset:16896\n" \
    "buffer_load_dwordx4 v8, s[28:31], s75 offen offset:3072 lds\n" \
    "s_mov_b32 m0, 159744\n" \
    "s_mov_b32 s75, 3796992\n" \
    "buffer_load_dwordx4 v5, s[28:31], s75 offen offset:0 lds\n" \
    "v_mfma_f32_32x32x16_bf16 v[208:223], a[116:119], v[60:63], v[208:223]\n" \
    "ds_read_b128 a[84:87], v29 offset:16896\n" \
    "buffer_load_dwordx4 v6, s[28:31], s75 offen offset:1024 lds\n" \
    "buffer_load_dwordx4 v7, s[28:31], s75 offen offset:2048 lds\n" \
    "v_mfma_f32_32x32x16_bf16 v[224:239], a[120:123], v[60:63], v[224:239]\n" \
    "ds_read_b128 a[88:91], v0 offset:17152\n" \
    "buffer_load_dwordx4 v8, s[28:31], s75 offen offset:3072 lds\n" \
    "s_lshr_b32 s55, %1, 6\n" \
    "s_mul_i32 s55, s55, 1179648\n" \
    "s_bfe_u32 s56, %1, 0x30003\n" \
    "s_mul_i32 s56, s56, 69120\n" \
    "s_add_u32 s55, s55, s56\n" \
    "s_add_u32 s55, s55, 626688\n" \
    "s_and_b32 s56, %1, 1\n" \
    "s_lshl_b32 s56, s56, 9\n" \
    "s_add_u32 s55, s55, s56\n" \
    "v_mfma_f32_32x32x16_bf16 v[240:255], a[124:127], v[60:63], v[240:255]\n" \
    "ds_read_b128 a[92:95], v29 offset:17152\n" \
    "v_lshrrev_b32 v9, 7, v12\n" \
    "v_add_u32 v9, 0, v9\n" \
    "v_min_u32 v9, 299, v9\n" \
    "v_mul_u32_u24 v34, 205, v9\n" \
    "v_lshrrev_b32 v34, 10, v34\n" \
    "v_lshlrev_b32 v9, 7, v9\n" \
    "v_lshl_add_u32 v9, v34, 9, v9\n" \
    "v_add_u32 v9, s55, v9\n" \
    "global_load_dword v13, v9, s[16:17]\n" \
    "v_lshrrev_b32 v9, 7, v12\n" \
    "v_add_u32 v9, 64, v9\n" \
    "v_min_u32 v9, 299, v9\n" \
    "v_mul_u32_u24 v34, 205, v9\n" \
    "v_lshrrev_b32 v34, 10, v34\n" \
    "v_lshlrev_b32 v9, 7, v9\n" \
    "v_lshl_add_u32 v9, v34, 9, v9\n" \
    "v_add_u32 v9, s55, v9\n" \
    "global_load_dword v13, v9, s[16:17]\n" \
    "s_waitcnt vmcnt(50)\n" \
    "s_barrier\n" \
    "s_waitcnt lgkmcnt(0)\n" \
    "v_mfma_f32_32x32x16_bf16 v[112:127], a[64:67], a[0:3], v[80:95]\n" \
    "ds_read_b64_tr_b16 a[96:97], v30 offset:0\n" \
    "v_exp_f32 v40, v96\n" \
    "v_exp_f32 v41, v97\n" \
    "ds_read_b64_tr_b16 a[98:99], v31 offset:0\n" \
    "v_exp_f32 v42, v98\n" \
    "v_exp_f32 v43, v99\n" \
    "s_mov_b32 m0, 49152\n" \
    "s_mov_b32 s75, 405504\n" \
    "buffer_load_dwordx4 v5, s[28:31], s75 offen offset:0 lds\n" \
    "ds_read_b64_tr_b16 a[100:101], v30 offset:256\n" \
    "v_add_f32 v36, v40, v41\n" \
    "v_cvt_pk_bf16_f32 v48, v40, v41\n" \
    "buffer_load_dwordx4 v6, s[28:31], s75 offen offset:1024 lds\n" \
    "v_mfma_f32_32x32x16_bf16 v[112:127], a[68:71], a[4:7], v[112:127]\n" \
    "ds_read_b64_tr_b16 a[102:103], v31 offset:256\n" \
    "v_exp_f32 v44, v100\n" \
    "v_exp_f32 v45, v101\n" \
    "buffer_load_dwordx4 v7, s[28:31], s75 offen offset:2048 lds\n" \
    "ds_read_b64_tr_b16 a[104:105], v30 offset:512\n" \
    "v_add_f32 v36, v36, v42\n" \
    "v_add_f32 v36, v36, v43\n" \
    "buffer_load_dwordx4 v8, s[28:31], s75 offen offset:3072 lds\n" \
    "v_mfma_f32_32x32x16_bf16 v[112:127], a[72:75], a[8:11], v[112:127]\n" \
    "ds_read_b64_tr_b16 a[106:107], v31 offset:512\n" \
    "v_cvt_pk_bf16_f32 v49, v42, v43\n" \
    "v_exp_f32 v46, v102\n" \
    "s_mov_b32 m0, 53248\n" \
    "s_mov_b32 s75, 1585152\n" \
    "buffer_load_dwordx4 v5, s[28:31], s75 offen offset:0 lds\n" \
    "ds_read_b64_tr_b16 a[108:109], v30 offset:768\n" \
    "v_exp_f32 v47, v103\n" \
    "v_add_f32 v36, v36, v44\n" \
    "buffer_load_dwordx4 v6, s[28:31], s75 offen offset:1024 lds\n" \
    "ds_read_b64_tr_b16 a[110:111], v31 offset:768\n" \
    "v_add_f32 v36, v36, v45\n" \
    "v_cvt_pk_bf16_f32 v50, v44, v45\n" \
    "buffer_load_dwordx4 v7, s[28:31], s75 offen offset:2048 lds\n" \
    "v_mfma_f32_32x32x16_bf16 v[112:127], a[76:79], a[12:15], v[112:127]\n" \
    "ds_read_b64_tr_b16 a[112:113], v30 offset:4096\n" \
    "v_exp_f32 v40, v104\n" \
    "v_exp_f32 v41, v105\n" \
    "buffer_load_dwordx4 v8, s[28:31], s75 offen offset:3072 lds\n" \
    "ds_read_b64_tr_b16 a[114:115], v31 offset:4096\n" \
    "v_add_f32 v36, v36, v46\n" \
    "v_add_f32 v36, v36, v47\n" \
    "s_mov_b32 m0, 57344\n" \
    "s_mov_b32 s75, 2764800\n" \
    "buffer_load_dwordx4 v5, s[28:31], s75 offen offset:0 lds\n" \
    "v_mfma_f32_32x32x16_bf16 v[112:127], a[80:83], a[16:19], v[112:127]\n" \
    "ds_read_b64_tr_b16 a[116:117], v30 offset:4352\n" \
    "v_cvt_pk_bf16_f32 v51, v46, v47\n" \
    "v_exp_f32 v42, v106\n" \
    "buffer_load_dwordx4 v6, s[28:31], s75 offen offset:1024 lds\n" \
    "ds_read_b64_tr_b16 a[118:119], v31 offset:4352\n" \
    "v_exp_f32 v43, v107\n" \
    "v_add_f32 v36, v36, v40\n" \
    "buffer_load_dwordx4 v7, s[28:31], s75 offen offset:2048 lds\n" \
    "ds_read_b64_tr_b16 a[120:121], v30 offset:4608\n" \
    "v_add_f32 v36, v36, v41\n" \
    "v_cvt_pk_bf16_f32 v52, v40, v41\n" \
    "buffer_load_dwordx4 v8, s[28:31], s75 offen offset:3072 lds\n" \
    "v_mfma_f32_32x32x16_bf16 v[112:127], a[84:87], a[20:23], v[112:127]\n" \
    "ds_read_b64_tr_b16 a[122:123], v31 offset:4608\n" \
    "v_exp_f32 v44, v108\n" \
    "v_exp_f32 v45, v109\n" \
    "s_mov_b32 m0, 61440\n" \
    "s_mov_b32 s75, 3944448\n" \
    "buffer_load_dwordx4 v5, s[28:31], s75 offen offset:0 lds\n" \
    "ds_read_b64_tr_b16 a[124:125], v30 offset:4864\n" \
    "v_add_f32 v36, v36, v42\n" \
    "v_add_f32 v36, v36, v43\n" \
    "buffer_load_dwordx4 v6, s[28:31], s75 offen offset:1024 lds\n" \
    "v_mfma_f32_32x32x16_bf16 v[112:127], a[88:91], a[24:27], v[112:127]\n" \
    "ds_read_b64_tr_b16 a[126:127], v31 offset:4864\n" \
    "v_cvt_pk_bf16_f32 v53, v42, v43\n" \
    "v_exp_f32 v46, v110\n" \
    "buffer_load_dwordx4 v7, s[28:31], s75 offen offset:2048 lds\n" \
    "v_exp_f32 v47, v111\n" \
    "v_add_f32 v36, v36, v44\n" \
    "buffer_load_dwordx4 v8, s[28:31], s75 offen offset:3072 lds\n" \
    "v_add_f32 v36, v36, v45\n" \
    "v_cvt_pk_bf16_f32 v54, v44, v45\n" \
    "s_mov_b32 m0, 32768\n" \
    "s_mov_b32 s75, 368640\n" \
    "buffer_load_dwordx4 v5, s[28:31], s75 offen offset:0 lds\n" \
    "v_mfma_f32_32x32x16_bf16 v[112:127], a[92:95], a[28:31], v[112:127]\n" \
    "v_add_f32 v36, v36, v46\n" \
    "v_add_f32 v36, v36, v47\n" \
    "buffer_load_dwordx4 v6, s[28:31], s75 offen offset:1024 lds\n" \
    "v_cvt_pk_bf16_f32 v55, v46, v47\n" \
    "buffer_load_dwordx4 v7, s[28:31], s75 offen offset:2048 lds\n" \
    "v_cmp_lt_f32 vcc, s69, v36\n" \
    "s_cbranch_vccnz Lmha_stub_si12b\n" \
    "Lmha_back_si12b:\n" \
    "s_waitcnt lgkmcnt(0)\n" \
    "v_mfma_f32_32x32x16_bf16 v[128:143], a[96:99], v[48:51], v[128:143]\n" \
    "v_add_f32 v38, v38, v36\n" \
    "ds_read_b128 a[64:67], v0 offset:32768\n" \
    "ds_read_b128 a[68:71], v29 offset:32768\n" \
    "buffer_load_dwordx4 v8, s[28:31], s75 offen offset:3072 lds\n" \
    "s_mov_b32 m0, 36864\n" \
    "s_mov_b32 s75, 1548288\n" \
    "buffer_load_dwordx4 v5, s[28:31], s75 offen offset:0 lds\n" \
    "v_mfma_f32_32x32x16_bf16 v[144:159], a[100:103], v[48:51], v[144:159]\n" \
    "ds_read_b128 a[72:75], v0 offset:33024\n" \
    "ds_read_b128 a[76:79], v29 offset:33024\n" \
    "buffer_load_dwordx4 v6, s[28:31], s75 offen offset:1024 lds\n" \
    "buffer_load_dwordx4 v7, s[28:31], s75 offen offset:2048 lds\n" \
    "v_mfma_f32_32x32x16_bf16 v[160:175], a[104:107], v[48:51], v[160:175]\n" \
    "ds_read_b128 a[80:83], v0 offset:33280\n" \
    "ds_read_b128 a[84:87], v29 offset:33280\n" \
    "buffer_load_dwordx4 v8, s[28:31], s75 offen offset:3072 lds\n" \
    "s_mov_b32 m0, 40960\n" \
    "s_mov_b32 s75, 2727936\n" \
    "buffer_load_dwordx4 v5, s[28:31], s75 offen offset:0 lds\n" \
    "v_mfma_f32_32x32x16_bf16 v[176:191], a[108:111], v[48:51], v[176:191]\n" \
    "ds_read_b128 a[88:91], v0 offset:33536\n" \
    "ds_read_b128 a[92:95], v29 offset:33536\n" \
    "buffer_load_dwordx4 v6, s[28:31], s75 offen offset:1024 lds\n" \
    "buffer_load_dwordx4 v7, s[28:31], s75 offen offset:2048 lds\n" \
    "v_mfma_f32_32x32x16_bf16 v[128:143], a[112:115], v[52:55], v[128:143]\n" \
    "ds_read_b64_tr_b16 a[224:225], v14 offset:0\n" \
    "ds_read_b64_tr_b16 a[226:227], v15 offset:0\n" \
    "buffer_load_dwordx4 v8, s[28:31], s75 offen offset:3072 lds\n" \
    "s_mov_b32 m0, 45056\n" \
    "s_mov_b32 s75, 3907584\n" \
    "buffer_load_dwordx4 v5, s[28:31], s75 offen offset:0 lds\n" \
    "v_mfma_f32_32x32x16_bf16 v[144:159], a[116:119], v[52:55], v[144:159]\n" \
    "ds_read_b64_tr_b16 a[228:229], v10 offset:0\n" \
    "ds_read_b64_tr_b16 a[230:231], v11 offset:0\n" \
    "buffer_load_dwordx4 v6, s[28:31], s75 offen offset:1024 lds\n" \
    "buffer_load_dwordx4 v7, s[28:31], s75 offen offset:2048 lds\n" \
    "v_mfma_f32_32x32x16_bf16 v[160:175], a[120:123], v[52:55], v[160:175]\n" \
    "ds_read_b64_tr_b16 a[232:233], v14 offset:2048\n" \
    "ds_read_b64_tr_b16 a[234:235], v15 offset:2048\n" \
    "buffer_load_dwordx4 v8, s[28:31], s75 offen offset:3072 lds\n" \
    "s_lshr_b32 s55, %1, 6\n" \
    "s_mul_i32 s55, s55, 1179648\n" \
    "s_bfe_u32 s56, %1, 0x30003\n" \
    "s_mul_i32 s56, s56, 69120\n" \
    "s_add_u32 s55, s55, s56\n" \
    "s_add_u32 s55, s55, 626688\n" \
    "s_and_b32 s56, %1, 1\n" \
    "s_lshl_b32 s56, s56, 9\n" \
    "s_add_u32 s55, s55, s56\n" \
    "v_mfma_f32_32x32x16_bf16 v[176:191], a[124:127], v[52:55], v[176:191]\n" \
    "ds_read_b64_tr_b16 a[236:237], v10 offset:2048\n" \
    "ds_read_b64_tr_b16 a[238:239], v11 offset:2048\n" \
    "v_lshrrev_b32 v9, 7, v12\n" \
    "v_add_u32 v9, 128, v9\n" \
    "v_min_u32 v9, 299, v9\n" \
    "v_mul_u32_u24 v34, 205, v9\n" \
    "v_lshrrev_b32 v34, 10, v34\n" \
    "v_lshlrev_b32 v9, 7, v9\n" \
    "v_lshl_add_u32 v9, v34, 9, v9\n" \
    "v_add_u32 v9, s55, v9\n" \
    "global_load_dword v13, v9, s[16:17]\n" \
    "v_lshrrev_b32 v9, 7, v12\n" \
    "v_add_u32 v9, 192, v9\n" \
    "v_min_u32 v9, 299, v9\n" \
    "v_mul_u32_u24 v34, 205, v9\n" \
    "v_lshrrev_b32 v34, 10, v34\n" \
    "v_lshlrev_b32 v9, 7, v9\n" \
    "v_lshl_add_u32 v9, v34, 9, v9\n" \
    "v_add_u32 v9, s55, v9\n" \
    "global_load_dword v13, v9, s[16:17]\n" \
    "s_waitcnt vmcnt(36)\n" \
    "s_barrier\n" \
    "s_waitcnt lgkmcnt(0)\n" \
    "v_mfma_f32_32x32x16_bf16 v[96:111], a[64:67], a[0:3], v[64:79]\n" \
    "ds_read_b64_tr_b16 a[96:97], v30 offset:16384\n" \
    "v_exp_f32 v40, v112\n" \
    "v_exp_f32 v41, v113\n" \
    "ds_read_b64_tr_b16 a[98:99], v31 offset:16384\n" \
    "v_exp_f32 v42, v114\n" \
    "v_exp_f32 v43, v115\n" \
    "s_mov_b32 m0, 131072\n" \
    "s_mov_b32 s75, 479232\n" \
    "buffer_load_dwordx4 v5, s[28:31], s75 offen offset:0 lds\n" \
    "ds_read_b64_tr_b16 a[100:101], v30 offset:16640\n" \
    "v_add_f32 v37, v40, v41\n" \
    "v_cvt_pk_bf16_f32 v56, v40, v41\n" \
    "buffer_load_dwordx4 v6, s[28:31], s75 offen offset:1024 lds\n" \
    "v_mfma_f32_32x32x16_bf16 v[96:111], a[68:71], a[4:7], v[96:111]\n" \
    "ds_read_b64_tr_b16 a[102:103], v31 offset:16640\n" \
    "v_exp_f32 v44, v116\n" \
    "v_exp_f32 v45, v117\n" \
    "buffer_load_dwordx4 v7, s[28:31], s75 offen offset:2048 lds\n" \
    "ds_read_b64_tr_b16 a[104:105], v30 offset:16896\n" \
    "v_add_f32 v37, v37, v42\n" \
    "v_add_f32 v37, v37, v43\n" \
    "buffer_load_dwordx4 v8, s[28:31], s75 offen offset:3072 lds\n" \
    "v_mfma_f32_32x32x16_bf16 v[96:111], a[72:75], a[8:11], v[96:111]\n" \
    "ds_read_b64_tr_b16 a[106:107], v31 offset:16896\n" \
    "v_cvt_pk_bf16_f32 v57, v42, v43\n" \
    "v_exp_f32 v46, v118\n" \
    "s_mov_b32 m0, 135168\n" \
    "s_mov_b32 s75, 1658880\n" \
    "buffer_load_dwordx4 v5, s[28:31], s75 offen offset:0 lds\n" \
    "ds_read_b64_tr_b16 a[108:109], v30 offset:17152\n" \
    "v_exp_f32 v47, v119\n" \
    "v_add_f32 v37, v37, v44\n" \
    "buffer_load_dwordx4 v6, s[28:31], s75 offen offset:1024 lds\n" \
    "ds_read_b64_tr_b16 a[110:111], v31 offset:17152\n" \
    "v_add_f32 v37, v37, v45\n" \
    "v_cvt_pk_bf16_f32 v58, v44, v45\n" \
    "buffer_load_dwordx4 v7, s[28:31], s75 offen offset:2048 lds\n" \
    "v_mfma_f32_32x32x16_bf16 v[96:111], a[76:79], a[12:15], v[96:111]\n" \
    "ds_read_b64_tr_b16 a[112:113], v30 offset:20480\n" \
    "v_exp_f32 v40, v120\n" \
    "v_exp_f32 v41, v121\n" \
    "buffer_load_dwordx4 v8, s[28:31], s75 offen offset:3072 lds\n" \
    "ds_read_b64_tr_b16 a[114:115], v31 offset:20480\n" \
    "v_add_f32 v37, v37, v46\n" \
    "v_add_f32 v37, v37, v47\n" \
    "s_mov_b32 m0, 139264\n" \
    "s_mov_b32 s75, 2838528\n" \
    "buffer_load_dwordx4 v5, s[28:31], s75 offen offset:0 lds\n" \
    "v_mfma_f32_32x32x16_bf16 v[96:111], a[80:83], a[16:19], v[96:111]\n" \
    "ds_read_b64_tr_b16 a[116:117], v30 offset:20736\n" \
    "v_cvt_pk_bf16_f32 v59, v46, v47\n" \
    "v_exp_f32 v42, v122\n" \
    "ds_read_b64_tr_b16 a[118:119], v31 offset:20736\n" \
    "v_exp_f32 v43, v123\n" \
    "v_add_f32 v37, v37, v40\n" \
    "buffer_load_dwordx4 v6, s[28:31], s75 offen offset:1024 lds\n" \
    "ds_read_b64_tr_b16 a[120:121], v30 offset:20992\n" \
    "v_add_f32 v37, v37, v41\n" \
    "v_cvt_pk_bf16_f32 v60, v40, v41\n" \
    "buffer_load_dwordx4 v7, s[28:31], s75 offen offset:2048 lds\n" \
    "v_mfma_f32_32x32x16_bf16 v[96:111], a[84:87], a[20:23], v[96:111]\n" \
    "ds_read_b64_tr_b16 a[122:123], v31 offset:20992\n" \
    "v_exp_f32 v44, v124\n" \
    "v_exp_f32 v45, v125\n" \
    "buffer_load_dwordx4 v8, s[28:31], s75 offen offset:3072 lds\n" \
    "ds_read_b64_tr_b16 a[124:125], v30 offset:21248\n" \
    "v_add_f32 v37, v37, v42\n" \
    "v_add_f32 v37, v37, v43\n" \
    "s_mov_b32 m0, 143360\n" \
    "s_mov_b32 s75, 4018176\n" \
    "buffer_load_dwordx4 v5, s[28:31], s75 offen offset:0 lds\n" \
    "v_mfma_f32_32x32x16_bf16 v[96:111], a[88:91], a[24:27], v[96:111]\n" \
    "ds_read_b64_tr_b16 a[126:127], v31 offset:21248\n" \
    "v_cvt_pk_bf16_f32 v61, v42, v43\n" \
    "v_exp_f32 v46, v126\n" \
    "buffer_load_dwordx4 v6, s[28:31], s75 offen offset:1024 lds\n" \
    "v_exp_f32 v47, v127\n" \
    "v_add_f32 v37, v37, v44\n" \
    "buffer_load_dwordx4 v7, s[28:31], s75 offen offset:2048 lds\n" \
    "v_add_f32 v37, v37, v45\n" \
    "v_cvt_pk_bf16_f32 v62, v44, v45\n" \
    "buffer_load_dwordx4 v8, s[28:31], s75 offen offset:3072 lds\n" \
    "v_mfma_f32_32x32x16_bf16 v[96:111], a[92:95], a[28:31], v[96:111]\n" \
    "v_add_f32 v37, v37, v46\n" \
    "v_add_f32 v37, v37, v47\n" \
    "s_mov_b32 m0, 65536\n" \
    "s_mov_b32 s75, 442368\n" \
    "buffer_load_dwordx4 v5, s[28:31], s75 offen offset:0 lds\n" \
    "v_cvt_pk_bf16_f32 v63, v46, v47\n" \
    "buffer_load_dwordx4 v6, s[28:31], s75 offen offset:1024 lds\n" \
    "v_cmp_lt_f32 vcc, s69, v37\n" \
    "s_cbranch_vccnz Lmha_stub_si13b\n" \
    "Lmha_back_si13b:\n" \
    "s_waitcnt lgkmcnt(0)\n" \
    "v_mfma_f32_32x32x16_bf16 v[192:207], a[96:99], v[56:59], v[192:207]\n" \
    "v_add_f32 v39, v39, v37\n" \
    "ds_read_b128 a[64:67], v0 offset:49152\n" \
    "ds_read_b128 a[68:71], v29 offset:49152\n" \
    "buffer_load_dwordx4 v7, s[28:31], s75 offen offset:2048 lds\n" \
    "buffer_load_dwordx4 v8, s[28:31], s75 offen offset:3072 lds\n" \
    "v_mfma_f32_32x32x16_bf16 v[208:223], a[100:103], v[56:59], v[208:223]\n" \
    "ds_read_b128 a[72:75], v0 offset:49408\n" \
    "ds_read_b128 a[76:79], v29 offset:49408\n" \
    "s_mov_b32 m0, 69632\n" \
    "s_mov_b32 s75, 1622016\n" \
    "buffer_load_dwordx4 v5, s[28:31], s75 offen offset:0 lds\n" \
    "buffer_load_dwordx4 v6, s[28:31], s75 offen offset:1024 lds\n" \
    "v_mfma_f32_32x32x16_bf16 v[224:239], a[104:107], v[56:59], v[224:239]\n" \
    "ds_read_b128 a[80:83], v0 offset:49664\n" \
    "ds_read_b128 a[84:87], v29 offset:49664\n" \
    "buffer_load_dwordx4 v7, s[28:31], s75 offen offset:2048 lds\n" \
    "buffer_load_dwordx4 v8, s[28:31], s75 offen offset:3072 lds\n" \
    "v_mfma_f32_32x32x16_bf16 v[240:255], a[108:111], v[56:59], v[240:255]\n" \
    "ds_read_b128 a[88:91], v0 offset:49920\n" \
    "ds_read_b128 a[92:95], v29 offset:49920\n" \
    "s_mov_b32 m0, 73728\n" \
    "s_mov_b32 s75, 2801664\n" \
    "buffer_load_dwordx4 v5, s[28:31], s75 offen offset:0 lds\n" \
    "buffer_load_dwordx4 v6, s[28:31], s75 offen offset:1024 lds\n" \
    "v_mfma_f32_32x32x16_bf16 v[192:207], a[112:115], v[60:63], v[192:207]\n" \
    "ds_read_b64_tr_b16 a[240:241], v14 offset:16384\n" \
    "ds_read_b64_tr_b16 a[242:243], v15 offset:16384\n" \
    "buffer_load_dwordx4 v7, s[28:31], s75 offen offset:2048 lds\n" \
    "buffer_load_dwordx4 v8, s[28:31], s75 offen offset:3072 lds\n" \
    "v_mfma_f32_32x32x16_bf16 v[208:223], a[116:119], v[60:63], v[208:223]\n" \
    "ds_read_b64_tr_b16 a[244:245], v10 offset:16384\n" \
    "ds_read_b64_tr_b16 a[246:247], v11 offset:16384\n" \
    "s_mov_b32 m0, 77824\n" \
    "s_mov_b32 s75, 3981312\n" \
    "buffer_load_dwordx4 v5, s[28:31], s75 offen offset:0 lds\n" \
    "buffer_load_dwordx4 v6, s[28:31], s75 offen offset:1024 lds\n" \
    "v_mfma_f32_32x32x16_bf16 v[224:239], a[120:123], v[60:63], v[224:239]\n" \
    "ds_read_b64_tr_b16 a[248:249], v14 offset:18432\n" \
    "ds_read_b64_tr_b16 a[250:251], v15 offset:18432\n" \
    "buffer_load_dwordx4 v7, s[28:31], s75 offen offset:2048 lds\n" \
    "buffer_load_dwordx4 v8, s[28:31], s75 offen offset:3072 lds\n" \
    "v_mfma_f32_32x32x16_bf16 v[240:255], a[124:127], v[60:63], v[240:255]\n" \
    "ds_read_b64_tr_b16 a[252:253], v10 offset:18432\n" \
    "ds_read_b64_tr_b16 a[254:255], v11 offset:18432\n" \
    "s_lshr_b32 s55, %1, 6\n" \
    "s_mul_i32 s55, s55, 1179648\n" \
    "s_bfe_u32 s56, %1, 0x30003\n" \
    "s_mul_i32 s56, s56, 69120\n" \
    "s_add_u32 s55, s55, s56\n" \
    "s_add_u32 s55, s55, 626688\n" \
    "s_and_b32 s56, %1, 1\n" \
    "s_lshl_b32 s56, s56, 9\n" \
    "s_add_u32 s55, s55, s56\n" \
    "v_lshrrev_b32 v9, 7, v12\n" \
    "v_add_u32 v9, 256, v9\n" \
    "v_min_u32 v9, 299, v9\n" \
    "v_mul_u32_u24 v34, 205, v9\n" \
    "v_lshrrev_b32 v34, 10, v34\n" \
    "v_lshlrev_b32 v9, 7, v9\n" \
    "v_lshl_add_u32 v9, v34, 9, v9\n" \
    "v_add_u32 v9, s55, v9\n" \
    "global_load_dword v13, v9, s[16:17]\n" \
    "s_waitcnt vmcnt(35)\n" \
    "s_barrier\n" \
    "s_waitcnt lgkmcnt(0)\n" \
    "v_mfma_f32_32x32x16_bf16 v[112:127], a[64:67], a[0:3], v[80:95]\n" \
    "ds_read_b64_tr_b16 a[96:97], v30 offset:32768\n" \
    "v_exp_f32 v40, v96\n" \
    "v_exp_f32 v41, v97\n" \
    "ds_read_b64_tr_b16 a[98:99], v31 offset:32768\n" \
    "v_exp_f32 v42, v98\n" \
    "v_exp_f32 v43, v99\n" \
    "s_mov_b32 m0, 81920\n" \
    "s_mov_b32 s75, 552960\n" \
    "buffer_load_dwordx4 v5, s[28:31], s75 offen offset:0 lds\n" \
    "ds_read_b64_tr_b16 a[100:101], v30 offset:33024\n" \
    "v_add_f32 v36, v40, v41\n" \
    "v_cvt_pk_bf16_f32 v48, v40, v41\n" \
    "buffer_load_dwordx4 v6, s[28:31], s75 offen offset:1024 lds\n" \
    "v_mfma_f32_32x32x16_bf16 v[112:127], a[68:71], a[4:7], v[112:127]\n" \
    "ds_read_b64_tr_b16 a[102:103], v31 offset:33024\n" \
    "v_exp_f32 v44, v100\n" \
    "v_exp_f32 v45, v101\n" \
    "buffer_load_dwordx4 v7, s[28:31], s75 offen offset:2048 lds\n" \
    "ds_read_b64_tr_b16 a[104:105], v30 offset:33280\n" \
    "v_add_f32 v36, v36, v42\n" \
    "v_add_f32 v36, v36, v43\n" \
    "buffer_load_dwordx4 v8, s[28:31], s75 offen offset:3072 lds\n" \
    "v_mfma_f32_32x32x16_bf16 v[112:127], a[72:75], a[8:11], v[112:127]\n" \
    "ds_read_b64_tr_b16 a[106:107], v31 offset:33280\n" \
    "v_cvt_pk_bf16_f32 v49, v42, v43\n" \
    "v_exp_f32 v46, v102\n" \
    "s_mov_b32 m0, 86016\n" \
    "s_mov_b32 s75, 1732608\n" \
    "buffer_load_dwordx4 v5, s[28:31], s75 offen offset:0 lds\n" \
    "ds_read_b64_tr_b16 a[108:109], v30 offset:33536\n" \
    "v_exp_f32 v47, v103\n" \
    "v_add_f32 v36, v36, v44\n" \
    "ds_read_b64_tr_b16 a[110:111], v31 offset:33536\n" \
    "v_add_f32 v36, v36, v45\n" \
    "v_cvt_pk_bf16_f32 v50, v44, v45\n" \
    "buffer_load_dwordx4 v6, s[28:31], s75 offen offset:1024 lds\n" \
    "v_mfma_f32_32x32x16_bf16 v[112:127], a[76:79], a[12:15], v[112:127]\n" \
    "ds_read_b64_tr_b16 a[112:113], v30 offset:36864\n" \
    "v_exp_f32 v40, v104\n" \
    "v_exp_f32 v41, v105\n" \
    "buffer_load_dwordx4 v7, s[28:31], s75 offen offset:2048 lds\n" \
    "ds_read_b64_tr_b16 a[114:115], v31 offset:36864\n" \
    "v_add_f32 v36, v36, v46\n" \
    "v_add_f32 v36, v36, v47\n" \
    "buffer_load_dwordx4 v8, s[28:31], s75 offen offset:3072 lds\n" \
    "v_mfma_f32_32x32x16_bf16 v[112:127], a[80:83], a[16:19], v[112:127]\n" \
    "ds_read_b64_tr_b16 a[116:117], v30 offset:37120\n" \
    "v_cvt_pk_bf16_f32 v51, v46, v47\n" \
    "v_exp_f32 v42, v106\n" \
    "s_mov_b32 m0, 90112\n" \
    "s_mov_b32 s75, 2912256\n" \
    "buffer_load_dwordx4 v5, s[28:31], s75 offen offset:0 lds\n" \
    "ds_read_b64_tr_b16 a[118:119], v31 offset:37120\n" \
    "v_exp_f32 v43, v107\n" \
    "v_add_f32 v36, v36, v40\n" \
    "buffer_load_dwordx4 v6, s[28:31], s75 offen offset:1024 lds\n" \
    "ds_read_b64_tr_b16 a[120:121], v30 offset:37376\n" \
    "v_add_f32 v36, v36, v41\n" \
    "v_cvt_pk_bf16_f32 v52, v40, v41\n" \
    "buffer_load_dwordx4 v7, s[28:31], s75 offen offset:2048 lds\n" \
    "v_mfma_f32_32x32x16_bf16 v[112:127], a[84:87], a[20:23], v[112:127]\n" \
    "ds_read_b64_tr_b16 a[122:123], v31 offset:37376\n" \
    "v_exp_f32 v44, v108\n" \
    "v_exp_f32 v45, v109\n" \
    "ds_read_b64_tr_b16 a[124:125], v30 offset:37632\n" \
    "v_add_f32 v36, v36, v42\n" \
    "v_add_f32 v36, v36, v43\n" \
    "buffer_load_dwordx4 v8, s[28:31], s75 offen offset:3072 lds\n" \
    "v_mfma_f32_32x32x16_bf16 v[112:127], a[88:91], a[24:27], v[112:127]\n" \
    "ds_read_b64_tr_b16 a[126:127], v31 offset:37632\n" \
    "v_cvt_pk_bf16_f32 v53, v42, v43\n" \
    "v_exp_f32 v46, v110\n" \
    "s_mov_b32 m0, 94208\n" \
    "s_mov_b32 s75, 4091904\n" \
    "buffer_load_dwordx4 v5, s[28:31], s75 offen offset:0 lds\n" \
    "v_exp_f32 v47, v111\n" \
    "v_add_f32 v36, v36, v44\n" \
    "buffer_load_dwordx4 v6, s[28:31], s75 offen offset:1024 lds\n" \
    "v_add_f32 v36, v36, v45\n" \
    "v_cvt_pk_bf16_f32 v54, v44, v45\n" \
    "buffer_load_dwordx4 v7, s[28:31], s75 offen offset:2048 lds\n" \
    "v_mfma_f32_32x32x16_bf16 v[112:127], a[92:95], a[28:31], v[112:127]\n" \
    "v_add_f32 v36, v36, v46\n" \
    "v_add_f32 v36, v36, v47\n" \
    "buffer_load_dwordx4 v8, s[28:31], s75 offen offset:3072 lds\n" \
    "v_cvt_pk_bf16_f32 v55, v46, v47\n" \
    "s_mov_b32 m0, 147456\n" \
    "s_mov_b32 s75, 516096\n" \
    "buffer_load_dwordx4 v5, s[28:31], s75 offen offset:0 lds\n" \
    "v_cmp_lt_f32 vcc, s69, v36\n" \
    "s_cbranch_vccnz Lmha_stub_si14b\n" \
    "Lmha_back_si14b:\n" \
    "s_waitcnt lgkmcnt(0)\n" \
    "v_mfma_f32_32x32x16_bf16 v[128:143], a[96:99], v[48:51], v[128:143]\n" \
    "v_add_f32 v38, v38, v36\n" \
    "buffer_load_dwordx4 v6, s[28:31], s75 offen offset:1024 lds\n" \
    "v_mfma_f32_32x32x16_bf16 v[144:159], a[100:103], v[48:51], v[144:159]\n" \
    "buffer_load_dwordx4 v7, s[28:31], s75 offen offset:2048 lds\n" \
    "buffer_load_dwordx4 v8, s[28:31], s75 offen offset:3072 lds\n" \
    "v_mfma_f32_32x32x16_bf16 v[160:175], a[104:107], v[48:51], v[160:175]\n" \
    "s_mov_b32 m0, 151552\n" \
    "s_mov_b32 s75, 1695744\n" \
    "buffer_load_dwordx4 v5, s[28:31], s75 offen offset:0 lds\n" \
    "buffer_load_dwordx4 v6, s[28:31], s75 offen offset:1024 lds\n" \
    "v_mfma_f32_32x32x16_bf16 v[176:191], a[108:111], v[48:51], v[176:191]\n" \
    "buffer_load_dwordx4 v7, s[28:31], s75 offen offset:2048 lds\n" \
    "buffer_load_dwordx4 v8, s[28:31], s75 offen offset:3072 lds\n" \
    "v_mfma_f32_32x32x16_bf16 v[128:143], a[112:115], v[52:55], v[128:143]\n" \
    "s_mov_b32 m0, 155648\n" \
    "s_mov_b32 s75, 2875392\n" \
    "buffer_load_dwordx4 v5, s[28:31], s75 offen offset:0 lds\n" \
    "buffer_load_dwordx4 v6, s[28:31], s75 offen offset:1024 lds\n" \
    "v_mfma_f32_32x32x16_bf16 v[144:159], a[116:119], v[52:55], v[144:159]\n" \
    "buffer_load_dwordx4 v7, s[28:31], s75 offen offset:2048 lds\n" \
    "buffer_load_dwordx4 v8, s[28:31], s75 offen offset:3072 lds\n" \
    "v_mfma_f32_32x32x16_bf16 v[160:175], a[120:123], v[52:55], v[160:175]\n" \
    "s_mov_b32 m0, 159744\n" \
    "s_mov_b32 s75, 4055040\n" \
    "buffer_load_dwordx4 v5, s[28:31], s75 offen offset:0 lds\n" \
    "buffer_load_dwordx4 v6, s[28:31], s75 offen offset:1024 lds\n" \
    "v_mfma_f32_32x32x16_bf16 v[176:191], a[124:127], v[52:55], v[176:191]\n" \
    "buffer_load_dwordx4 v7, s[28:31], s75 offen offset:2048 lds\n" \
    "buffer_load_dwordx4 v8, s[28:31], s75 offen offset:3072 lds\n" \
    "s_waitcnt vmcnt(33)\n" \
    "s_barrier\n" \
    "ds_read_b64_tr_b16 a[96:97], v30 offset:49152\n" \
    "v_exp_f32 v40, v112\n" \
    "v_exp_f32 v41, v113\n" \
    "ds_read_b64_tr_b16 a[98:99], v31 offset:49152\n" \
    "v_exp_f32 v42, v114\n" \
    "v_exp_f32 v43, v115\n" \
    "ds_read_b64_tr_b16 a[100:101], v30 offset:49408\n" \
    "v_add_f32 v37, v40, v41\n" \
    "v_cvt_pk_bf16_f32 v56, v40, v41\n" \
    "s_mov_b32 m0, 98304\n" \
    "s_mov_b32 s75, 589824\n" \
    "buffer_load_dwordx4 v5, s[28:31], s75 offen offset:0 lds\n" \
    "ds_read_b64_tr_b16 a[102:103], v31 offset:49408\n" \
    "v_exp_f32 v44, v116\n" \
    "v_exp_f32 v45, v117\n" \
    "ds_read_b64_tr_b16 a[104:105], v30 offset:49664\n" \
    "v_add_f32 v37, v37, v42\n" \
    "v_add_f32 v37, v37, v43\n" \
    "buffer_load_dwordx4 v6, s[28:31], s75 offen offset:1024 lds\n" \
    "ds_read_b64_tr_b16 a[106:107], v31 offset:49664\n" \
    "v_cvt_pk_bf16_f32 v57, v42, v43\n" \
    "v_exp_f32 v46, v118\n" \
    "ds_read_b64_tr_b16 a[108:109], v30 offset:49920\n" \
    "v_exp_f32 v47, v119\n" \
    "v_add_f32 v37, v37, v44\n" \
    "ds_read_b64_tr_b16 a[110:111], v31 offset:49920\n" \
    "v_add_f32 v37, v37, v45\n" \
    "v_cvt_pk_bf16_f32 v58, v44, v45\n" \
    "buffer_load_dwordx4 v7, s[28:31], s75 offen offset:2048 lds\n" \
    "ds_read_b64_tr_b16 a[112:113], v30 offset:53248\n" \
    "v_exp_f32 v40, v120\n" \
    "v_exp_f32 v41, v121\n" \
    "ds_read_b64_tr_b16 a[114:115], v31 offset:53248\n" \
    "v_add_f32 v37, v37, v46\n" \
    "v_add_f32 v37, v37, v47\n" \
    "buffer_load_dwordx4 v8, s[28:31], s75 offen offset:3072 lds\n" \
    "ds_read_b64_tr_b16 a[116:117], v30 offset:53504\n" \
    "v_cvt_pk_bf16_f32 v59, v46, v47\n" \
    "v_exp_f32 v42, v122\n" \
    "ds_read_b64_tr_b16 a[118:119], v31 offset:53504\n" \
    "v_exp_f32 v43, v123\n" \
    "v_add_f32 v37, v37, v40\n" \
    "ds_read_b64_tr_b16 a[120:121], v30 offset:53760\n" \
    "v_add_f32 v37, v37, v41\n" \
    "v_cvt_pk_bf16_f32 v60, v40, v41\n" \
    "s_mov_b32 m0, 102400\n" \
    "s_mov_b32 s75, 1769472\n" \
    "buffer_load_dwordx4 v5, s[28:31], s75 offen offset:0 lds\n" \
    "ds_read_b64_tr_b16 a[122:123], v31 offset:53760\n" \
    "v_exp_f32 v44, v124\n" \
    "v_exp_f32 v45, v125\n" \
    "ds_read_b64_tr_b16 a[124:125], v30 offset:54016\n" \
    "v_add_f32 v37, v37, v42\n" \
    "v_add_f32 v37, v37, v43\n" \
    "buffer_load_dwordx4 v6, s[28:31], s75 offen offset:1024 lds\n" \
    "ds_read_b64_tr_b16 a[126:127], v31 offset:54016\n" \
    "v_cvt_pk_bf16_f32 v61, v42, v43\n" \
    "v_exp_f32 v46, v126\n" \
    "v_exp_f32 v47, v127\n" \
    "v_add_f32 v37, v37, v44\n" \
    "v_add_f32 v37, v37, v45\n" \
    "v_cvt_pk_bf16_f32 v62, v44, v45\n" \
    "buffer_load_dwordx4 v7, s[28:31], s75 offen offset:2048 lds\n" \
    "v_add_f32 v37, v37, v46\n" \
    "v_add_f32 v37, v37, v47\n" \
    "v_cvt_pk_bf16_f32 v63, v46, v47\n" \
    "buffer_load_dwordx4 v8, s[28:31], s75 offen offset:3072 lds\n" \
    "v_cmp_lt_f32 vcc, s69, v37\n" \
    "s_cbranch_vccnz Lmha_stub_si15b\n" \
    "Lmha_back_si15b:\n" \
    "s_waitcnt lgkmcnt(2)\n" \
    "v_mfma_f32_32x32x16_bf16 v[192:207], a[96:99], v[56:59], v[192:207]\n" \
    "v_add_f32 v39, v39, v37\n" \
    "s_mov_b32 m0, 106496\n" \
    "s_mov_b32 s75, 2949120\n" \
    "buffer_load_dwordx4 v5, s[28:31], s75 offen offset:0 lds\n" \
    "v_mfma_f32_32x32x16_bf16 v[208:223], a[100:103], v[56:59], v[208:223]\n" \
    "buffer_load_dwordx4 v6, s[28:31], s75 offen offset:1024 lds\n" \
    "v_mfma_f32_32x32x16_bf16 v[224:239], a[104:107], v[56:59], v[224:239]\n" \
    "buffer_load_dwordx4 v7, s[28:31], s75 offen offset:2048 lds\n" \
    "v_mfma_f32_32x32x16_bf16 v[240:255], a[108:111], v[56:59], v[240:255]\n" \
    "buffer_load_dwordx4 v8, s[28:31], s75 offen offset:3072 lds\n" \
    "v_mfma_f32_32x32x16_bf16 v[192:207], a[112:115], v[60:63], v[192:207]\n" \
    "s_mov_b32 m0, 110592\n" \
    "s_mov_b32 s75, 4128768\n" \
    "buffer_load_dwordx4 v5, s[28:31], s75 offen offset:0 lds\n" \
    "v_mfma_f32_32x32x16_bf16 v[208:223], a[116:119], v[60:63], v[208:223]\n" \
    "buffer_load_dwordx4 v6, s[28:31], s75 offen offset:1024 lds\n" \
    "v_mfma_f32_32x32x16_bf16 v[224:239], a[120:123], v[60:63], v[224:239]\n" \
    "buffer_load_dwordx4 v7, s[28:31], s75 offen offset:2048 lds\n" \
    "s_waitcnt lgkmcnt(0)\n" \
    "v_mfma_f32_32x32x16_bf16 v[240:255], a[124:127], v[60:63], v[240:255]\n" \
    "buffer_load_dwordx4 v8, s[28:31], s75 offen offset:3072 lds\n" \
    "v_sub_f32 v40, v80, v64\n" \
    "v_min_f32 v41, 0, v40\n" \
    "v_sub_f32 v40, 0, v40\n" \
    "v_min_f32 v40, 0, v40\n" \
    "v_exp_f32 v32, v41\n" \
    "v_exp_f32 v34, v40\n" \
    "s_lshl_b32 s55, s52, 19\n" \
    "s_add_u32 s56, s55, 4096\n" \
    "v_mul_f32 v38, v38, v32\n" \
    "v_fma_f32 v38, v39, v34, v38\n" \
    "v_mov_b32 v47, v38\n" \
    "s_nop 1\n" \
    "v_permlane32_swap_b32 v38, v47\n" \
    "v_add_f32 v38, v38, v47\n" \
    "v_rcp_f32 v46, v38\n" \
    "s_nop 0\n" \
    "v_mul_f32 v32, v32, v46\n" \
    "v_mul_f32 v34, v34, v46\n" \
    "v_mul_f32 v128, v32, v128\n" \
    "v_mul_f32 v129, v32, v129\n" \
    "v_mul_f32 v130, v32, v130\n" \
    "v_mul_f32 v131, v32, v131\n" \
    "v_mul_f32 v132, v32, v132\n" \
    "v_mul_f32 v133, v32, v133\n" \
    "v_mul_f32 v134, v32, v134\n" \
    "v_mul_f32 v135, v32, v135\n" \
    "v_fma_f32 v128, v192, v34, v128\n" \
    "v_fma_f32 v129, v193, v34, v129\n" \
    "v_fma_f32 v130, v194, v34, v130\n" \
    "v_fma_f32 v131, v195, v34, v131\n" \
    "v_fma_f32 v132, v196, v34, v132\n" \
    "v_fma_f32 v133, v197, v34, v133\n" \
    "v_fma_f32 v134, v198, v34, v134\n" \
    "v_fma_f32 v135, v199, v34, v135\n" \
    "v_cvt_pk_bf16_f32 v96, v128, v129\n" \
    "v_cvt_pk_bf16_f32 v97, v130, v131\n" \
    "v_cvt_pk_bf16_f32 v98, v132, v133\n" \
    "v_cvt_pk_bf16_f32 v99, v134, v135\n" \
    "buffer_store_dwordx4 v[96:99], v24, s[40:43], s55 offen offset:0 sc1\n" \
    "v_mul_f32 v136, v32, v136\n" \
    "v_mul_f32 v137, v32, v137\n" \
    "v_mul_f32 v138, v32, v138\n" \
    "v_mul_f32 v139, v32, v139\n" \
    "v_mul_f32 v140, v32, v140\n" \
    "v_mul_f32 v141, v32, v141\n" \
    "v_mul_f32 v142, v32, v142\n" \
    "v_mul_f32 v143, v32, v143\n" \
    "v_fma_f32 v136, v200, v34, v136\n" \
    "v_fma_f32 v137, v201, v34, v137\n" \
    "v_fma_f32 v138, v202, v34, v138\n" \
    "v_fma_f32 v139, v203, v34, v139\n" \
    "v_fma_f32 v140, v204, v34, v140\n" \
    "v_fma_f32 v141, v205, v34, v141\n" \
    "v_fma_f32 v142, v206, v34, v142\n" \
    "v_fma_f32 v143, v207, v34, v143\n" \
    "v_cvt_pk_bf16_f32 v100, v136, v137\n" \
    "v_cvt_pk_bf16_f32 v101, v138, v139\n" \
    "v_cvt_pk_bf16_f32 v102, v140, v141\n" \
    "v_cvt_pk_bf16_f32 v103, v142, v143\n" \
    "buffer_store_dwordx4 v[100:103], v24, s[40:43], s55 offen offset:1024 sc1\n" \
    "v_mul_f32 v144, v32, v144\n" \
    "v_mul_f32 v145, v32, v145\n" \
    "v_mul_f32 v146, v32, v146\n" \
    "v_mul_f32 v147, v32, v147\n" \
    "v_mul_f32 v148, v32, v148\n" \
    "v_mul_f32 v149, v32, v149\n" \
    "v_mul_f32 v150, v32, v150\n" \
    "v_mul_f32 v151, v32, v151\n" \
    "v_fma_f32 v144, v208, v34, v144\n" \
    "v_fma_f32 v145, v209, v34, v145\n" \
    "v_fma_f32 v146, v210, v34, v146\n" \
    "v_fma_f32 v147, v211, v34, v147\n" \
    "v_fma_f32 v148, v212, v34, v148\n" \
    "v_fma_f32 v149, v213, v34, v149\n" \
    "v_fma_f32 v150, v214, v34, v150\n" \
    "v_fma_f32 v151, v215, v34, v151\n" \
    "v_cvt_pk_bf16_f32 v104, v144, v145\n" \
    "v_cvt_pk_bf16_f32 v105, v146, v147\n" \
    "v_cvt_pk_bf16_f32 v106, v148, v149\n" \
    "v_cvt_pk_bf16_f32 v107, v150, v151\n" \
    "buffer_store_dwordx4 v[104:107], v24, s[40:43], s55 offen offset:2048 sc1\n" \
    "v_mul_f32 v152, v32, v152\n" \
    "v_mul_f32 v153, v32, v153\n" \
    "v_mul_f32 v154, v32, v154\n" \
    "v_mul_f32 v155, v32, v155\n" \
    "v_mul_f32 v156, v32, v156\n" \
    "v_mul_f32 v157, v32, v157\n" \
    "v_mul_f32 v158, v32, v158\n" \
    "v_mul_f32 v159, v32, v159\n" \
    "v_fma_f32 v152, v216, v34, v152\n" \
    "v_fma_f32 v153, v217, v34, v153\n" \
    "v_fma_f32 v154, v218, v34, v154\n" \
    "v_fma_f32 v155, v219, v34, v155\n" \
    "v_fma_f32 v156, v220, v34, v156\n" \
    "v_fma_f32 v157, v221, v34, v157\n" \
    "v_fma_f32 v158, v222, v34, v158\n" \
    "v_fma_f32 v159, v223, v34, v159\n" \
    "v_cvt_pk_bf16_f32 v108, v152, v153\n" \
    "v_cvt_pk_bf16_f32 v109, v154, v155\n" \
    "v_cvt_pk_bf16_f32 v110, v156, v157\n" \
    "v_cvt_pk_bf16_f32 v111, v158, v159\n" \
    "buffer_store_dwordx4 v[108:111], v24, s[40:43], s55 offen offset:3072 sc1\n" \
    "v_mul_f32 v160, v32, v160\n" \
    "v_mul_f32 v161, v32, v161\n" \
    "v_mul_f32 v162, v32, v162\n" \
    "v_mul_f32 v163, v32, v163\n" \
    "v_mul_f32 v164, v32, v164\n" \
    "v_mul_f32 v165, v32, v165\n" \
    "v_mul_f32 v166, v32, v166\n" \
    "v_mul_f32 v167, v32, v167\n" \
    "v_fma_f32 v160, v224, v34, v160\n" \
    "v_fma_f32 v161, v225, v34, v161\n" \
    "v_fma_f32 v162, v226, v34, v162\n" \
    "v_fma_f32 v163, v227, v34, v163\n" \
    "v_fma_f32 v164, v228, v34, v164\n" \
    "v_fma_f32 v165, v229, v34, v165\n" \
    "v_fma_f32 v166, v230, v34, v166\n" \
    "v_fma_f32 v167, v231, v34, v167\n" \
    "v_cvt_pk_bf16_f32 v112, v160, v161\n" \
    "v_cvt_pk_bf16_f32 v113, v162, v163\n" \
    "v_cvt_pk_bf16_f32 v114, v164, v165\n" \
    "v_cvt_pk_bf16_f32 v115, v166, v167\n" \
    "buffer_store_dwordx4 v[112:115], v24, s[40:43], s56 offen offset:0 sc1\n" \
    "v_mul_f32 v168, v32, v168\n" \
    "v_mul_f32 v169, v32, v169\n" \
    "v_mul_f32 v170, v32, v170\n" \
    "v_mul_f32 v171, v32, v171\n" \
    "v_mul_f32 v172, v32, v172\n" \
    "v_mul_f32 v173, v32, v173\n" \
    "v_mul_f32 v174, v32, v174\n" \
    "v_mul_f32 v175, v32, v175\n" \
    "v_fma_f32 v168, v232, v34, v168\n" \
    "v_fma_f32 v169, v233, v34, v169\n" \
    "v_fma_f32 v170, v234, v34, v170\n" \
    "v_fma_f32 v171, v235, v34, v171\n" \
    "v_fma_f32 v172, v236, v34, v172\n" \
    "v_fma_f32 v173, v237, v34, v173\n" \
    "v_fma_f32 v174, v238, v34, v174\n" \
    "v_fma_f32 v175, v239, v34, v175\n" \
    "v_cvt_pk_bf16_f32 v116, v168, v169\n" \
    "v_cvt_pk_bf16_f32 v117, v170, v171\n" \
    "v_cvt_pk_bf16_f32 v118, v172, v173\n" \
    "v_cvt_pk_bf16_f32 v119, v174, v175\n" \
    "buffer_store_dwordx4 v[116:119], v24, s[40:43], s56 offen offset:1024 sc1\n" \
    "v_mul_f32 v176, v32, v176\n" \
    "v_mul_f32 v177, v32, v177\n" \
    "v_mul_f32 v178, v32, v178\n" \
    "v_mul_f32 v179, v32, v179\n" \
    "v_mul_f32 v180, v32, v180\n" \
    "v_mul_f32 v181, v32, v181\n" \
    "v_mul_f32 v182, v32, v182\n" \
    "v_mul_f32 v183, v32, v183\n" \
    "v_fma_f32 v176, v240, v34, v176\n" \
    "v_fma_f32 v177, v241, v34, v177\n" \
    "v_fma_f32 v178, v242, v34, v178\n" \
    "v_fma_f32 v179, v243, v34, v179\n" \
    "v_fma_f32 v180, v244, v34, v180\n" \
    "v_fma_f32 v181, v245, v34, v181\n" \
    "v_fma_f32 v182, v246, v34, v182\n" \
    "v_fma_f32 v183, v247, v34, v183\n" \
    "v_cvt_pk_bf16_f32 v120, v176, v177\n" \
    "v_cvt_pk_bf16_f32 v121, v178, v179\n" \
    "v_cvt_pk_bf16_f32 v122, v180, v181\n" \
    "v_cvt_pk_bf16_f32 v123, v182, v183\n" \
    "buffer_store_dwordx4 v[120:123], v24, s[40:43], s56 offen offset:2048 sc1\n" \
    "v_mul_f32 v184, v32, v184\n" \
    "v_mul_f32 v185, v32, v185\n" \
    "v_mul_f32 v186, v32, v186\n" \
    "v_mul_f32 v187, v32, v187\n" \
    "v_mul_f32 v188, v32, v188\n" \
    "v_mul_f32 v189, v32, v189\n" \
    "v_mul_f32 v190, v32, v190\n" \
    "v_mul_f32 v191, v32, v191\n" \
    "v_fma_f32 v184, v248, v34, v184\n" \
    "v_fma_f32 v185, v249, v34, v185\n" \
    "v_fma_f32 v186, v250, v34, v186\n" \
    "v_fma_f32 v187, v251, v34, v187\n" \
    "v_fma_f32 v188, v252, v34, v188\n" \
    "v_fma_f32 v189, v253, v34, v189\n" \
    "v_fma_f32 v190, v254, v34, v190\n" \
    "v_fma_f32 v191, v255, v34, v191\n" \
    "v_cvt_pk_bf16_f32 v124, v184, v185\n" \
    "v_cvt_pk_bf16_f32 v125, v186, v187\n" \
    "v_cvt_pk_bf16_f32 v126, v188, v189\n" \
    "v_cvt_pk_bf16_f32 v127, v190, v191\n" \
    "buffer_store_dwordx4 v[124:127], v24, s[40:43], s56 offen offset:3072 sc1\n" \
    "s_mov_b32 s55, 16777216\n" \
    "s_add_u32 s36, s20, s55\n" \
    "s_addc_u32 s37, s21, 0\n" \
    "s_and_b32 s37, s37, 0xffff\n" \
    "s_mov_b32 s38, 131080\n" \
    "s_mov_b32 s39, 0x00020000\n" \
    "s_lshl_b32 s55, s53, 5\n" \
    "s_add_u32 s55, s55, s54\n" \
    "s_lshl2_add_u32 s55, s55, s52\n" \
    "s_lshl_b32 s55, s55, 7\n" \
    "v_mov_b32 v28, s55\n" \
    "v_mov_b32 v30, s22\n" \
    "v_mov_b32 v31, s23\n" \
    "ds_read_b64_tr_b16 a[0:1], v16 offset:0\n" \
    "ds_read_b64_tr_b16 a[2:3], v17 offset:0\n" \
    "ds_read_b64_tr_b16 a[4:5], v1 offset:0\n" \
    "ds_read_b64_tr_b16 a[6:7], v2 offset:0\n" \
    "ds_read_b64_tr_b16 a[8:9], v16 offset:2048\n" \
    "ds_read_b64_tr_b16 a[10:11], v17 offset:2048\n" \
    "ds_read_b64_tr_b16 a[12:13], v1 offset:2048\n" \
    "ds_read_b64_tr_b16 a[14:15], v2 offset:2048\n" \
    "ds_read_b64_tr_b16 a[16:17], v16 offset:16384\n" \
    "ds_read_b64_tr_b16 a[18:19], v17 offset:16384\n" \
    "ds_read_b64_tr_b16 a[20:21], v1 offset:16384\n" \
    "ds_read_b64_tr_b16 a[22:23], v2 offset:16384\n" \
    "ds_read_b64_tr_b16 a[24:25], v16 offset:18432\n" \
    "ds_read_b64_tr_b16 a[26:27], v17 offset:18432\n" \
    "ds_read_b64_tr_b16 a[28:29], v1 offset:18432\n" \
    "ds_read_b64_tr_b16 a[30:31], v2 offset:18432\n" \
    "ds_read_b64_tr_b16 a[32:33], v16 offset:32768\n" \
    "ds_read_b64_tr_b16 a[34:35], v17 offset:32768\n" \
    "ds_read_b64_tr_b16 a[36:37], v1 offset:32768\n" \
    "ds_read_b64_tr_b16 a[38:39], v2 offset:32768\n" \
    "ds_read_b64_tr_b16 a[40:41], v16 offset:34816\n" \
    "ds_read_b64_tr_b16 a[42:43], v17 offset:34816\n" \
    "ds_read_b64_tr_b16 a[44:45], v1 offset:34816\n" \
    "ds_read_b64_tr_b16 a[46:47], v2 offset:34816\n" \
    "ds_read_b64_tr_b16 a[48:49], v16 offset:49152\n" \
    "ds_read_b64_tr_b16 a[50:51], v17 offset:49152\n" \
    "ds_read_b64_tr_b16 a[52:53], v1 offset:49152\n" \
    "ds_read_b64_tr_b16 a[54:55], v2 offset:49152\n" \
    "ds_read_b64_tr_b16 a[56:57], v16 offset:51200\n" \
    "ds_read_b64_tr_b16 a[58:59], v17 offset:51200\n" \
    "ds_read_b64_tr_b16 a[60:61], v1 offset:51200\n" \
    "ds_read_b64_tr_b16 a[62:63], v2 offset:51200\n" \
    "ds_read_b64_tr_b16 a[64:65], v18 offset:0\n" \
    "ds_read_b64_tr_b16 a[66:67], v19 offset:0\n" \
    "ds_read_b64_tr_b16 a[68:69], v3 offset:0\n" \
    "ds_read_b64_tr_b16 a[70:71], v4 offset:0\n" \
    "ds_read_b64_tr_b16 a[72:73], v18 offset:2048\n" \
    "ds_read_b64_tr_b16 a[74:75], v19 offset:2048\n" \
    "ds_read_b64_tr_b16 a[76:77], v3 offset:2048\n" \
    "ds_read_b64_tr_b16 a[78:79], v4 offset:2048\n" \
    "ds_read_b64_tr_b16 a[80:81], v14 offset:0\n" \
    "ds_read_b64_tr_b16 a[82:83], v15 offset:0\n" \
    "ds_read_b64_tr_b16 a[84:85], v10 offset:0\n" \
    "ds_read_b64_tr_b16 a[86:87], v11 offset:0\n" \
    "ds_read_b64_tr_b16 a[88:89], v14 offset:2048\n" \
    "ds_read_b64_tr_b16 a[90:91], v15 offset:2048\n" \
    "ds_read_b64_tr_b16 a[92:93], v10 offset:2048\n" \
    "ds_read_b64_tr_b16 a[94:95], v11 offset:2048\n" \
    "s_waitcnt vmcnt(0)\n" \
    "s_mov_b64 exec, 1\n" \
    "buffer_store_dwordx2 v[30:31], v28, s[36:39], 0 offen offset:0 sc1\n" \
    "s_mov_b64 exec, -1\n" \
    "s_waitcnt lgkmcnt(0)\n" \
    "s_barrier\n" \
    "s_mov_b32 m0, 114688\n" \
    "s_mov_b32 s75, 626688\n" \
    "buffer_load_dwordx4 v5, s[28:31], s75 offen offset:0 lds\n" \
    "buffer_load_dwordx4 v6, s[28:31], s75 offen offset:1024 lds\n" \
    "buffer_load_dwordx4 v7, s[28:31], s75 offen offset:2048 lds\n" \
    "buffer_load_dwordx4 v8, s[28:31], s75 offen offset:3072 lds\n" \
    "s_mov_b32 m0, 118784\n" \
    "s_mov_b32 s75, 1806336\n" \
    "buffer_load_dwordx4 v5, s[28:31], s75 offen offset:0 lds\n" \
    "buffer_load_dwordx4 v6, s[28:31], s75 offen offset:1024 lds\n" \
    "buffer_load_dwordx4 v7, s[28:31], s75 offen offset:2048 lds\n" \
    "buffer_load_dwordx4 v8, s[28:31], s75 offen offset:3072 lds\n" \
    "s_mov_b32 m0, 122880\n" \
    "s_mov_b32 s75, 2985984\n" \
    "buffer_load_dwordx4 v5, s[28:31], s75 offen offset:0 lds\n" \
    "buffer_load_dwordx4 v6, s[28:31], s75 offen offset:1024 lds\n" \
    "buffer_load_dwordx4 v7, s[28:31], s75 offen offset:2048 lds\n" \
    "buffer_load_dwordx4 v8, s[28:31], s75 offen offset:3072 lds\n" \
    "s_mov_b32 m0, 126976\n" \
    "s_mov_b32 s75, 4165632\n" \
    "buffer_load_dwordx4 v5, s[28:31], s75 offen offset:0 lds\n" \
    "buffer_load_dwordx4 v6, s[28:31], s75 offen offset:1024 lds\n" \
    "buffer_load_dwordx4 v7, s[28:31], s75 offen offset:2048 lds\n" \
    "buffer_load_dwordx4 v8, s[28:31], s75 offen offset:3072 lds\n" \
    "s_mov_b32 m0, 0\n" \
    "s_mov_b32 s75, 663552\n" \
    "buffer_load_dwordx4 v5, s[28:31], s75 offen offset:0 lds\n" \
    "buffer_load_dwordx4 v6, s[28:31], s75 offen offset:1024 lds\n" \
    "buffer_load_dwordx4 v7, s[28:31], s75 offen offset:2048 lds\n" \
    "buffer_load_dwordx4 v8, s[28:31], s75 offen offset:3072 lds\n" \
    "s_mov_b32 m0, 4096\n" \
    "s_mov_b32 s75, 1843200\n" \
    "buffer_load_dwordx4 v5, s[28:31], s75 offen offset:0 lds\n" \
    "buffer_load_dwordx4 v6, s[28:31], s75 offen offset:1024 lds\n" \
    "buffer_load_dwordx4 v7, s[28:31], s75 offen offset:2048 lds\n" \
    "buffer_load_dwordx4 v8, s[28:31], s75 offen offset:3072 lds\n" \
    "s_mov_b32 m0, 8192\n" \
    "s_mov_b32 s75, 3022848\n" \
    "buffer_load_dwordx4 v5, s[28:31], s75 offen offset:0 lds\n" \
    "buffer_load_dwordx4 v6, s[28:31], s75 offen offset:1024 lds\n" \
    "buffer_load_dwordx4 v7, s[28:31], s75 offen offset:2048 lds\n" \
    "buffer_load_dwordx4 v8, s[28:31], s75 offen offset:3072 lds\n" \
    "s_mov_b32 m0, 12288\n" \
    "s_mov_b32 s75, 4202496\n" \
    "buffer_load_dwordx4 v5, s[28:31], s75 offen offset:0 lds\n" \
    "buffer_load_dwordx4 v6, s[28:31], s75 offen offset:1024 lds\n" \
    "buffer_load_dwordx4 v7, s[28:31], s75 offen offset:2048 lds\n" \
    "buffer_load_dwordx4 v8, s[28:31], s75 offen offset:3072 lds\n" \
    "s_mov_b32 m0, 16384\n" \
    "s_mov_b32 s75, 700416\n" \
    "buffer_load_dwordx4 v5, s[28:31], s75 offen offset:0 lds\n" \
    "buffer_load_dwordx4 v6, s[28:31], s75 offen offset:1024 lds\n" \
    "buffer_load_dwordx4 v7, s[28:31], s75 offen offset:2048 lds\n" \
    "buffer_load_dwordx4 v8, s[28:31], s75 offen offset:3072 lds\n" \
    "s_mov_b32 m0, 20480\n" \
    "s_mov_b32 s75, 1880064\n" \
    "buffer_load_dwordx4 v5, s[28:31], s75 offen offset:0 lds\n" \
    "buffer_load_dwordx4 v6, s[28:31], s75 offen offset:1024 lds\n" \
    "buffer_load_dwordx4 v7, s[28:31], s75 offen offset:2048 lds\n" \
    "buffer_load_dwordx4 v8, s[28:31], s75 offen offset:3072 lds\n" \
    "s_mov_b32 m0, 24576\n" \
    "s_mov_b32 s75, 3059712\n" \
    "buffer_load_dwordx4 v5, s[28:31], s75 offen offset:0 lds\n" \
    "buffer_load_dwordx4 v6, s[28:31], s75 offen offset:1024 lds\n" \
    "buffer_load_dwordx4 v7, s[28:31], s75 offen offset:2048 lds\n" \
    "buffer_load_dwordx4 v8, s[28:31], s75 offen offset:3072 lds\n" \
    "s_mov_b32 m0, 28672\n" \
    "s_mov_b32 s75, 4239360\n" \
    "buffer_load_dwordx4 v5, s[28:31], s75 offen offset:0 lds\n" \
    "buffer_load_dwordx4 v6, s[28:31], s75 offen offset:1024 lds\n" \
    "buffer_load_dwordx4 v7, s[28:31], s75 offen offset:2048 lds\n" \
    "buffer_load_dwordx4 v8, s[28:31], s75 offen offset:3072 lds\n" \
    "ds_read_b64_tr_b16 a[96:97], v14 offset:16384\n" \
    "ds_read_b64_tr_b16 a[98:99], v15 offset:16384\n" \
    "ds_read_b64_tr_b16 a[100:101], v10 offset:16384\n" \
    "ds_read_b64_tr_b16 a[102:103], v11 offset:16384\n" \
    "ds_read_b64_tr_b16 a[104:105], v14 offset:18432\n" \
    "ds_read_b64_tr_b16 a[106:107], v15 offset:18432\n" \
    "ds_read_b64_tr_b16 a[108:109], v10 offset:18432\n" \
    "ds_read_b64_tr_b16 a[110:111], v11 offset:18432\n" \
    "ds_read_b64_tr_b16 a[112:113], v18 offset:16384\n" \
    "ds_read_b64_tr_b16 a[114:115], v19 offset:16384\n" \
    "ds_read_b64_tr_b16 a[116:117], v3 offset:16384\n" \
    "ds_read_b64_tr_b16 a[118:119], v4 offset:16384\n" \
    "ds_read_b64_tr_b16 a[120:121], v18 offset:18432\n" \
    "ds_read_b64_tr_b16 a[122:123], v19 offset:18432\n" \
    "ds_read_b64_tr_b16 a[124:125], v3 offset:18432\n" \
    "ds_read_b64_tr_b16 a[126:127], v4 offset:18432\n" \
    "s_waitcnt lgkmcnt(0)\n" \
    "s_nop 15\n" \
    "s_branch Lmha_join\n" \
    "Lmha_idle:\n" \
    "s_cmp_lg_u32 %1, 249\n" \
    "s_cbranch_scc1 Lmha_end\n" \
    "s_lshl_b32 s55, s52, 15\n" \
    "v_lshlrev_b32 v28, 5, v24\n" \
    "v_add_u32 v28, s55, v28\n" \
    "s_mov_b32 s63, 400000\n" \
    "buffer_load_dwordx2 v[30:31], v28, s[36:39], 0 offen offset:0 sc1\n" \
    "s_waitcnt vmcnt(0)\n" \
    "v_cmp_ne_u64 vcc, s[22:23], v[30:31]\n" \
    "s_cbranch_vccz Lmha_okE\n" \
    "Lmha_spE:\n" \
    "s_sleep 1\n" \
    "buffer_load_dwordx2 v[30:31], v28, s[36:39], 0 offen offset:0 sc1\n" \
    "s_waitcnt vmcnt(0)\n" \
    "s_sub_u32 s63, s63, 1\n" \
    "s_cbranch_scc1 Lmha_okE\n" \
    "v_cmp_ne_u64 vcc, s[22:23], v[30:31]\n" \
    "s_cbranch_vccnz Lmha_spE\n" \
    "Lmha_okE:\n" \
    "s_barrier\n" \
    "s_cmp_lg_u32 s52, 0\n" \
    "s_cbranch_scc1 Lmha_end\n" \
    "v_mov_b32 v28, 131072\n" \
    "v_mov_b32 v30, s22\n" \
    "v_mov_b32 v31, s23\n" \
    "s_mov_b64 exec, 1\n" \
    "buffer_store_dwordx2 v[30:31], v28, s[36:39], 0 offen offset:0 sc1\n" \
    "s_branch Lmha_end\n" \
    "Lmha_stub_mf0:\n" \
    "s_getpc_b64 s[90:91]\n" \
    "Lmha_pc_mf0:\n" \
    "s_add_u32 s90, s90, Lmha_back_mf0-Lmha_pc_mf0\n" \
    "s_addc_u32 s91, s91, -1\n" \
    "s_branch Lmha_rare_F0\n" \
    "Lmha_stub_mPa:\n" \
    "s_getpc_b64 s[90:91]\n" \
    "Lmha_pc_mPa:\n" \
    "s_add_u32 s90, s90, Lmha_back_mPa-Lmha_pc_mPa\n" \
    "s_addc_u32 s91, s91, -1\n" \
    "s_branch Lmha_rare_F1\n" \
    "Lmha_stub_mPb:\n" \
    "s_getpc_b64 s[90:91]\n" \
    "Lmha_pc_mPb:\n" \
    "s_add_u32 s90, s90, Lmha_back_mPb-Lmha_pc_mPb\n" \
    "s_addc_u32 s91, s91, -1\n" \
    "s_branch Lmha_rare_F0\n" \
    "Lmha_stub_mAa:\n" \
    "s_getpc_b64 s[90:91]\n" \
    "Lmha_pc_mAa:\n" \
    "s_add_u32 s90, s90, Lmha_back_mAa-Lmha_pc_mAa\n" \
    "s_addc_u32 s91, s91, -1\n" \
    "s_branch Lmha_rare_F1\n" \
    "Lmha_stub_mAb:\n" \
    "s_getpc_b64 s[90:91]\n" \
    "Lmha_pc_mAb:\n" \
    "s_add_u32 s90, s90, Lmha_back_mAb-Lmha_pc_mAb\n" \
    "s_addc_u32 s91, s91, -1\n" \
    "s_branch Lmha_rare_F0\n" \
    "Lmha_stub_mh0:\n" \
    "s_getpc_b64 s[90:91]\n" \
    "Lmha_pc_mh0:\n" \
    "s_add_u32 s90, s90, Lmha_back_mh0-Lmha_pc_mh0\n" \
    "s_addc_u32 s91, s91, -1\n" \
    "s_branch Lmha_rare_F1\n" \
    "Lmha_stub_mh1:\n" \
    "s_getpc_b64 s[90:91]\n" \
    "Lmha_pc_mh1:\n" \
    "s_add_u32 s90, s90, Lmha_back_mh1-Lmha_pc_mh1\n" \
    "s_addc_u32 s91, s91, -1\n" \
    "s_branch Lmha_rare_F1\n" \
    "Lmha_stub_mh2:\n" \
    "s_getpc_b64 s[90:91]\n" \
    "Lmha_pc_mh2:\n" \
    "s_add_u32 s90, s90, Lmha_back_mh2-Lmha_pc_mh2\n" \
    "s_addc_u32 s91, s91, -1\n" \
    "s_branch Lmha_rare_F1\n" \
    "Lmha_stub_mh3:\n" \
    "s_getpc_b64 s[90:91]\n" \
    "Lmha_pc_mh3:\n" \
    "s_add_u32 s90, s90, Lmha_back_mh3-Lmha_pc_mh3\n" \
    "s_addc_u32 s91, s91, -1\n" \
    "s_branch Lmha_rare_F1\n" \
    "Lmha_stub_mh4:\n" \
    "s_getpc_b64 s[90:91]\n" \
    "Lmha_pc_mh4:\n" \
    "s_add_u32 s90, s90, Lmha_back_mh4-Lmha_pc_mh4\n" \
    "s_addc_u32 s91, s91, -1\n" \
    "s_branch Lmha_rare_F1\n" \
    "Lmha_stub_mh5:\n" \
    "s_getpc_b64 s[90:91]\n" \
    "Lmha_pc_mh5:\n" \
    "s_add_u32 s90, s90, Lmha_back_mh5-Lmha_pc_mh5\n" \
    "s_addc_u32 s91, s91, -1\n" \
    "s_branch Lmha_rare_F1\n" \
    "Lmha_stub_mh6:\n" \
    "s_getpc_b64 s[90:91]\n" \
    "Lmha_pc_mh6:\n" \
    "s_add_u32 s90, s90, Lmha_back_mh6-Lmha_pc_mh6\n" \
    "s_addc_u32 s91, s91, -1\n" \
    "s_branch Lmha_rare_F1\n" \
    "Lmha_stub_mh7:\n" \
    "s_getpc_b64 s[90:91]\n" \
    "Lmha_pc_mh7:\n" \
    "s_add_u32 s90, s90, Lmha_back_mh7-Lmha_pc_mh7\n" \
    "s_addc_u32 s91, s91, -1\n" \
    "s_branch Lmha_rare_F1\n" \
    "Lmha_stub_mCa:\n" \
    "s_getpc_b64 s[90:91]\n" \
    "Lmha_pc_mCa:\n" \
    "s_add_u32 s90, s90, Lmha_back_mCa-Lmha_pc_mCa\n" \
    "s_addc_u32 s91, s91, -1\n" \
    "s_branch Lmha_rare_F1\n" \
    "Lmha_stub_mCb:\n" \
    "s_getpc_b64 s[90:91]\n" \
    "Lmha_pc_mCb:\n" \
    "s_add_u32 s90, s90, Lmha_back_mCb-Lmha_pc_mCb\n" \
    "s_addc_u32 s91, s91, -1\n" \
    "s_branch Lmha_rare_F0\n" \
    "Lmha_stub_me1:\n" \
    "s_getpc_b64 s[90:91]\n" \
    "Lmha_pc_me1:\n" \
    "s_add_u32 s90, s90, Lmha_back_me1-Lmha_pc_me1\n" \
    "s_addc_u32 s91, s91, -1\n" \
    "s_branch Lmha_rare_F1\n" \
    "Lmha_stub_si0b:\n" \
    "s_getpc_b64 s[90:91]\n" \
    "Lmha_pc_si0b:\n" \
    "s_add_u32 s90, s90, Lmha_back_si0b-Lmha_pc_si0b\n" \
    "s_addc_u32 s91, s91, -1\n" \
    "s_branch Lmha_rare_F0\n" \
    "Lmha_stub_si1b:\n" \
    "s_getpc_b64 s[90:91]\n" \
    "Lmha_pc_si1b:\n" \
    "s_add_u32 s90, s90, Lmha_back_si1b-Lmha_pc_si1b\n" \
    "s_addc_u32 s91, s91, -1\n" \
    "s_branch Lmha_rare_F1\n" \
    "Lmha_stub_si2b:\n" \
    "s_getpc_b64 s[90:91]\n" \
    "Lmha_pc_si2b:\n" \
    "s_add_u32 s90, s90, Lmha_back_si2b-Lmha_pc_si2b\n" \
    "s_addc_u32 s91, s91, -1\n" \
    "s_branch Lmha_rare_F0\n" \
    "Lmha_stub_si3b:\n" \
    "s_getpc_b64 s[90:91]\n" \
    "Lmha_pc_si3b:\n" \
    "s_add_u32 s90, s90, Lmha_back_si3b-Lmha_pc_si3b\n" \
    "s_addc_u32 s91, s91, -1\n" \
    "s_branch Lmha_rare_F1\n" \
    "Lmha_stub_si4b:\n" \
    "s_getpc_b64 s[90:91]\n" \
    "Lmha_pc_si4b:\n" \
    "s_add_u32 s90, s90, Lmha_back_si4b-Lmha_pc_si4b\n" \
    "s_addc_u32 s91, s91, -1\n" \
    "s_branch Lmha_rare_F0\n" \
    "Lmha_stub_si5b:\n" \
    "s_getpc_b64 s[90:91]\n" \
    "Lmha_pc_si5b:\n" \
    "s_add_u32 s90, s90, Lmha_back_si5b-Lmha_pc_si5b\n" \
    "s_addc_u32 s91, s91, -1\n" \
    "s_branch Lmha_rare_F1\n" \
    "Lmha_stub_si6b:\n" \
    "s_getpc_b64 s[90:91]\n" \
    "Lmha_pc_si6b:\n" \
    "s_add_u32 s90, s90, Lmha_back_si6b-Lmha_pc_si6b\n" \
    "s_addc_u32 s91, s91, -1\n" \
    "s_branch Lmha_rare_F0\n" \
    "Lmha_stub_si7b:\n" \
    "s_getpc_b64 s[90:91]\n" \
    "Lmha_pc_si7b:\n" \
    "s_add_u32 s90, s90, Lmha_back_si7b-Lmha_pc_si7b\n" \
    "s_addc_u32 s91, s91, -1\n" \
    "s_branch Lmha_rare_F1\n" \
    "Lmha_stub_si8b:\n" \
    "s_getpc_b64 s[90:91]\n" \
    "Lmha_pc_si8b:\n" \
    "s_add_u32 s90, s90, Lmha_back_si8b-Lmha_pc_si8b\n" \
    "s_addc_u32 s91, s91, -1\n" \
    "s_branch Lmha_rare_F0\n" \
    "Lmha_stub_si9b:\n" \
    "s_getpc_b64 s[90:91]\n" \
    "Lmha_pc_si9b:\n" \
    "s_add_u32 s90, s90, Lmha_back_si9b-Lmha_pc_si9b\n" \
    "s_addc_u32 s91, s91, -1\n" \
    "s_branch Lmha_rare_F1\n" \
    "Lmha_stub_si10b:\n" \
    "s_getpc_b64 s[90:91]\n" \
    "Lmha_pc_si10b:\n" \
    "s_add_u32 s90, s90, Lmha_back_si10b-Lmha_pc_si10b\n" \
    "s_addc_u32 s91, s91, -1\n" \
    "s_branch Lmha_rare_F0\n" \
    "Lmha_stub_si11b:\n" \
    "s_getpc_b64 s[90:91]\n" \
    "Lmha_pc_si11b:\n" \
    "s_add_u32 s90, s90, Lmha_back_si11b-Lmha_pc_si11b\n" \
    "s_addc_u32 s91, s91, -1\n" \
    "s_branch Lmha_rare_F1\n" \
    "Lmha_stub_si12b:\n" \
    "s_getpc_b64 s[90:91]\n" \
    "Lmha_pc_si12b:\n" \
    "s_add_u32 s90, s90, Lmha_back_si12b-Lmha_pc_si12b\n" \
    "s_addc_u32 s91, s91, -1\n" \
    "s_branch Lmha_rare_F0\n" \
    "Lmha_stub_si13b:\n" \
    "s_getpc_b64 s[90:91]\n" \
    "Lmha_pc_si13b:\n" \
    "s_add_u32 s90, s90, Lmha_back_si13b-Lmha_pc_si13b\n" \
    "s_addc_u32 s91, s91, -1\n" \
    "s_branch Lmha_rare_F1\n" \
    "Lmha_stub_si14b:\n" \
    "s_getpc_b64 s[90:91]\n" \
    "Lmha_pc_si14b:\n" \
    "s_add_u32 s90, s90, Lmha_back_si14b-Lmha_pc_si14b\n" \
    "s_addc_u32 s91, s91, -1\n" \
    "s_branch Lmha_rare_F0\n" \
    "Lmha_stub_si15b:\n" \
    "s_getpc_b64 s[90:91]\n" \
    "Lmha_pc_si15b:\n" \
    "s_add_u32 s90, s90, Lmha_back_si15b-Lmha_pc_si15b\n" \
    "s_addc_u32 s91, s91, -1\n" \
    "s_branch Lmha_rare_F1\n" \
    "Lmha_hot_end:\n" \
    "Lmha_rare_F0:\n" \
    "s_nop 15\n" \
    "v_max3_f32 v32, v96, v97, v98\n" \
    "v_max3_f32 v9, v99, v100, v101\n" \
    "v_max3_f32 v32, v32, v102, v103\n" \
    "v_max3_f32 v9, v9, v104, v105\n" \
    "v_max3_f32 v32, v32, v106, v107\n" \
    "v_max3_f32 v9, v9, v108, v109\n" \
    "v_max3_f32 v32, v32, v110, v111\n" \
    "v_max_f32 v32, v32, v9\n" \
    "v_mov_b32 v9, v32\n" \
    "s_nop 1\n" \
    "v_permlane32_swap_b32 v32, v9\n" \
    "v_max_f32 v32, v32, v9\n" \
    "v_max_f32 v32, 0, v32\n" \
    "v_sub_f32 v33, 0, v32\n" \
    "v_exp_f32 v33, v33\n" \
    "v_sub_f32 v64, v64, v32\n" \
    "v_sub_f32 v65, v65, v32\n" \
    "v_sub_f32 v66, v66, v32\n" \
    "v_sub_f32 v67, v67, v32\n" \
    "v_sub_f32 v68, v68, v32\n" \
    "v_sub_f32 v69, v69, v32\n" \
    "v_sub_f32 v70, v70, v32\n" \
    "v_sub_f32 v71, v71, v32\n" \
    "v_sub_f32 v72, v72, v32\n" \
    "v_sub_f32 v73, v73, v32\n" \
    "v_sub_f32 v74, v74, v32\n" \
    "v_sub_f32 v75, v75, v32\n" \
    "v_sub_f32 v76, v76, v32\n" \
    "v_sub_f32 v77, v77, v32\n" \
    "v_sub_f32 v78, v78, v32\n" \
    "v_sub_f32 v79, v79, v32\n" \
    "v_mul_f32 v38, v38, v33\n" \
    "v_mul_f32 v128, v128, v33\n" \
    "v_mul_f32 v129, v129, v33\n" \
    "v_mul_f32 v130, v130, v33\n" \
    "v_mul_f32 v131, v131, v33\n" \
    "v_mul_f32 v132, v132, v33\n" \
    "v_mul_f32 v133, v133, v33\n" \
    "v_mul_f32 v134, v134, v33\n" \
    "v_mul_f32 v135, v135, v33\n" \
    "v_mul_f32 v136, v136, v33\n" \
    "v_mul_f32 v137, v137, v33\n" \
    "v_mul_f32 v138, v138, v33\n" \
    "v_mul_f32 v139, v139, v33\n" \
    "v_mul_f32 v140, v140, v33\n" \
    "v_mul_f32 v141, v141, v33\n" \
    "v_mul_f32 v142, v142, v33\n" \
    "v_mul_f32 v143, v143, v33\n" \
    "v_mul_f32 v144, v144, v33\n" \
    "v_mul_f32 v145, v145, v33\n" \
    "v_mul_f32 v146, v146, v33\n" \
    "v_mul_f32 v147, v147, v33\n" \
    "v_mul_f32 v148, v148, v33\n" \
    "v_mul_f32 v149, v149, v33\n" \
    "v_mul_f32 v150, v150, v33\n" \
    "v_mul_f32 v151, v151, v33\n" \
    "v_mul_f32 v152, v152, v33\n" \
    "v_mul_f32 v153, v153, v33\n" \
    "v_mul_f32 v154, v154, v33\n" \
    "v_mul_f32 v155, v155, v33\n" \
    "v_mul_f32 v156, v156, v33\n" \
    "v_mul_f32 v157, v157, v33\n" \
    "v_mul_f32 v158, v158, v33\n" \
    "v_mul_f32 v159, v159, v33\n" \
    "v_mul_f32 v160, v160, v33\n" \
    "v_mul_f32 v161, v161, v33\n" \
    "v_mul_f32 v162, v162, v33\n" \
    "v_mul_f32 v163, v163, v33\n" \
    "v_mul_f32 v164, v164, v33\n" \
    "v_mul_f32 v165, v165, v33\n" \
    "v_mul_f32 v166, v166, v33\n" \
    "v_mul_f32 v167, v167, v33\n" \
    "v_mul_f32 v168, v168, v33\n" \
    "v_mul_f32 v169, v169, v33\n" \
    "v_mul_f32 v170, v170, v33\n" \
    "v_mul_f32 v171, v171, v33\n" \
    "v_mul_f32 v172, v172, v33\n" \
    "v_mul_f32 v173, v173, v33\n" \
    "v_mul_f32 v174, v174, v33\n" \
    "v_mul_f32 v175, v175, v33\n" \
    "v_mul_f32 v176, v176, v33\n" \
    "v_mul_f32 v177, v177, v33\n" \
    "v_mul_f32 v178, v178, v33\n" \
    "v_mul_f32 v179, v179, v33\n" \
    "v_mul_f32 v180, v180, v33\n" \
    "v_mul_f32 v181, v181, v33\n" \
    "v_mul_f32 v182, v182, v33\n" \
    "v_mul_f32 v183, v183, v33\n" \
    "v_mul_f32 v184, v184, v33\n" \
    "v_mul_f32 v185, v185, v33\n" \
    "v_mul_f32 v186, v186, v33\n" \
    "v_mul_f32 v187, v187, v33\n" \
    "v_mul_f32 v188, v188, v33\n" \
    "v_mul_f32 v189, v189, v33\n" \
    "v_mul_f32 v190, v190, v33\n" \
    "v_mul_f32 v191, v191, v33\n" \
    "v_sub_f32 v96, v96, v32\n" \
    "v_sub_f32 v97, v97, v32\n" \
    "v_sub_f32 v98, v98, v32\n" \
    "v_sub_f32 v99, v99, v32\n" \
    "v_sub_f32 v100, v100, v32\n" \
    "v_sub_f32 v101, v101, v32\n" \
    "v_sub_f32 v102, v102, v32\n" \
    "v_sub_f32 v103, v103, v32\n" \
    "v_sub_f32 v104, v104, v32\n" \
    "v_sub_f32 v105, v105, v32\n" \
    "v_sub_f32 v106, v106, v32\n" \
    "v_sub_f32 v107, v107, v32\n" \
    "v_sub_f32 v108, v108, v32\n" \
    "v_sub_f32 v109, v109, v32\n" \
    "v_sub_f32 v110, v110, v32\n" \
    "v_sub_f32 v111, v111, v32\n" \
    "v_exp_f32 v34, v96\n" \
    "v_exp_f32 v35, v97\n" \
    "s_nop 0\n" \
    "v_add_f32 v36, v34, v35\n" \
    "v_cvt_pk_bf16_f32 v48, v34, v35\n" \
    "v_exp_f32 v34, v98\n" \
    "v_exp_f32 v35, v99\n" \
    "v_add_f32 v36, v36, v34\n" \
    "v_add_f32 v36, v36, v35\n" \
    "v_cvt_pk_bf16_f32 v49, v34, v35\n" \
    "v_exp_f32 v34, v100\n" \
    "v_exp_f32 v35, v101\n" \
    "v_add_f32 v36, v36, v34\n" \
    "v_add_f32 v36, v36, v35\n" \
    "v_cvt_pk_bf16_f32 v50, v34, v35\n" \
    "v_exp_f32 v34, v102\n" \
    "v_exp_f32 v35, v103\n" \
    "v_add_f32 v36, v36, v34\n" \
    "v_add_f32 v36, v36, v35\n" \
    "v_cvt_pk_bf16_f32 v51, v34, v35\n" \
    "v_exp_f32 v34, v104\n" \
    "v_exp_f32 v35, v105\n" \
    "v_add_f32 v36, v36, v34\n" \
    "v_add_f32 v36, v36, v35\n" \
    "v_cvt_pk_bf16_f32 v52, v34, v35\n" \
    "v_exp_f32 v34, v106\n" \
    "v_exp_f32 v35, v107\n" \
    "v_add_f32 v36, v36, v34\n" \
    "v_add_f32 v36, v36, v35\n" \
    "v_cvt_pk_bf16_f32 v53, v34, v35\n" \
    "v_exp_f32 v34, v108\n" \
    "v_exp_f32 v35, v109\n" \
    "v_add_f32 v36, v36, v34\n" \
    "v_add_f32 v36, v36, v35\n" \
    "v_cvt_pk_bf16_f32 v54, v34, v35\n" \
    "v_exp_f32 v34, v110\n" \
    "v_exp_f32 v35, v111\n" \
    "v_add_f32 v36, v36, v34\n" \
    "v_add_f32 v36, v36, v35\n" \
    "v_cvt_pk_bf16_f32 v55, v34, v35\n" \
    "s_nop 3\n" \
    "s_setpc_b64 s[90:91]\n" \
    "Lmha_rare_F1:\n" \
    "s_nop 15\n" \
    "v_max3_f32 v32, v112, v113, v114\n" \
    "v_max3_f32 v9, v115, v116, v117\n" \
    "v_max3_f32 v32, v32, v118, v119\n" \
    "v_max3_f32 v9, v9, v120, v121\n" \
    "v_max3_f32 v32, v32, v122, v123\n" \
    "v_max3_f32 v9, v9, v124, v125\n" \
    "v_max3_f32 v32, v32, v126, v127\n" \
    "v_max_f32 v32, v32, v9\n" \
    "v_mov_b32 v9, v32\n" \
    "s_nop 1\n" \
    "v_permlane32_swap_b32 v32, v9\n" \
    "v_max_f32 v32, v32, v9\n" \
    "v_max_f32 v32, 0, v32\n" \
    "v_sub_f32 v33, 0, v32\n" \
    "v_exp_f32 v33, v33\n" \
    "v_sub_f32 v80, v80, v32\n" \
    "v_sub_f32 v81, v81, v32\n" \
    "v_sub_f32 v82, v82, v32\n" \
    "v_sub_f32 v83, v83, v32\n" \
    "v_sub_f32 v84, v84, v32\n" \
    "v_sub_f32 v85, v85, v32\n" \
    "v_sub_f32 v86, v86, v32\n" \
    "v_sub_f32 v87, v87, v32\n" \
    "v_sub_f32 v88, v88, v32\n" \
    "v_sub_f32 v89, v89, v32\n" \
    "v_sub_f32 v90, v90, v32\n" \
    "v_sub_f32 v91, v91, v32\n" \
    "v_sub_f32 v92, v92, v32\n" \
    "v_sub_f32 v93, v93, v32\n" \
    "v_sub_f32 v94, v94, v32\n" \
    "v_sub_f32 v95, v95, v32\n" \
    "v_mul_f32 v39, v39, v33\n" \
    "v_mul_f32 v192, v192, v33\n" \
    "v_mul_f32 v193, v193, v33\n" \
    "v_mul_f32 v194, v194, v33\n" \
    "v_mul_f32 v195, v195, v33\n" \
    "v_mul_f32 v196, v196, v33\n" \
    "v_mul_f32 v197, v197, v33\n" \
    "v_mul_f32 v198, v198, v33\n" \
    "v_mul_f32 v199, v199, v33\n" \
    "v_mul_f32 v200, v200, v33\n" \
    "v_mul_f32 v201, v201, v33\n" \
    "v_mul_f32 v202, v202, v33\n" \
    "v_mul_f32 v203, v203, v33\n" \
    "v_mul_f32 v204, v204, v33\n" \
    "v_mul_f32 v205, v205, v33\n" \
    "v_mul_f32 v206, v206, v33\n" \
    "v_mul_f32 v207, v207, v33\n" \
    "v_mul_f32 v208, v208, v33\n" \
    "v_mul_f32 v209, v209, v33\n" \
    "v_mul_f32 v210, v210, v33\n" \
    "v_mul_f32 v211, v211, v33\n" \
    "v_mul_f32 v212, v212, v33\n" \
    "v_mul_f32 v213, v213, v33\n" \
    "v_mul_f32 v214, v214, v33\n" \
    "v_mul_f32 v215, v215, v33\n" \
    "v_mul_f32 v216, v216, v33\n" \
    "v_mul_f32 v217, v217, v33\n" \
    "v_mul_f32 v218, v218, v33\n" \
    "v_mul_f32 v219, v219, v33\n" \
    "v_mul_f32 v220, v220, v33\n" \
    "v_mul_f32 v221, v221, v33\n" \
    "v_mul_f32 v222, v222, v33\n" \
    "v_mul_f32 v223, v223, v33\n" \
    "v_mul_f32 v224, v224, v33\n" \
    "v_mul_f32 v225, v225, v33\n" \
    "v_mul_f32 v226, v226, v33\n" \
    "v_mul_f32 v227, v227, v33\n" \
    "v_mul_f32 v228, v228, v33\n" \
    "v_mul_f32 v229, v229, v33\n" \
    "v_mul_f32 v230, v230, v33\n" \
    "v_mul_f32 v231, v231, v33\n" \
    "v_mul_f32 v232, v232, v33\n" \
    "v_mul_f32 v233, v233, v33\n" \
    "v_mul_f32 v234, v234, v33\n" \
    "v_mul_f32 v235, v235, v33\n" \
    "v_mul_f32 v236, v236, v33\n" \
    "v_mul_f32 v237, v237, v33\n" \
    "v_mul_f32 v238, v238, v33\n" \
    "v_mul_f32 v239, v239, v33\n" \
    "v_mul_f32 v240, v240, v33\n" \
    "v_mul_f32 v241, v241, v33\n" \
    "v_mul_f32 v242, v242, v33\n" \
    "v_mul_f32 v243, v243, v33\n" \
    "v_mul_f32 v244, v244, v33\n" \
    "v_mul_f32 v245, v245, v33\n" \
    "v_mul_f32 v246, v246, v33\n" \
    "v_mul_f32 v247, v247, v33\n" \
    "v_mul_f32 v248, v248, v33\n" \
    "v_mul_f32 v249, v249, v33\n" \
    "v_mul_f32 v250, v250, v33\n" \
    "v_mul_f32 v251, v251, v33\n" \
    "v_mul_f32 v252, v252, v33\n" \
    "v_mul_f32 v253, v253, v33\n" \
    "v_mul_f32 v254, v254, v33\n" \
    "v_mul_f32 v255, v255, v33\n" \
    "v_sub_f32 v112, v112, v32\n" \
    "v_sub_f32 v113, v113, v32\n" \
    "v_sub_f32 v114, v114, v32\n" \
    "v_sub_f32 v115, v115, v32\n" \
    "v_sub_f32 v116, v116, v32\n" \
    "v_sub_f32 v117, v117, v32\n" \
    "v_sub_f32 v118, v118, v32\n" \
    "v_sub_f32 v119, v119, v32\n" \
    "v_sub_f32 v120, v120, v32\n" \
    "v_sub_f32 v121, v121, v32\n" \
    "v_sub_f32 v122, v122, v32\n" \
    "v_sub_f32 v123, v123, v32\n" \
    "v_sub_f32 v124, v124, v32\n" \
    "v_sub_f32 v125, v125, v32\n" \
    "v_sub_f32 v126, v126, v32\n" \
    "v_sub_f32 v127, v127, v32\n" \
    "v_exp_f32 v34, v112\n" \
    "v_exp_f32 v35, v113\n" \
    "s_nop 0\n" \
    "v_add_f32 v37, v34, v35\n" \
    "v_cvt_pk_bf16_f32 v56, v34, v35\n" \
    "v_exp_f32 v34, v114\n" \
    "v_exp_f32 v35, v115\n" \
    "v_add_f32 v37, v37, v34\n" \
    "v_add_f32 v37, v37, v35\n" \
    "v_cvt_pk_bf16_f32 v57, v34, v35\n" \
    "v_exp_f32 v34, v116\n" \
    "v_exp_f32 v35, v117\n" \
    "v_add_f32 v37, v37, v34\n" \
    "v_add_f32 v37, v37, v35\n" \
    "v_cvt_pk_bf16_f32 v58, v34, v35\n" \
    "v_exp_f32 v34, v118\n" \
    "v_exp_f32 v35, v119\n" \
    "v_add_f32 v37, v37, v34\n" \
    "v_add_f32 v37, v37, v35\n" \
    "v_cvt_pk_bf16_f32 v59, v34, v35\n" \
    "v_exp_f32 v34, v120\n" \
    "v_exp_f32 v35, v121\n" \
    "v_add_f32 v37, v37, v34\n" \
    "v_add_f32 v37, v37, v35\n" \
    "v_cvt_pk_bf16_f32 v60, v34, v35\n" \
    "v_exp_f32 v34, v122\n" \
    "v_exp_f32 v35, v123\n" \
    "v_add_f32 v37, v37, v34\n" \
    "v_add_f32 v37, v37, v35\n" \
    "v_cvt_pk_bf16_f32 v61, v34, v35\n" \
    "v_exp_f32 v34, v124\n" \
    "v_exp_f32 v35, v125\n" \
    "v_add_f32 v37, v37, v34\n" \
    "v_add_f32 v37, v37, v35\n" \
    "v_cvt_pk_bf16_f32 v62, v34, v35\n" \
    "v_exp_f32 v34, v126\n" \
    "v_exp_f32 v35, v127\n" \
    "v_add_f32 v37, v37, v34\n" \
    "v_add_f32 v37, v37, v35\n" \
    "v_cvt_pk_bf16_f32 v63, v34, v35\n" \
    "s_nop 3\n" \
    "s_setpc_b64 s[90:91]\n" \
    "Lmha_end:\n"
#define MHA_CLOBBERS "v1", "v2", "v3", "v4", "v5", "v6", "v7", "v8", "v9", "v10", "v11", "v12", "v13", "v14", "v15", "v16", "v17", "v18", "v19", "v20", "v21", "v22", "v23", "v24", "v25", "v26", "v27", "v28", "v29", "v30", "v31", "v32", "v33", "v34", "v35", "v36", "v37", "v38", "v39", "v40", "v41", "v42", "v43", "v44", "v45", "v46", "v47", "v48", "v49", "v50", "v51", "v52", "v53", "v54", "v55", "v56", "v57", "v58", "v59", "v60", "v61", "v62", "v63", "v64", "v65", "v66", "v67", "v68", "v69", "v70", "v71", "v72", "v73", "v74", "v75", "v76", "v77", "v78", "v79", "v80", "v81", "v82", "v83", "v84", "v85", "v86", "v87", "v88", "v89", "v90", "v91", "v92", "v93", "v94", "v95", "v96", "v97", "v98", "v99", "v100", "v101", "v102", "v103", "v104", "v105", "v106", "v107", "v108", "v109", "v110", "v111", "v112", "v113", "v114", "v115", "v116", "v117", "v118", "v119", "v120", "v121", "v122", "v123", "v124", "v125", "v126", "v127", "v128", "v129", "v130", "v131", "v132", "v133", "v134", "v135", "v136", "v137", "v138", "v139", "v140", "v141", "v142", "v143", "v144", "v145", "v146", "v147", "v148", "v149", "v150", "v151", "v152", "v153", "v154", "v155", "v156", "v157", "v158", "v159", "v160", "v161", "v162", "v163", "v164", "v165", "v166", "v167", "v168", "v169", "v170", "v171", "v172", "v173", "v174", "v175", "v176", "v177", "v178", "v179", "v180", "v181", "v182", "v183", "v184", "v185", "v186", "v187", "v188", "v189", "v190", "v191", "v192", "v193", "v194", "v195", "v196", "v197", "v198", "v199", "v200", "v201", "v202", "v203", "v204", "v205", "v206", "v207", "v208", "v209", "v210", "v211", "v212", "v213", "v214", "v215", "v216", "v217", "v218", "v219", "v220", "v221", "v222", "v223", "v224", "v225", "v226", "v227", "v228", "v229", "v230", "v231", "v232", "v233", "v234", "v235", "v236", "v237", "v238", "v239", "v240", "v241", "v242", "v243", "v244", "v245", "v246", "v247", "v248", "v249", "v250", "v251", "v252", "v253", "v254", "v255", "a0", "a1", "a2", "a3", "a4", "a5", "a6", "a7", "a8", "a9", "a10", "a11", "a12", "a13", "a14", "a15", "a16", "a17", "a18", "a19", "a20", "a21", "a22", "a23", "a24", "a25", "a26", "a27", "a28", "a29", "a30", "a31", "a32", "a33", "a34", "a35", "a36", "a37", "a38", "a39", "a40", "a41", "a42", "a43", "a44", "a45", "a46", "a47", "a48", "a49", "a50", "a51", "a52", "a53", "a54", "a55", "a56", "a57", "a58", "a59", "a60", "a61", "a62", "a63", "a64", "a65", "a66", "a67", "a68", "a69", "a70", "a71", "a72", "a73", "a74", "a75", "a76", "a77", "a78", "a79", "a80", "a81", "a82", "a83", "a84", "a85", "a86", "a87", "a88", "a89", "a90", "a91", "a92", "a93", "a94", "a95", "a96", "a97", "a98", "a99", "a100", "a101", "a102", "a103", "a104", "a105", "a106", "a107", "a108", "a109", "a110", "a111", "a112", "a113", "a114", "a115", "a116", "a117", "a118", "a119", "a120", "a121", "a122", "a123", "a124", "a125", "a126", "a127", "a128", "a129", "a130", "a131", "a132", "a133", "a134", "a135", "a136", "a137", "a138", "a139", "a140", "a141", "a142", "a143", "a144", "a145", "a146", "a147", "a148", "a149", "a150", "a151", "a152", "a153", "a154", "a155", "a156", "a157", "a158", "a159", "a160", "a161", "a162", "a163", "a164", "a165", "a166", "a167", "a168", "a169", "a170", "a171", "a172", "a173", "a174", "a175", "a176", "a177", "a178", "a179", "a180", "a181", "a182", "a183", "a184", "a185", "a186", "a187", "a188", "a189", "a190", "a191", "a192", "a193", "a194", "a195", "a196", "a197", "a198", "a199", "a200", "a201", "a202", "a203", "a204", "a205", "a206", "a207", "a208", "a209", "a210", "a211", "a212", "a213", "a214", "a215", "a216", "a217", "a218", "a219", "a220", "a221", "a222", "a223", "a224", "a225", "a226", "a227", "a228", "a229", "a230", "a231", "a232", "a233", "a234", "a235", "a236", "a237", "a238", "a239", "a240", "a241", "a242", "a243", "a244", "a245", "a246", "a247", "a248", "a249", "a250", "a251", "a252", "a253", "a254", "a255", "s8", "s9", "s10", "s11", "s12", "s13", "s14", "s15", "s16", "s17", "s18", "s19", "s20", "s21", "s22", "s23", "s24", "s25", "s26", "s27", "s28", "s29", "s30", "s31", "s34", "s35", "s36", "s37", "s38", "s39", "s40", "s41", "s42", "s43", "s44", "s45", "s46", "s47", "s48", "s49", "s50", "s51", "s52", "s53", "s54", "s55", "s56", "s57", "s58", "s59", "s60", "s61", "s62", "s63", "s64", "s65", "s66", "s67", "s68", "s69", "s70", "s71", "s72", "s73", "s74", "s75", "s76", "s77", "s78", "s79", "s80", "s81", "s82", "s83", "s84", "s85", "s86", "s87", "s88", "s89", "s90", "s91", "s92", "s93", "s94", "s95", "s96", "s97", "s98", "s99", "vcc", "scc", "memory"

__global__ __launch_bounds__(256, 1) void mha_fused(const u16* x, const u16* kc, const u16* vc, const u16* wq, const u16* wo,
                                                    u16* out, void* ws) {
  asm volatile(MHA_ASM
               :
               : "s"(__builtin_amdgcn_kernarg_segment_ptr()), "s"(blockIdx.x), "v"(threadIdx.x)
               : MHA_CLOBBERS);
}

extern "C" void kernel_launch(void* const* d_in, const int* in_sizes, int n_in, void* d_out, int out_size, void* d_ws,
                              size_t ws_size, hipStream_t stream) {
  (void)in_sizes; (void)n_in; (void)out_size; (void)ws_size;
  (void)hipFuncSetAttribute((const void*)mha_fused, hipFuncAttributeMaxDynamicSharedMemorySize, 163840);
  hipLaunchKernelGGL(mha_fused, dim3(256), dim3(256), 163840, stream, (const u16*)d_in[0], (const u16*)d_in[1],
                     (const u16*)d_in[2], (const u16*)d_in[3], (const u16*)d_in[4], (u16*)d_out, d_ws);
}
```
